```python
import torch

def _softmax(x, axis=-1):
    return torch.softmax(x, dim=axis)
def _gelu(x):
    return torch.nn.functional.gelu(x, approximate="tanh")
def _silu(x):
    return torch.nn.functional.silu(x)
def _sigmoid(x):
    return torch.sigmoid(x)
def _layernorm(x, axis=-1, eps=1e-5):
    m = x.mean(dim=axis, keepdim=True)
    v = x.var(dim=axis, keepdim=True, unbiased=False)
    return (x - m) / torch.sqrt(v + eps)
def _scatter_add(tgt, idx, src, axis=0):
    return tgt.clone().index_add_(axis, idx, src)
def _segment_sum(src, seg, n):
    out = torch.zeros((n,) + src.shape[1:], dtype=src.dtype)
    return out.index_add_(0, seg, src)
def _pad(x, pw):
    flat = tuple(v for pair in reversed(pw) for v in pair)
    return torch.nn.functional.pad(x, flat)

kernel_name = 'recurrent_scan__sliding_block_cached__glu_gated_reglu__decode'

def setup_inputs():

    _w = (lambda a: (lambda t: t.to(torch.bfloat16) if t.is_floating_point() else t)(torch.as_tensor(a)))
    def _A_setup():
        B, Sq, D = 4, 1, 448
        Hq, Hkv, Sk, Dh = 16, 8, 8192, 256
        assert Hq % Hkv == 0, (Hq, Hkv)
        return {
            "x": _w((lambda *s: torch.randn(*s, dtype=torch.bfloat16) * 0.05)(B, Sq, D)),
            "k_cache": _w((lambda *s: torch.randn(*s, dtype=torch.bfloat16) * 3.2)(B, Hkv, Sk, Dh)),
            "v_cache": _w((lambda *s: torch.randn(*s, dtype=torch.bfloat16) * 3.2)(B, Hkv, Sk, Dh)),
            "wq": _w((lambda *s: torch.randn(*s, dtype=torch.bfloat16) * 3.2)(D, Hq * Dh) * (D ** -0.5)),
            "wo": _w((lambda *s: torch.randn(*s, dtype=torch.bfloat16) * 3.2)(Hq * Dh, D) * ((Hq * Dh) ** -0.5)),
        }
    _A_in = _A_setup()

    def _B_setup():
        B, S, D, F = 4, 1, 448, 1792
        return {
            "x": _w((lambda *s: torch.randn(*s, dtype=torch.bfloat16) * 0.05)(B, S, D)),
            "W_gate": _w((lambda *s: torch.randn(*s, dtype=torch.bfloat16) * 0.4)(D, F) * (2.0 / D) ** 0.5),
            "W_up": _w((lambda *s: torch.randn(*s, dtype=torch.bfloat16) * 0.4)(D, F) * (2.0 / D) ** 0.5),
            "W_down": _w((lambda *s: torch.randn(*s, dtype=torch.bfloat16) * 0.4)(F, D) * (1.0 / F) ** 0.5),
        }
    _B_in = _B_setup()

    return {
        "A_x": _A_in["x"],
        "A_k_cache": _A_in["k_cache"],
        "A_v_cache": _A_in["v_cache"],
        "A_wq": _A_in["wq"],
        "A_wo": _A_in["wo"],
        "B_x": _B_in["x"],
        "B_W_gate": _B_in["W_gate"],
        "B_W_up": _B_in["W_up"],
        "B_W_down": _B_in["W_down"],
    }

def reference(A_x, A_k_cache, A_v_cache, A_wq, A_wo, B_x, B_W_gate, B_W_up, B_W_down):
    def _A_ref(x, k_cache, v_cache, wq, wo):
        B, Sq, D = x.shape
        Hq, Dh = 16, 256
        Hkv, Sk = k_cache.shape[1], k_cache.shape[2]
        W = min(4096, max(256, Sk // 4))
        rep = Hq // Hkv
        q = (lambda x, *d: x.permute(*d))((x @ wq).reshape(B, Sq, Hq, Dh), 0, 2, 1, 3)
        k = (lambda x, n, axis: torch.repeat_interleave(x, n, dim=axis))(k_cache[:, :, Sk - W:, :], rep, axis=1)
        v = (lambda x, n, axis: torch.repeat_interleave(x, n, dim=axis))(v_cache[:, :, Sk - W:, :], rep, axis=1)
        scores = torch.einsum("bhqd,bhkd->bhqk", q, k) * (Dh ** -0.5)
        p = _softmax(scores, axis=-1)
        o = torch.einsum("bhqk,bhkd->bhqd", p, v)
        return (lambda x, *d: x.permute(*d))(o, 0, 2, 1, 3).reshape(B, Sq, Hq * Dh) @ wo

    def _B_ref(x, W_gate, W_up, W_down):
        gate = torch.einsum("bsd,df->bsf", x, W_gate)
        up = torch.einsum("bsd,df->bsf", x, W_up)
        gate = gate * (lambda x, dt: x.to(dt))(gate > 0, gate.dtype)
        h = gate * up
        return torch.einsum("bsf,fd->bsd", h, W_down)

    x = A_x
    xn = _layernorm(x, axis=-1)
    s = _A_ref(xn, A_k_cache, A_v_cache, A_wq, A_wo)
    g = _silu(_B_ref(xn, B_W_gate, B_W_up, B_W_down))
    return x + s * g
```

```cpp
#include <hip/hip_runtime.h>
#include <hip/hip_bf16.h>
#include <stdint.h>

#define MIMIC 1

typedef unsigned short u16;
constexpr int B = 4, D = 448, HQ = 16, HKV = 8, SK = 8192, DH = 256, W = 2048, F = 1792;

__device__ __forceinline__ float bf2f(u16 v) { return __uint_as_float(((unsigned)v) << 16); }
__device__ __forceinline__ u16 f2bf(float f) {
  unsigned u = __float_as_uint(f);
  return (u16)((u + 0x7FFFu + ((u >> 16) & 1u)) >> 16);
}
__device__ __forceinline__ float rnd(float f) { return MIMIC ? bf2f(f2bf(f)) : f; }

__device__ void layernorm_all(const u16* x, float* xn, int tid) {
  int wave = tid >> 6, lane = tid & 63;
  if (wave < B) {
    float v[7], s = 0.f;
    for (int i = 0; i < 7; ++i) { v[i] = bf2f(x[wave * D + lane + 64 * i]); s += v[i]; }
    for (int o = 32; o; o >>= 1) s += __shfl_xor(s, o);
    float mean = s / D, q = 0.f;
    for (int i = 0; i < 7; ++i) { float d = v[i] - mean; q += d * d; }
    for (int o = 32; o; o >>= 1) q += __shfl_xor(q, o);
    float var = rnd(q / D), m = rnd(mean);
    float den = rnd(sqrtf(rnd(var + 1e-5f)));
    for (int i = 0; i < 7; ++i) xn[wave * D + lane + 64 * i] = rnd(rnd(v[i] - m) / den);
  }
}

__global__ void __launch_bounds__(256) k_proj(const u16* x, const u16* wq, const u16* wg, const u16* wu, float* q, float* h) {
  __shared__ float xn[B * D];
  layernorm_all(x, xn, threadIdx.x);
  __syncthreads();
  int col = blockIdx.x * 256 + threadIdx.x;
  if (col < HQ * DH) {
    float a[B] = {0, 0, 0, 0};
    for (int k = 0; k < D; ++k) { float w = bf2f(wq[(size_t)k * HQ * DH + col]);
      for (int b = 0; b < B; ++b) a[b] = fmaf(xn[b * D + k], w, a[b]); }
    for (int b = 0; b < B; ++b) q[b * HQ * DH + col] = rnd(a[b]);
  } else {
    int c = col - HQ * DH;
    float g[B] = {0, 0, 0, 0}, u[B] = {0, 0, 0, 0};
    for (int k = 0; k < D; ++k) { float w1 = bf2f(wg[(size_t)k * F + c]), w2 = bf2f(wu[(size_t)k * F + c]);
      for (int b = 0; b < B; ++b) { g[b] = fmaf(xn[b * D + k], w1, g[b]); u[b] = fmaf(xn[b * D + k], w2, u[b]); } }
    for (int b = 0; b < B; ++b) { float gg = rnd(g[b]); gg = gg > 0.f ? gg : 0.f; h[b * F + c] = rnd(gg * rnd(u[b])); }
  }
}

__global__ void __launch_bounds__(256) k_scores(const float* q, const u16* kc, float* sc) {
  int bh = blockIdx.x >> 3, j = blockIdx.x & 7, b = bh >> 3, hk = bh & 7;
  __shared__ float qs[2 * DH];
  for (int i = threadIdx.x; i < 2 * DH; i += 256) qs[i] = q[b * HQ * DH + hk * 2 * DH + i];
  __syncthreads();
  int key = j * 256 + threadIdx.x;
  const u16* kr = kc + ((size_t)bh * SK + (SK - W) + key) * DH;
  float a0 = 0.f, a1 = 0.f;
  for (int d = 0; d < DH; ++d) { float kv = bf2f(kr[d]); a0 = fmaf(qs[d], kv, a0); a1 = fmaf(qs[DH + d], kv, a1); }
  sc[(size_t)(bh * 2 + 0) * W + key] = rnd(a0) * 0.0625f;
  sc[(size_t)(bh * 2 + 1) * W + key] = rnd(a1) * 0.0625f;
}

__global__ void __launch_bounds__(256) k_pv(const float* sc, const u16* vc, float* o) {
  int row = blockIdx.x, bh = row >> 1;
  __shared__ float p[W];
  __shared__ float red[256];
  float m = -INFINITY;
  for (int i = threadIdx.x; i < W; i += 256) m = fmaxf(m, sc[(size_t)row * W + i]);
  red[threadIdx.x] = m; __syncthreads();
  for (int s = 128; s; s >>= 1) { if (threadIdx.x < s) red[threadIdx.x] = fmaxf(red[threadIdx.x], red[threadIdx.x + s]); __syncthreads(); }
  m = red[0]; __syncthreads();
  float l = 0.f;
  for (int i = threadIdx.x; i < W; i += 256) { float e = expf(sc[(size_t)row * W + i] - m); p[i] = e; l += e; }
  red[threadIdx.x] = l; __syncthreads();
  for (int s = 128; s; s >>= 1) { if (threadIdx.x < s) red[threadIdx.x] += red[threadIdx.x + s]; __syncthreads(); }
  l = red[0]; __syncthreads();
  for (int i = threadIdx.x; i < W; i += 256) p[i] = rnd(p[i] / l);
  __syncthreads();
  const u16* vb = vc + ((size_t)bh * SK + (SK - W)) * DH + threadIdx.x;
  float a = 0.f;
  for (int k = 0; k < W; ++k) a = fmaf(p[k], bf2f(vb[(size_t)k * DH]), a);
  o[row * DH + threadIdx.x] = rnd(a);
}

__global__ void __launch_bounds__(256) k_out(const u16* x, const float* o, const float* h, const u16* wo, const u16* wd, u16* out) {
  int b = blockIdx.y, c = blockIdx.x * 64 + (threadIdx.x & 63), part = threadIdx.x >> 6;
  __shared__ float rs[4][64], rd[4][64];
  float s = 0.f, dn = 0.f;
  for (int k = part; k < HQ * DH; k += 4) s = fmaf(o[b * HQ * DH + k], bf2f(wo[(size_t)k * D + c]), s);
  for (int k = part; k < F; k += 4) dn = fmaf(h[b * F + k], bf2f(wd[(size_t)k * D + c]), dn);
  rs[part][threadIdx.x & 63] = s; rd[part][threadIdx.x & 63] = dn;
  __syncthreads();
  if (part == 0) {
    int l = threadIdx.x;
    s = rnd(rs[0][l] + rs[1][l] + rs[2][l] + rs[3][l]);
    dn = rnd(rd[0][l] + rd[1][l] + rd[2][l] + rd[3][l]);
    float g = rnd(dn / (1.f + expf(-dn)));
    out[b * D + c] = f2bf(bf2f(x[b * D + c]) + rnd(s * g));
  }
}

extern "C" void kernel_launch(void* const* d_in, const int* in_sizes, int n_in, void* d_out, int out_size, void* d_ws, size_t ws_size, hipStream_t stream) {
  const u16* x = (const u16*)d_in[0]; const u16* kc = (const u16*)d_in[1]; const u16* vc = (const u16*)d_in[2];
  const u16* wq = (const u16*)d_in[3]; const u16* wo = (const u16*)d_in[4];
  const u16* wg = (const u16*)d_in[6]; const u16* wu = (const u16*)d_in[7]; const u16* wd = (const u16*)d_in[8];
  float* ws = (float*)d_ws;
  float* q = ws; float* h = q + B * HQ * DH; float* sc = h + B * F; float* o = sc + B * HQ * W;
  k_proj<<<(HQ * DH + F) / 256, 256, 0, stream>>>(x, wq, wg, wu, q, h);
  k_scores<<<256, 256, 0, stream>>>(q, kc, sc);
  k_pv<<<B * HQ, 256, 0, stream>>>(sc, vc, o);
  k_out<<<dim3(7, B), 256, 0, stream>>>(x, o, h, wo, wd, (u16*)d_out);
}
```

```cpp
#include <hip/hip_runtime.h>
#include <hip/hip_bf16.h>
#include <stdint.h>

#define DIAG 0
#define KV_AUX 2
#define W_AUX 2
#define WS_OFF (500u  __H0__ )

namespace {

typedef unsigned short u16;
typedef unsigned long long u64;
typedef unsigned u32x2 __attribute__((ext_vector_type(2)));
typedef unsigned u32x4 __attribute__((ext_vector_type(4)));
typedef float f32x2 __attribute__((ext_vector_type(2)));
typedef float f32x4 __attribute__((ext_vector_type(4)));
typedef short s16x4 __attribute__((ext_vector_type(4)));
typedef __bf16 bf16x2 __attribute__((ext_vector_type(2)));
typedef __bf16 bf16x8 __attribute__((ext_vector_type(8)));
#define LAS __attribute__((address_space(3)))
typedef __amdgpu_buffer_rsrc_t rsrc_t;

constexpr int NB = 4, DM = 448, SK = 8192, WIN = 2048, FF = 1792, QW = 4096;
constexpr int AUX_SC1 = 16;
constexpr unsigned SPIN_MAX = 1u << 18;
constexpr float LOG2E = 1.4426950408889634f;

constexpr unsigned WS_EPOCH = 0;
constexpr unsigned WS_QP = 4096;
constexpr unsigned WS_GP = WS_QP + 8 * 4 * 8192;
constexpr unsigned WS_OP = WS_GP + 56 * 4096;
constexpr unsigned WS_ML = WS_OP + 128 * 4096;
constexpr unsigned WS_RP = WS_ML + 16 * 32 * 16;
constexpr unsigned WS_END = WS_RP + 56 * 184 * 128;
constexpr unsigned WS_DIAG = 8u << 20;

constexpr int L_TILE = 0;
constexpr int L_PV = L_TILE + 7 * 4096;
constexpr int L_EP = L_PV + 8 * 2048;
constexpr int L_X = L_EP + 7 * 1024;
constexpr int L_Q = L_X + 4 * 896;
constexpr int L_P = L_Q + 1024;
constexpr int L_FIN = L_P + 1024;
constexpr int L_A = L_FIN + 1024;
constexpr int L_ML = L_A + 256;
constexpr int L_ST = L_ML + 128;
constexpr int L_FLAG = L_ST + 32;
constexpr int L_TAG = L_FLAG + 16;
constexpr int L_END = L_TAG + 16;

#define SCHED_FENCE() __builtin_amdgcn_sched_barrier(0)
#define WG_BARRIER() do { SCHED_FENCE(); asm volatile("s_waitcnt lgkmcnt(0)\n\ts_barrier" ::: "memory"); SCHED_FENCE(); } while (0)

__device__ __forceinline__ float asf(unsigned u) { return __builtin_bit_cast(float, u); }
__device__ __forceinline__ unsigned asu(float f) { return __builtin_bit_cast(unsigned, f); }
__device__ __forceinline__ unsigned pack_bf16(float lo, float hi) {
  f32x2 f = {lo, hi};
  return __builtin_bit_cast(unsigned, __builtin_convertvector(f, bf16x2));
}
__device__ __forceinline__ float round_bf16(float v) { return asf(pack_bf16(v, v) & 0xffff0000u); }
__device__ __forceinline__ float bf_lo(unsigned u) { return asf(u << 16); }
__device__ __forceinline__ float bf_hi(unsigned u) { return asf(u & 0xffff0000u); }
__device__ __forceinline__ float dot2(unsigned a, unsigned b, float c) {
  return __builtin_amdgcn_fdot2_f32_bf16(__builtin_bit_cast(bf16x2, a), __builtin_bit_cast(bf16x2, b), c, false);
}
template <int CTRL> __device__ __forceinline__ float dpp(float x) {
  return asf((unsigned)__builtin_amdgcn_mov_dpp((int)asu(x), CTRL, 0xf, 0xf, true));
}
constexpr int XOR1 = 0xB1, XOR2 = 0x4E, XOR7 = 0x141, XOR8 = 0x128;
__device__ __forceinline__ float fold16(float a, float b) {
  auto s = __builtin_amdgcn_permlane16_swap(asu(a), asu(b), false, false);
  return asf(s[0]) + asf(s[1]);
}
__device__ __forceinline__ float fold32(float a, float b) {
  auto s = __builtin_amdgcn_permlane32_swap(asu(a), asu(b), false, false);
  return asf(s[0]) + asf(s[1]);
}
__device__ __forceinline__ float max16(float a) {
  auto s = __builtin_amdgcn_permlane16_swap(asu(a), asu(a), false, false);
  return fmaxf(asf(s[0]), asf(s[1]));
}
__device__ __forceinline__ float max32(float a) {
  auto s = __builtin_amdgcn_permlane32_swap(asu(a), asu(a), false, false);
  return fmaxf(asf(s[0]), asf(s[1]));
}
template <int CTRL> __device__ __forceinline__ float foldrow(float lo, float hi, bool bit) {
  const float keep = bit ? hi : lo, send = bit ? lo : hi;
  return keep + dpp<CTRL>(send);
}
__device__ __forceinline__ float row_sum(float v) {
  v += dpp<XOR8>(v); v += dpp<XOR7>(v); v += dpp<XOR2>(v); v += dpp<XOR1>(v);
  return v;
}
__device__ __forceinline__ float row_max(float v) {
  v = fmaxf(v, dpp<XOR8>(v)); v = fmaxf(v, dpp<XOR7>(v)); v = fmaxf(v, dpp<XOR2>(v)); v = fmaxf(v, dpp<XOR1>(v));
  return v;
}
__device__ __forceinline__ rsrc_t make_rsrc(const void* p, unsigned bytes) {
  return __builtin_amdgcn_make_buffer_rsrc(const_cast<void*>(p), 0, bytes, 0x00020000);
}
__device__ __forceinline__ u32x4 tag4(f32x4 v, unsigned tag) {
  u32x4 r;
  r[0] = __builtin_amdgcn_perm(asu(v[0]), tag, 0x07060500u);
  r[1] = __builtin_amdgcn_perm(asu(v[1]), tag, 0x07060501u);
  r[2] = __builtin_amdgcn_perm(asu(v[2]), tag, 0x07060502u);
  r[3] = __builtin_amdgcn_perm(asu(v[3]), tag, 0x07060503u);
  return r;
}
__device__ __forceinline__ bool tag4_ok(u32x4 v, unsigned tag) {
  return (__builtin_amdgcn_perm(v[1], v[0], 0x0c0c0400u) | __builtin_amdgcn_perm(v[3], v[2], 0x04000c0cu)) == tag;
}
__device__ __forceinline__ f32x4 untag4(u32x4 v) {
  return f32x4{asf(v[0] & 0xffffff00u), asf(v[1] & 0xffffff00u), asf(v[2] & 0xffffff00u), asf(v[3] & 0xffffff00u)};
}
__device__ __forceinline__ u32x4 ld_sc1(rsrc_t r, unsigned voff, unsigned soff) {
  return __builtin_amdgcn_raw_buffer_load_b128(r, voff, soff, AUX_SC1);
}
__device__ __forceinline__ void st_sc1(u32x4 v, rsrc_t r, unsigned voff, unsigned soff) {
  __builtin_amdgcn_raw_buffer_store_b128(v, r, voff, soff, AUX_SC1);
}
__device__ __forceinline__ bool wave_all(bool ok) { return __builtin_amdgcn_ballot_w64(!ok) == 0ull; }
__device__ __forceinline__ void poll_pause() { asm volatile("s_sleep 1" ::: "memory"); }

template <typename T> __device__ __forceinline__ T lds_ld(LAS char* l, int off) { return *(LAS T*)(l + off); }
template <typename T> __device__ __forceinline__ void lds_st(LAS char* l, int off, T v) { *(LAS T*)(l + off) = v; }
__device__ __forceinline__ void lds_wait_eq(LAS char* l, int off, unsigned want) {
  _Pragma("nounroll") for (unsigned s = 0; s < SPIN_MAX; ++s) {
    const unsigned v = __builtin_amdgcn_readfirstlane(*(volatile LAS unsigned*)(l + off));
    if (v == want) break;
    poll_pause();
  }
  asm volatile("" ::: "memory");
}
__device__ __forceinline__ unsigned lds_arrive(LAS char* l, int off, int lane) {
  asm volatile("s_waitcnt lgkmcnt(0)" ::: "memory");
  unsigned old = 0;
  if (lane == 0) old = __hip_atomic_fetch_add((LAS unsigned*)(l + off), 1u, __ATOMIC_RELAXED, __HIP_MEMORY_SCOPE_WORKGROUP);
  old = __builtin_amdgcn_readfirstlane(old);
  asm volatile("" ::: "memory");
  return old;
}

__device__ __forceinline__ void tile_fetch(rsrc_t r, unsigned stride, unsigned soff, LAS char* img, int lane) {
  const int r8 = lane >> 3, c = lane & 7;
  const unsigned voff = r8 * stride + ((c ^ (r8 & 6)) << 4);
#pragma unroll
  for (int i = 0; i < 4; ++i)
    __builtin_amdgcn_raw_ptr_buffer_load_lds(r, (LAS void*)(img + i * 1024), 16, voff, soff + i * 8 * stride, 0, W_AUX);
}
__device__ __forceinline__ void tile_frags(LAS char* img, int lane, bf16x8 (&w)[4]) {
  const int g = lane >> 4, q = (lane >> 2) & 3, p = lane & 3, row = 4 * g + q;
#pragma unroll
  for (int t = 0; t < 4; ++t) {
    LAS char* a = img + row * 128 + (((2 * t + (p >> 1)) ^ (row & 6)) << 4) + 8 * (p & 1);
    const s16x4 lo = __builtin_amdgcn_ds_read_tr16_b64_v4i16((LAS s16x4*)a);
    const s16x4 hi = __builtin_amdgcn_ds_read_tr16_b64_v4i16((LAS s16x4*)(a + 2048));
    w[t] = __builtin_bit_cast(bf16x8, __builtin_shufflevector(lo, hi, 0, 1, 2, 3, 4, 5, 6, 7));
  }
}
template <bool ACT_IS_A> __device__ __forceinline__ f32x4 tile_mma(u32x4 act, const bf16x8 (&w)[4], int lane) {
  const int jj = (lane >> 2) & 3;
  f32x4 acc = {0.f, 0.f, 0.f, 0.f};
#pragma unroll
  for (int t = 0; t < 4; ++t) {
    const u32x4 z = {0u, 0u, 0u, 0u};
    const bf16x8 a = __builtin_bit_cast(bf16x8, jj == t ? act : z);
    acc = ACT_IS_A ? __builtin_amdgcn_mfma_f32_16x16x32_bf16(a, w[t], acc, 0, 0, 0)
                   : __builtin_amdgcn_mfma_f32_16x16x32_bf16(w[t], a, acc, 0, 0, 0);
  }
  return acc;
}
__device__ __forceinline__ float fold_hi(f32x4 v) {
  const float z = fold16(fold32(v[0], v[2]), fold32(v[1], v[3]));
  return z + dpp<XOR8>(z);
}

#define STAMP(k)
#define STAMP_OUT()

__global__ void __launch_bounds__(1024, 4)
fused_decode(const u16* x, const u16* kc, const u16* vc, const u16* wq,
             const u16* wo, const u16* wg, const u16* wu, const u16* wd,
             u16* out, char* ws) {
  __shared__ __attribute__((aligned(16))) char lds_raw[L_END];
  LAS char* lds = (LAS char*)lds_raw;
  STAMP(0);
  asm volatile("" : "+s"(x), "+s"(kc), "+s"(vc), "+s"(wq), "+s"(wo), "+s"(wg), "+s"(wu), "+s"(wd), "+s"(out), "+s"(ws));
  STAMP(11);
  const int bid = blockIdx.x, lane = threadIdx.x & 63, wave = __builtin_amdgcn_readfirstlane(threadIdx.x >> 6);
  const int hk = bid & 7, idx = bid >> 3, j = idx & 7, b = idx >> 3;
  const int w8 = wave & 7;
  const rsrc_t rws = make_rsrc(ws, WS_END);
  const size_t kv_base = ((size_t)(b * 8 + hk) * SK + (SK - WIN) + j * 256) * 256;
  const unsigned rot = (unsigned)(((b * 8 + hk) * 8 + j) * 37);

  const bool is_wo = idx >= 16, is_down = idx >= 9 && idx < 16, is_red = idx < 7;

  if (wave >= 8) {
    __builtin_amdgcn_s_setprio(2);
    const bool has_early = idx < 30;
    const bool is_q = idx < 16, is_gate = idx >= 16 && idx < 23;
    const int gu = ((is_gate ? idx - 16 : idx - 23) << 3) + hk;
    const int half = is_q ? (idx >> 3) : (gu & 1);
    const int etile = is_q ? (hk * 8 + (idx & 7)) : (gu >> 1);
    u32x4 xr[4];
    unsigned epoch, tag;
    if (w8 == 7) {
      epoch = __builtin_amdgcn_raw_buffer_load_b32(rws, 0, WS_EPOCH, AUX_SC1);
      const rsrc_t rx = make_rsrc(x, NB * DM * 2);
#pragma unroll
      for (int i = 0; i < 4; ++i) xr[i] = __builtin_amdgcn_raw_buffer_load_b128(rx, lane < 56 ? lane * 16 : 0x7fffffff, i * 896, 0);
      if (lane < 4) lds_st<unsigned>(lds, L_FLAG + lane * 4, 0u);
    } else if (has_early) {
      const unsigned stride = is_q ? QW * 2 : FF * 2;
      const rsrc_t rw = make_rsrc(is_q ? wq : (is_gate ? wg : wu), DM * stride);
      tile_fetch(rw, stride, (half * 224 + w8 * 32) * stride + etile * 128, lds + L_TILE + w8 * 4096, lane);
    }
    WG_BARRIER();
    STAMP(1);
    if (w8 == 7) {
      if (lane == 0) lds_st<unsigned>(lds, L_TAG, ((epoch & 0x7f7f7f7fu) + 0x01010101u) ^ (epoch & 0x80808080u));
      float s[4], v[4][8];
#pragma unroll
      for (int i = 0; i < 4; ++i) {
        if (lane < 56) lds_st<u32x4>(lds, L_X + i * 896 + lane * 16, xr[i]);
        float t = 0.f;
#pragma unroll
        for (int k = 0; k < 4; ++k) { v[i][2 * k] = bf_lo(xr[i][k]); v[i][2 * k + 1] = bf_hi(xr[i][k]); t += v[i][2 * k] + v[i][2 * k + 1]; }
        s[i] = t;
      }
      const float mean_l = row_sum(fold16(fold32(s[0], s[2]), fold32(s[1], s[3]))) * (1.0f / DM);
#pragma unroll
      for (int i = 0; i < 4; ++i) {
        const float mean = asf(__builtin_amdgcn_readlane(asu(mean_l), i * 16));
        float t = 0.f;
#pragma unroll
        for (int k = 0; k < 8; ++k) { const float d = v[i][k] - mean; t = fmaf(d, d, t); }
        s[i] = lane < 56 ? t : 0.f;
      }
      const float var = round_bf16(row_sum(fold16(fold32(s[0], s[2]), fold32(s[1], s[3]))) * (1.0f / DM));
      const float den = round_bf16(sqrtf(round_bf16(var + 1e-5f)));
      if ((lane & 15) == 0) lds_st<f32x2>(lds, L_ST + (lane >> 4) * 8, f32x2{round_bf16(mean_l), den});
      asm volatile("s_waitcnt lgkmcnt(0)" ::: "memory");
      if (lane == 0) *(volatile LAS unsigned*)(lds + L_FLAG) = 1u;
    } else if (has_early) {
      lds_wait_eq(lds, L_FLAG, 1u);
      const int bt = lane & 3, g = lane >> 4;
      const f32x2 stt = lds_ld<f32x2>(lds, L_ST + bt * 8);
      u32x4 act;
#pragma unroll
      for (int h = 0; h < 2; ++h) {
        const u32x2 xx = lds_ld<u32x2>(lds, L_X + bt * 896 + (half * 224 + w8 * 32 + 16 * h + 4 * g) * 2);
#pragma unroll
        for (int k = 0; k < 2; ++k) {
          const unsigned d = pack_bf16(bf_lo(xx[k]) - stt[0], bf_hi(xx[k]) - stt[0]);
          act[2 * h + k] = pack_bf16(bf_lo(d) / stt[1], bf_hi(d) / stt[1]);
        }
      }
      asm volatile("s_waitcnt vmcnt(0)" ::: "memory");
      bf16x8 wf[4];
      tile_frags(lds + L_TILE + w8 * 4096, lane, wf);
      const f32x4 acc = tile_mma<false>(act, wf, lane);
      lds_st<f32x4>(lds, L_EP + w8 * 1024 + bt * 256 + ((lane & 12) + g) * 16, acc);
      if (lds_arrive(lds, L_FLAG + 4, lane) == 6u) {
        f32x4 sum = lds_ld<f32x4>(lds, L_EP + lane * 16);
#pragma unroll
        for (int p = 1; p < 7; ++p) sum += lds_ld<f32x4>(lds, L_EP + p * 1024 + lane * 16);
        tag = lds_ld<unsigned>(lds, L_TAG);
        const int pb = lane >> 4, cq = lane & 15;
        const unsigned voff = is_q ? pb * 8192 + cq * 16 : (cq >> 3) * 4096 + pb * 256 + (cq & 7) * 16;
        const unsigned soff = __builtin_amdgcn_readfirstlane(is_q ? WS_QP + hk * 32768 + half * 4096 + (idx & 7) * 512
                                                                  : WS_GP + etile * 8192 + (is_gate ? 0 : 2048) + half * 1024);
        const unsigned step = __builtin_amdgcn_readfirstlane(is_q ? 256 : 128);
        st_sc1(u32x4{asu(sum[0]), tag, asu(sum[1]), tag}, rws, voff, soff);
        st_sc1(u32x4{asu(sum[2]), tag, asu(sum[3]), tag}, rws, voff, soff + step);
      }
    }
    STAMP(2);
    __builtin_amdgcn_s_setprio(0);
    WG_BARRIER();
    STAMP(3);
    const rsrc_t rv = make_rsrc(vc + kv_base, 131072);
    u32x4 vr[16];
#pragma unroll
    for (int i = 0; i < 16; ++i) vr[i] = __builtin_amdgcn_raw_buffer_load_b128(rv, lane * 16, ((w8 * 16 + i + rot) & 127) << 10, KV_AUX);
    STAMP(4);
    WG_BARRIER();
    STAMP(5);
    tag = __builtin_amdgcn_readfirstlane(lds_ld<unsigned>(lds, L_TAG));
    u32x4 pp[2][2];
#pragma unroll
    for (int h = 0; h < 2; ++h)
#pragma unroll
      for (int u = 0; u < 2; ++u) pp[h][u] = lds_ld<u32x4>(lds, L_P + w8 * 128 + (lane >> 5) * 64 + h * 32 + u * 16);
    float acc[2][8];
#pragma unroll
    for (int k = 0; k < 8; ++k) { acc[0][k] = 0.f; acc[1][k] = 0.f; }
#pragma unroll
    for (int a = 0; a < 8; ++a) {
#pragma unroll
      for (int k = 0; k < 4; ++k) {
        const unsigned lo = __builtin_amdgcn_perm(vr[2 * a + 1][k], vr[2 * a][k], 0x05040100u);
        const unsigned hi = __builtin_amdgcn_perm(vr[2 * a + 1][k], vr[2 * a][k], 0x07060302u);
#pragma unroll
        for (int h = 0; h < 2; ++h) {
          acc[h][2 * k] = dot2(lo, pp[h][a >> 2][a & 3], acc[h][2 * k]);
          acc[h][2 * k + 1] = dot2(hi, pp[h][a >> 2][a & 3], acc[h][2 * k + 1]);
        }
      }
      SCHED_FENCE();
    }
    {
      f32x4 o0, o1;
#pragma unroll
      for (int k = 0; k < 4; ++k) { o0[k] = fold32(acc[0][k], acc[1][k]); o1[k] = fold32(acc[0][4 + k], acc[1][4 + k]); }
      lds_st<f32x4>(lds, L_PV + w8 * 2048 + lane * 32, o0);
      lds_st<f32x4>(lds, L_PV + w8 * 2048 + lane * 32 + 16, o1);
    }
    float fw[8], mwg = 0.f, lwg = 0.f;
    if (w8 < 2) {
      f32x4 ml[4];
#pragma unroll
      for (int u = 0; u < 4; ++u) ml[u] = lds_ld<f32x4>(lds, L_ML + w8 * 64 + u * 16);
      mwg = ml[0][0];
#pragma unroll
      for (int u = 0; u < 4; ++u) mwg = fmaxf(mwg, fmaxf(ml[u][0], ml[u][2]));
#pragma unroll
      for (int u = 0; u < 4; ++u) {
        fw[2 * u] = __builtin_amdgcn_exp2f((ml[u][0] - mwg) * LOG2E);
        fw[2 * u + 1] = __builtin_amdgcn_exp2f((ml[u][2] - mwg) * LOG2E);
        lwg = fmaf(fw[2 * u], ml[u][1], lwg);
        lwg = fmaf(fw[2 * u + 1], ml[u][3], lwg);
      }
      if (lane == 0) st_sc1(tag4(f32x4{mwg, lwg, 0.f, 0.f}, tag), rws, 0, WS_ML + (((hk * 2 + w8) * 4 + b) * 8 + j) * 16);
    }
    STAMP(6);
    WG_BARRIER();
    STAMP(7);
    if (w8 < 2) {
      f32x4 o = lds_ld<f32x4>(lds, L_PV + w8 * 1024 + lane * 16) * fw[0];
#pragma unroll
      for (int p = 1; p < 8; ++p) o += lds_ld<f32x4>(lds, L_PV + p * 2048 + w8 * 1024 + lane * 16) * fw[p];
      st_sc1(tag4(o, tag), rws, (lane >> 3) * 4096 + (lane & 7) * 16, WS_OP + (hk * 2 + w8) * 32768 + (b * 8 + j) * 128);
      STAMP(8);
      if (is_wo || is_red) WG_BARRIER();
    } else if (is_wo && w8 >= 4) {
      const int bw = w8 - 4, un = hk * 16 + (idx - 16);
      u32x4 ov, mv;
      _Pragma("nounroll") for (unsigned s = 0; s < SPIN_MAX; ++s) {
        ov = ld_sc1(rws, lane * 16, WS_OP + un * 4096 + bw * 1024);
        mv = ld_sc1(rws, (lane >> 3) * 16, WS_ML + ((un >> 3) * 4 + bw) * 128);
        if (wave_all(tag4_ok(ov, tag) & tag4_ok(mv, tag))) break;
        poll_pause();
      }
      STAMP(8);
      const float m = asf(mv[0] & 0xffffff00u), l = asf(mv[1] & 0xffffff00u);
      float mx = fmaxf(m, dpp<XOR8>(m));
      mx = max32(max16(mx));
      const float f = __builtin_amdgcn_exp2f((m - mx) * LOG2E);
      float ls = f * l;
      ls += dpp<XOR8>(ls);
      ls = fold16(ls, ls);
      ls = fold32(ls, ls);
      const float o = fold_hi(untag4(ov) * f) / ls;
      const int c8 = lane & 7;
      if (!(lane & 8)) lds_st<u16>(lds, L_A + bw * 64 + (c8 & 3) * 16 + ((c8 >> 2) * 4 + (lane >> 4)) * 2, (u16)pack_bf16(o, o));
      WG_BARRIER();
      STAMP(9);
    } else if (is_red && w8 == 2) {
      const int r = idx * 8 + hk, oi = (lane >> 4) * DM + r * 8 + (lane & 7);
      const float xv = bf_lo(lds_ld<u16>(lds, L_X + oi * 2));
      u32x4 dv[7];
      _Pragma("nounroll") for (unsigned s = 0; s < SPIN_MAX; ++s) {
        bool ok = true;
#pragma unroll
        for (int u = 0; u < 7; ++u) dv[u] = ld_sc1(rws, lane * 16, WS_RP + (r * 184 + 128) * 128 + u * 1024);
#pragma unroll
        for (int u = 0; u < 7; ++u) ok &= tag4_ok(dv[u], tag);
        if (wave_all(ok)) break;
        poll_pause();
      }
      f32x4 sum = untag4(dv[0]);
#pragma unroll
      for (int u = 1; u < 7; ++u) sum += untag4(dv[u]);
      const float dn = round_bf16(fold_hi(sum));
      float g = round_bf16(dn / (1.0f + __builtin_amdgcn_exp2f(-dn * LOG2E)));
      asm volatile("" : "+v"(g));
      STAMP(8);
      WG_BARRIER();
      STAMP(9);
      const f32x4 fp = lds_ld<f32x4>(lds, L_FIN + ((lane >> 4) * 8 + (lane & 7)) * 32 + (lane & 8) * 2);
      float sv = (fp[0] + fp[1]) + (fp[2] + fp[3]);
      sv = round_bf16(sv + dpp<XOR8>(sv));
      const float res = xv + round_bf16(sv * g);
      if (!(lane & 8)) __builtin_amdgcn_raw_buffer_store_b16((u16)pack_bf16(res, res), make_rsrc(out, NB * DM * 2), oi * 2, 0, 0);
      if (r == 0 && lane == 0) __builtin_amdgcn_raw_buffer_store_b32(tag, rws, 0, WS_EPOCH, AUX_SC1);
      STAMP(10);
    } else if (is_wo || is_red) {
      WG_BARRIER();
    }
    STAMP_OUT();
  } else {
    WG_BARRIER();
    STAMP(1);
    const rsrc_t rk = make_rsrc(kc + kv_base, 131072);
    u32x4 kr[16];
#pragma unroll
    for (int i = 0; i < 16; ++i) kr[i] = __builtin_amdgcn_raw_buffer_load_b128(rk, lane * 16, ((w8 * 16 + i + rot) & 127) << 10, KV_AUX);
    STAMP(2);
    WG_BARRIER();
    STAMP(3);
    const unsigned tag = __builtin_amdgcn_readfirstlane(lds_ld<unsigned>(lds, L_TAG));
    if (w8 < 4) {
      u32x4 a0, a1;
      _Pragma("nounroll") for (unsigned s = 0; s < SPIN_MAX; ++s) {
        a0 = ld_sc1(rws, lane * 16, WS_QP + hk * 32768 + b * 8192 + w8 * 1024);
        a1 = ld_sc1(rws, lane * 16, WS_QP + hk * 32768 + b * 8192 + 4096 + w8 * 1024);
        if (wave_all((a0[1] == tag) & (a0[3] == tag) & (a1[1] == tag) & (a1[3] == tag))) break;
        poll_pause();
      }
      const int col = (w8 * 2 + (lane >> 5)) * 64 + (lane & 15) * 4 + ((lane >> 4) & 1) * 2;
      lds_st<unsigned>(lds, L_Q + col * 2, pack_bf16(asf(a0[0]) + asf(a1[0]), asf(a0[2]) + asf(a1[2])));
      lds_arrive(lds, L_FLAG + 8, lane);
    } else if (w8 == 4 && is_down) {
      const int un = (idx - 9) * 8 + hk;
      u32x4 gv[4];
      _Pragma("nounroll") for (unsigned s = 0; s < SPIN_MAX; ++s) {
        bool ok = true;
#pragma unroll
        for (int u = 0; u < 4; ++u) gv[u] = ld_sc1(rws, lane * 16, WS_GP + un * 4096 + u * 1024);
#pragma unroll
        for (int u = 0; u < 4; ++u) ok &= (gv[u][1] == tag) & (gv[u][3] == tag);
        if (wave_all(ok)) break;
        poll_pause();
      }
      const unsigned gt = pack_bf16(asf(gv[0][0]) + asf(gv[1][0]), asf(gv[0][2]) + asf(gv[1][2]));
      const unsigned up = pack_bf16(asf(gv[2][0]) + asf(gv[3][0]), asf(gv[2][2]) + asf(gv[3][2]));
      const float g0 = bf_lo(gt), g1 = bf_hi(gt);
      const unsigned hv = pack_bf16(g0 * (g0 > 0.f ? 1.f : 0.f) * bf_lo(up), g1 * (g1 > 0.f ? 1.f : 0.f) * bf_hi(up));
      const int c8 = lane & 7;
      lds_st<unsigned>(lds, L_A + (lane >> 4) * 64 + (c8 & 3) * 16 + ((c8 >> 2) * 4 + ((lane >> 3) & 1) * 2) * 2, hv);
    }
    STAMP(4);
    lds_wait_eq(lds, L_FLAG + 8, 4u);
    STAMP(5);
    {
      const u32x4 q0 = lds_ld<u32x4>(lds, L_Q + (lane & 31) * 16), q1 = lds_ld<u32x4>(lds, L_Q + 512 + (lane & 31) * 16);
      float v[16];
#pragma unroll
      for (int i = 0; i < 16; ++i) {
        float d0 = 0.f, d1 = 0.f;
#pragma unroll
        for (int k = 0; k < 4; ++k) { d0 = dot2(kr[i][k], q0[k], d0); d1 = dot2(kr[i][k], q1[k], d1); }
        v[i] = fold16(d0, d1);
      }
#pragma unroll
      for (int i = 0; i < 8; ++i) v[i] = foldrow<XOR8>(v[i], v[i + 8], lane & 8);
#pragma unroll
      for (int i = 0; i < 4; ++i) v[i] = foldrow<XOR7>(v[i], v[i + 4], lane & 4);
#pragma unroll
      for (int i = 0; i < 2; ++i) v[i] = foldrow<XOR2>(v[i], v[i + 2], lane & 2);
      const float sc = round_bf16(foldrow<XOR1>(v[0], v[1], lane & 1)) * 0.0625f;
      const float m = max32(row_max(sc));
      const float p = __builtin_amdgcn_exp2f((sc - m) * LOG2E);
      float l = row_sum(p);
      l = fold32(l, l);
      lds_st<u16>(lds, L_P + w8 * 128 + lane * 2, (u16)pack_bf16(p, p));
      if ((lane & 47) == 0) lds_st<f32x2>(lds, L_ML + (lane >> 4) * 64 + w8 * 8, f32x2{m, l});
    }
    STAMP(6);
    WG_BARRIER();
    const bool late = (is_wo || is_down) && w8 < 7;
    const int prod = is_wo ? hk * 16 + (idx - 16) : 128 + (idx - 9) * 8 + hk;
    if (late) {
      const rsrc_t rw = make_rsrc(is_wo ? wo : wd, (is_wo ? QW : FF) * DM * 2);
      tile_fetch(rw, DM * 2, (is_wo ? prod : prod - 128) * 32 * DM * 2 + w8 * 128, lds + L_TILE + w8 * 4096, lane);
    }
    STAMP(7);
    WG_BARRIER();
    if (late) {
      asm volatile("s_waitcnt vmcnt(0)" ::: "memory");
      STAMP(8);
      bf16x8 wf[4];
      tile_frags(lds + L_TILE + w8 * 4096, lane, wf);
      if (is_wo) WG_BARRIER();
      STAMP(9);
      const u32x4 act = lds_ld<u32x4>(lds, L_A + (lane & 3) * 64 + (lane >> 4) * 16);
      const f32x4 acc = tile_mma<true>(act, wf, lane);
      st_sc1(tag4(acc, tag), rws, (lane >> 3) * (184 * 128) + (lane & 7) * 16, WS_RP + (w8 * 8 * 184 + prod) * 128);
      STAMP(10);
    } else if (is_red) {
      const int r = idx * 8 + hk;
      u32x4 a0, a1;
      _Pragma("nounroll") for (unsigned s = 0; s < SPIN_MAX; ++s) {
        a0 = ld_sc1(rws, lane * 16, WS_RP + r * 184 * 128 + w8 * 2048);
        a1 = ld_sc1(rws, lane * 16, WS_RP + r * 184 * 128 + w8 * 2048 + 1024);
        if (wave_all(tag4_ok(a0, tag) & tag4_ok(a1, tag))) break;
        poll_pause();
      }
      STAMP(8);
      const float sv = fold_hi(untag4(a0) + untag4(a1));
      if (!(lane & 8)) lds_st<float>(lds, L_FIN + ((lane >> 4) * 8 + (lane & 7)) * 32 + w8 * 4, sv);
      WG_BARRIER();
      STAMP(9);
    } else if (is_wo) {
      WG_BARRIER();
    }
    STAMP_OUT();
  }
}

}

extern "C" void kernel_launch(void* const* d_in, const int* in_sizes, int n_in, void* d_out, int out_size, void* d_ws,
                              size_t ws_size, hipStream_t stream) {
  fused_decode<<<256, 1024, 0, stream>>>((const u16*)d_in[0], (const u16*)d_in[1], (const u16*)d_in[2], (const u16*)d_in[3],
                                         (const u16*)d_in[4], (const u16*)d_in[6], (const u16*)d_in[7], (const u16*)d_in[8],
                                         (u16*)d_out, (char*)d_ws + WS_OFF);
}
```

```cpp
#define PROBE 0
#define PROBE_GRP 0
#include <hip/hip_runtime.h>
#include <hip/hip_bf16.h>
#include <stdint.h>

#define DIAG 0
#define KV_AUX 2
#define W_AUX 2
#define WS_OFF 0

namespace {

typedef unsigned short u16;
typedef unsigned long long u64;
typedef unsigned u32x2 __attribute__((ext_vector_type(2)));
typedef unsigned u32x4 __attribute__((ext_vector_type(4)));
typedef float f32x2 __attribute__((ext_vector_type(2)));
typedef float f32x4 __attribute__((ext_vector_type(4)));
typedef short s16x4 __attribute__((ext_vector_type(4)));
typedef __bf16 bf16x2 __attribute__((ext_vector_type(2)));
typedef __bf16 bf16x8 __attribute__((ext_vector_type(8)));
#define LAS __attribute__((address_space(3)))
typedef __amdgpu_buffer_rsrc_t rsrc_t;

constexpr int NB = 4, DM = 448, SK = 8192, WIN = 2048, FF = 1792, QW = 4096;
constexpr int AUX_SC1 = 16;
constexpr unsigned SPIN_MAX = 1u << 18;
constexpr float LOG2E = 1.4426950408889634f;

constexpr unsigned WS_EPOCH = 0;
constexpr unsigned WS_QP = 4096;
constexpr unsigned WS_GP = WS_QP + 8 * 4 * 8192;
constexpr unsigned WS_OP = WS_GP + 56 * 4096;
constexpr unsigned WS_ML = WS_OP + 128 * 4096;
constexpr unsigned WS_RP = WS_ML + 16 * 32 * 16;
constexpr unsigned WS_END = WS_RP + 56 * 184 * 128;
constexpr unsigned WS_DIAG = 8u << 20;

constexpr int L_TILE = 0;
constexpr int L_PV = L_TILE + 7 * 4096;
constexpr int L_EP = L_PV + 8 * 2048;
constexpr int L_X = L_EP + 7 * 1024;
constexpr int L_Q = L_X + 4 * 896;
constexpr int L_P = L_Q + 1024;
constexpr int L_FIN = L_P + 1024;
constexpr int L_A = L_FIN + 1024;
constexpr int L_ML = L_A + 256;
constexpr int L_ST = L_ML + 128;
constexpr int L_FLAG = L_ST + 32;
constexpr int L_TAG = L_FLAG + 16;
constexpr int L_END = L_TAG + 16;

#define SCHED_FENCE() __builtin_amdgcn_sched_barrier(0)
#define WG_BARRIER() do { SCHED_FENCE(); asm volatile("s_waitcnt lgkmcnt(0)\n\ts_barrier" ::: "memory"); SCHED_FENCE(); } while (0)

__device__ __forceinline__ float asf(unsigned u) { return __builtin_bit_cast(float, u); }
__device__ __forceinline__ unsigned asu(float f) { return __builtin_bit_cast(unsigned, f); }
__device__ __forceinline__ unsigned pack_bf16(float lo, float hi) {
  f32x2 f = {lo, hi};
  return __builtin_bit_cast(unsigned, __builtin_convertvector(f, bf16x2));
}
__device__ __forceinline__ float round_bf16(float v) { return asf(pack_bf16(v, v) & 0xffff0000u); }
__device__ __forceinline__ float bf_lo(unsigned u) { return asf(u << 16); }
__device__ __forceinline__ float bf_hi(unsigned u) { return asf(u & 0xffff0000u); }
__device__ __forceinline__ float dot2(unsigned a, unsigned b, float c) {
  return __builtin_amdgcn_fdot2_f32_bf16(__builtin_bit_cast(bf16x2, a), __builtin_bit_cast(bf16x2, b), c, false);
}
template <int CTRL> __device__ __forceinline__ float dpp(float x) {
  return asf((unsigned)__builtin_amdgcn_mov_dpp((int)asu(x), CTRL, 0xf, 0xf, true));
}
constexpr int XOR1 = 0xB1, XOR2 = 0x4E, XOR7 = 0x141, XOR8 = 0x128;
__device__ __forceinline__ float fold16(float a, float b) {
  auto s = __builtin_amdgcn_permlane16_swap(asu(a), asu(b), false, false);
  return asf(s[0]) + asf(s[1]);
}
__device__ __forceinline__ float fold32(float a, float b) {
  auto s = __builtin_amdgcn_permlane32_swap(asu(a), asu(b), false, false);
  return asf(s[0]) + asf(s[1]);
}
__device__ __forceinline__ float max16(float a) {
  auto s = __builtin_amdgcn_permlane16_swap(asu(a), asu(a), false, false);
  return fmaxf(asf(s[0]), asf(s[1]));
}
__device__ __forceinline__ float max32(float a) {
  auto s = __builtin_amdgcn_permlane32_swap(asu(a), asu(a), false, false);
  return fmaxf(asf(s[0]), asf(s[1]));
}
template <int CTRL> __device__ __forceinline__ float foldrow(float lo, float hi, bool bit) {
  const float keep = bit ? hi : lo, send = bit ? lo : hi;
  return keep + dpp<CTRL>(send);
}
__device__ __forceinline__ float row_sum(float v) {
  v += dpp<XOR8>(v); v += dpp<XOR7>(v); v += dpp<XOR2>(v); v += dpp<XOR1>(v);
  return v;
}
__device__ __forceinline__ float row_max(float v) {
  v = fmaxf(v, dpp<XOR8>(v)); v = fmaxf(v, dpp<XOR7>(v)); v = fmaxf(v, dpp<XOR2>(v)); v = fmaxf(v, dpp<XOR1>(v));
  return v;
}
__device__ __forceinline__ rsrc_t make_rsrc(const void* p, unsigned bytes) {
  return __builtin_amdgcn_make_buffer_rsrc(const_cast<void*>(p), 0, bytes, 0x00020000);
}
__device__ __forceinline__ u32x4 tag4(f32x4 v, unsigned tag) {
  u32x4 r;
  r[0] = __builtin_amdgcn_perm(asu(v[0]), tag, 0x07060500u);
  r[1] = __builtin_amdgcn_perm(asu(v[1]), tag, 0x07060501u);
  r[2] = __builtin_amdgcn_perm(asu(v[2]), tag, 0x07060502u);
  r[3] = __builtin_amdgcn_perm(asu(v[3]), tag, 0x07060503u);
  return r;
}
__device__ __forceinline__ bool tag4_ok(u32x4 v, unsigned tag) {
  return (__builtin_amdgcn_perm(v[1], v[0], 0x0c0c0400u) | __builtin_amdgcn_perm(v[3], v[2], 0x04000c0cu)) == tag;
}
__device__ __forceinline__ f32x4 untag4(u32x4 v) {
  return f32x4{asf(v[0] & 0xffffff00u), asf(v[1] & 0xffffff00u), asf(v[2] & 0xffffff00u), asf(v[3] & 0xffffff00u)};
}
__device__ __forceinline__ u32x4 ld_sc1(rsrc_t r, unsigned voff, unsigned soff) {
  return __builtin_amdgcn_raw_buffer_load_b128(r, voff, soff, AUX_SC1);
}
__device__ __forceinline__ void st_sc1(u32x4 v, rsrc_t r, unsigned voff, unsigned soff) {
  __builtin_amdgcn_raw_buffer_store_b128(v, r, voff, soff, AUX_SC1);
}
__device__ __forceinline__ bool wave_all(bool ok) { return __builtin_amdgcn_ballot_w64(!ok) == 0ull; }
__device__ __forceinline__ void poll_pause() { asm volatile("s_sleep 1" ::: "memory"); }

template <typename T> __device__ __forceinline__ T lds_ld(LAS char* l, int off) { return *(LAS T*)(l + off); }
template <typename T> __device__ __forceinline__ void lds_st(LAS char* l, int off, T v) { *(LAS T*)(l + off) = v; }
__device__ __forceinline__ void lds_wait_eq(LAS char* l, int off, unsigned want) {
  _Pragma("nounroll") for (unsigned s = 0; s < SPIN_MAX; ++s) {
    const unsigned v = __builtin_amdgcn_readfirstlane(*(volatile LAS unsigned*)(l + off));
    if (v == want) break;
    poll_pause();
  }
  asm volatile("" ::: "memory");
}
__device__ __forceinline__ unsigned lds_arrive(LAS char* l, int off, int lane) {
  asm volatile("s_waitcnt lgkmcnt(0)" ::: "memory");
  unsigned old = 0;
  if (lane == 0) old = __hip_atomic_fetch_add((LAS unsigned*)(l + off), 1u, __ATOMIC_RELAXED, __HIP_MEMORY_SCOPE_WORKGROUP);
  old = __builtin_amdgcn_readfirstlane(old);
  asm volatile("" ::: "memory");
  return old;
}

__device__ __forceinline__ void tile_fetch(rsrc_t r, unsigned stride, unsigned soff, LAS char* img, int lane) {
  const int r8 = lane >> 3, c = lane & 7;
  const unsigned voff = r8 * stride + ((c ^ (r8 & 6)) << 4);
#pragma unroll
  for (int i = 0; i < 4; ++i)
    __builtin_amdgcn_raw_ptr_buffer_load_lds(r, (LAS void*)(img + i * 1024), 16, voff, soff + i * 8 * stride, 0, W_AUX);
}
__device__ __forceinline__ void tile_frags(LAS char* img, int lane, bf16x8 (&w)[4]) {
  const int g = lane >> 4, q = (lane >> 2) & 3, p = lane & 3, row = 4 * g + q;
#pragma unroll
  for (int t = 0; t < 4; ++t) {
    LAS char* a = img + row * 128 + (((2 * t + (p >> 1)) ^ (row & 6)) << 4) + 8 * (p & 1);
    const s16x4 lo = __builtin_amdgcn_ds_read_tr16_b64_v4i16((LAS s16x4*)a);
    const s16x4 hi = __builtin_amdgcn_ds_read_tr16_b64_v4i16((LAS s16x4*)(a + 2048));
    w[t] = __builtin_bit_cast(bf16x8, __builtin_shufflevector(lo, hi, 0, 1, 2, 3, 4, 5, 6, 7));
  }
}
template <bool ACT_IS_A> __device__ __forceinline__ f32x4 tile_mma(u32x4 act, const bf16x8 (&w)[4], int lane) {
  const int jj = (lane >> 2) & 3;
  f32x4 acc = {0.f, 0.f, 0.f, 0.f};
#pragma unroll
  for (int t = 0; t < 4; ++t) {
    const u32x4 z = {0u, 0u, 0u, 0u};
    const bf16x8 a = __builtin_bit_cast(bf16x8, jj == t ? act : z);
    acc = ACT_IS_A ? __builtin_amdgcn_mfma_f32_16x16x32_bf16(a, w[t], acc, 0, 0, 0)
                   : __builtin_amdgcn_mfma_f32_16x16x32_bf16(w[t], a, acc, 0, 0, 0);
  }
  return acc;
}
__device__ __forceinline__ float fold_hi(f32x4 v) {
  const float z = fold16(fold32(v[0], v[2]), fold32(v[1], v[3]));
  return z + dpp<XOR8>(z);
}

#define STAMP(k) do { if ((k) == PROBE) { SCHED_FENCE(); pst = __builtin_amdgcn_s_memrealtime(); SCHED_FENCE(); } } while (0)
#define STAMP_OUT() do { if ((wave >> 3) == PROBE_GRP && pst) while (__builtin_amdgcn_s_memrealtime() < pst + 4000) __builtin_amdgcn_s_sleep(4); } while (0)

__global__ void __launch_bounds__(1024, 4)
fused_decode(const u16* x, const u16* kc, const u16* vc, const u16* wq,
             const u16* wo, const u16* wg, const u16* wu, const u16* wd,
             u16* out, char* ws) {
  __shared__ __attribute__((aligned(16))) char lds_raw[L_END];
  LAS char* lds = (LAS char*)lds_raw;
  u64 pst = 0;
  STAMP(0);
  asm volatile("" : "+s"(x), "+s"(kc), "+s"(vc), "+s"(wq), "+s"(wo), "+s"(wg), "+s"(wu), "+s"(wd), "+s"(out), "+s"(ws));
  STAMP(11);
  const int bid = blockIdx.x, lane = threadIdx.x & 63, wave = __builtin_amdgcn_readfirstlane(threadIdx.x >> 6);
  const int hk = bid & 7, idx = bid >> 3, j = idx & 7, b = idx >> 3;
  const int w8 = wave & 7;
  const rsrc_t rws = make_rsrc(ws, WS_END);
  const size_t kv_base = ((size_t)(b * 8 + hk) * SK + (SK - WIN) + j * 256) * 256;
  const unsigned rot = (unsigned)(((b * 8 + hk) * 8 + j) * 37);

  const bool is_wo = idx >= 16, is_down = idx >= 9 && idx < 16, is_red = idx < 7;

  if (wave >= 8) {
    __builtin_amdgcn_s_setprio(2);
    const bool has_early = idx < 30;
    const bool is_q = idx < 16, is_gate = idx >= 16 && idx < 23;
    const int gu = ((is_gate ? idx - 16 : idx - 23) << 3) + hk;
    const int half = is_q ? (idx >> 3) : (gu & 1);
    const int etile = is_q ? (hk * 8 + (idx & 7)) : (gu >> 1);
    u32x4 xr[4];
    unsigned epoch, tag;
    if (w8 == 7) {
      epoch = __builtin_amdgcn_raw_buffer_load_b32(rws, 0, WS_EPOCH, AUX_SC1);
      const rsrc_t rx = make_rsrc(x, NB * DM * 2);
#pragma unroll
      for (int i = 0; i < 4; ++i) xr[i] = __builtin_amdgcn_raw_buffer_load_b128(rx, lane < 56 ? lane * 16 : 0x7fffffff, i * 896, 0);
      if (lane < 4) lds_st<unsigned>(lds, L_FLAG + lane * 4, 0u);
    } else if (has_early) {
      const unsigned stride = is_q ? QW * 2 : FF * 2;
      const rsrc_t rw = make_rsrc(is_q ? wq : (is_gate ? wg : wu), DM * stride);
      tile_fetch(rw, stride, (half * 224 + w8 * 32) * stride + etile * 128, lds + L_TILE + w8 * 4096, lane);
    }
    WG_BARRIER();
    STAMP(1);
    if (w8 == 7) {
      if (lane == 0) lds_st<unsigned>(lds, L_TAG, ((epoch & 0x7f7f7f7fu) + 0x01010101u) ^ (epoch & 0x80808080u));
      float s[4], v[4][8];
#pragma unroll
      for (int i = 0; i < 4; ++i) {
        if (lane < 56) lds_st<u32x4>(lds, L_X + i * 896 + lane * 16, xr[i]);
        float t = 0.f;
#pragma unroll
        for (int k = 0; k < 4; ++k) { v[i][2 * k] = bf_lo(xr[i][k]); v[i][2 * k + 1] = bf_hi(xr[i][k]); t += v[i][2 * k] + v[i][2 * k + 1]; }
        s[i] = t;
      }
      const float mean_l = row_sum(fold16(fold32(s[0], s[2]), fold32(s[1], s[3]))) * (1.0f / DM);
#pragma unroll
      for (int i = 0; i < 4; ++i) {
        const float mean = asf(__builtin_amdgcn_readlane(asu(mean_l), i * 16));
        float t = 0.f;
#pragma unroll
        for (int k = 0; k < 8; ++k) { const float d = v[i][k] - mean; t = fmaf(d, d, t); }
        s[i] = lane < 56 ? t : 0.f;
      }
      const float var = round_bf16(row_sum(fold16(fold32(s[0], s[2]), fold32(s[1], s[3]))) * (1.0f / DM));
      const float den = round_bf16(sqrtf(round_bf16(var + 1e-5f)));
      if ((lane & 15) == 0) lds_st<f32x2>(lds, L_ST + (lane >> 4) * 8, f32x2{round_bf16(mean_l), den});
      asm volatile("s_waitcnt lgkmcnt(0)" ::: "memory");
      if (lane == 0) *(volatile LAS unsigned*)(lds + L_FLAG) = 1u;
    } else if (has_early) {
      lds_wait_eq(lds, L_FLAG, 1u);
      const int bt = lane & 3, g = lane >> 4;
      const f32x2 stt = lds_ld<f32x2>(lds, L_ST + bt * 8);
      u32x4 act;
#pragma unroll
      for (int h = 0; h < 2; ++h) {
        const u32x2 xx = lds_ld<u32x2>(lds, L_X + bt * 896 + (half * 224 + w8 * 32 + 16 * h + 4 * g) * 2);
#pragma unroll
        for (int k = 0; k < 2; ++k) {
          const unsigned d = pack_bf16(bf_lo(xx[k]) - stt[0], bf_hi(xx[k]) - stt[0]);
          act[2 * h + k] = pack_bf16(bf_lo(d) / stt[1], bf_hi(d) / stt[1]);
        }
      }
      asm volatile("s_waitcnt vmcnt(0)" ::: "memory");
      bf16x8 wf[4];
      tile_frags(lds + L_TILE + w8 * 4096, lane, wf);
      const f32x4 acc = tile_mma<false>(act, wf, lane);
      lds_st<f32x4>(lds, L_EP + w8 * 1024 + bt * 256 + ((lane & 12) + g) * 16, acc);
      if (lds_arrive(lds, L_FLAG + 4, lane) == 6u) {
        f32x4 sum = lds_ld<f32x4>(lds, L_EP + lane * 16);
#pragma unroll
        for (int p = 1; p < 7; ++p) sum += lds_ld<f32x4>(lds, L_EP + p * 1024 + lane * 16);
        tag = lds_ld<unsigned>(lds, L_TAG);
        const int pb = lane >> 4, cq = lane & 15;
        const unsigned voff = is_q ? pb * 8192 + cq * 16 : (cq >> 3) * 4096 + pb * 256 + (cq & 7) * 16;
        const unsigned soff = __builtin_amdgcn_readfirstlane(is_q ? WS_QP + hk * 32768 + half * 4096 + (idx & 7) * 512
                                                                  : WS_GP + etile * 8192 + (is_gate ? 0 : 2048) + half * 1024);
        const unsigned step = __builtin_amdgcn_readfirstlane(is_q ? 256 : 128);
        st_sc1(u32x4{asu(sum[0]), tag, asu(sum[1]), tag}, rws, voff, soff);
        st_sc1(u32x4{asu(sum[2]), tag, asu(sum[3]), tag}, rws, voff, soff + step);
      }
    }
    STAMP(2);
    __builtin_amdgcn_s_setprio(0);
    WG_BARRIER();
    STAMP(3);
    const rsrc_t rv = make_rsrc(vc + kv_base, 131072);
    u32x4 vr[16];
#pragma unroll
    for (int i = 0; i < 16; ++i) vr[i] = __builtin_amdgcn_raw_buffer_load_b128(rv, lane * 16, ((w8 * 16 + i + rot) & 127) << 10, KV_AUX);
    STAMP(4);
    WG_BARRIER();
    STAMP(5);
    tag = __builtin_amdgcn_readfirstlane(lds_ld<unsigned>(lds, L_TAG));
    u32x4 pp[2][2];
#pragma unroll
    for (int h = 0; h < 2; ++h)
#pragma unroll
      for (int u = 0; u < 2; ++u) pp[h][u] = lds_ld<u32x4>(lds, L_P + w8 * 128 + (lane >> 5) * 64 + h * 32 + u * 16);
    float acc[2][8];
#pragma unroll
    for (int k = 0; k < 8; ++k) { acc[0][k] = 0.f; acc[1][k] = 0.f; }
#pragma unroll
    for (int a = 0; a < 8; ++a) {
#pragma unroll
      for (int k = 0; k < 4; ++k) {
        const unsigned lo = __builtin_amdgcn_perm(vr[2 * a + 1][k], vr[2 * a][k], 0x05040100u);
        const unsigned hi = __builtin_amdgcn_perm(vr[2 * a + 1][k], vr[2 * a][k], 0x07060302u);
#pragma unroll
        for (int h = 0; h < 2; ++h) {
          acc[h][2 * k] = dot2(lo, pp[h][a >> 2][a & 3], acc[h][2 * k]);
          acc[h][2 * k + 1] = dot2(hi, pp[h][a >> 2][a & 3], acc[h][2 * k + 1]);
        }
      }
      SCHED_FENCE();
    }
    {
      f32x4 o0, o1;
#pragma unroll
      for (int k = 0; k < 4; ++k) { o0[k] = fold32(acc[0][k], acc[1][k]); o1[k] = fold32(acc[0][4 + k], acc[1][4 + k]); }
      lds_st<f32x4>(lds, L_PV + w8 * 2048 + lane * 32, o0);
      lds_st<f32x4>(lds, L_PV + w8 * 2048 + lane * 32 + 16, o1);
    }
    float fw[8], mwg = 0.f, lwg = 0.f;
    if (w8 < 2) {
      f32x4 ml[4];
#pragma unroll
      for (int u = 0; u < 4; ++u) ml[u] = lds_ld<f32x4>(lds, L_ML + w8 * 64 + u * 16);
      mwg = ml[0][0];
#pragma unroll
      for (int u = 0; u < 4; ++u) mwg = fmaxf(mwg, fmaxf(ml[u][0], ml[u][2]));
#pragma unroll
      for (int u = 0; u < 4; ++u) {
        fw[2 * u] = __builtin_amdgcn_exp2f((ml[u][0] - mwg) * LOG2E);
        fw[2 * u + 1] = __builtin_amdgcn_exp2f((ml[u][2] - mwg) * LOG2E);
        lwg = fmaf(fw[2 * u], ml[u][1], lwg);
        lwg = fmaf(fw[2 * u + 1], ml[u][3], lwg);
      }
      if (lane == 0) st_sc1(tag4(f32x4{mwg, lwg, 0.f, 0.f}, tag), rws, 0, WS_ML + (((hk * 2 + w8) * 4 + b) * 8 + j) * 16);
    }
    STAMP(6);
    WG_BARRIER();
    STAMP(7);
    if (w8 < 2) {
      f32x4 o = lds_ld<f32x4>(lds, L_PV + w8 * 1024 + lane * 16) * fw[0];
#pragma unroll
      for (int p = 1; p < 8; ++p) o += lds_ld<f32x4>(lds, L_PV + p * 2048 + w8 * 1024 + lane * 16) * fw[p];
      st_sc1(tag4(o, tag), rws, (lane >> 3) * 4096 + (lane & 7) * 16, WS_OP + (hk * 2 + w8) * 32768 + (b * 8 + j) * 128);
      STAMP(8);
      if (is_wo || is_red) WG_BARRIER();
    } else if (is_wo && w8 >= 4) {
      const int bw = w8 - 4, un = hk * 16 + (idx - 16);
      u32x4 ov, mv;
      _Pragma("nounroll") for (unsigned s = 0; s < SPIN_MAX; ++s) {
        ov = ld_sc1(rws, lane * 16, WS_OP + un * 4096 + bw * 1024);
        mv = ld_sc1(rws, (lane >> 3) * 16, WS_ML + ((un >> 3) * 4 + bw) * 128);
        if (wave_all(tag4_ok(ov, tag) & tag4_ok(mv, tag))) break;
        poll_pause();
      }
      STAMP(8);
      const float m = asf(mv[0] & 0xffffff00u), l = asf(mv[1] & 0xffffff00u);
      float mx = fmaxf(m, dpp<XOR8>(m));
      mx = max32(max16(mx));
      const float f = __builtin_amdgcn_exp2f((m - mx) * LOG2E);
      float ls = f * l;
      ls += dpp<XOR8>(ls);
      ls = fold16(ls, ls);
      ls = fold32(ls, ls);
      const float o = fold_hi(untag4(ov) * f) / ls;
      const int c8 = lane & 7;
      if (!(lane & 8)) lds_st<u16>(lds, L_A + bw * 64 + (c8 & 3) * 16 + ((c8 >> 2) * 4 + (lane >> 4)) * 2, (u16)pack_bf16(o, o));
      WG_BARRIER();
      STAMP(9);
    } else if (is_red && w8 == 2) {
      const int r = idx * 8 + hk, oi = (lane >> 4) * DM + r * 8 + (lane & 7);
      const float xv = bf_lo(lds_ld<u16>(lds, L_X + oi * 2));
      u32x4 dv[7];
      _Pragma("nounroll") for (unsigned s = 0; s < SPIN_MAX; ++s) {
        bool ok = true;
#pragma unroll
        for (int u = 0; u < 7; ++u) dv[u] = ld_sc1(rws, lane * 16, WS_RP + (r * 184 + 128) * 128 + u * 1024);
#pragma unroll
        for (int u = 0; u < 7; ++u) ok &= tag4_ok(dv[u], tag);
        if (wave_all(ok)) break;
        poll_pause();
      }
      f32x4 sum = untag4(dv[0]);
#pragma unroll
      for (int u = 1; u < 7; ++u) sum += untag4(dv[u]);
      const float dn = round_bf16(fold_hi(sum));
      float g = round_bf16(dn / (1.0f + __builtin_amdgcn_exp2f(-dn * LOG2E)));
      asm volatile("" : "+v"(g));
      STAMP(8);
      WG_BARRIER();
      STAMP(9);
      const f32x4 fp = lds_ld<f32x4>(lds, L_FIN + ((lane >> 4) * 8 + (lane & 7)) * 32 + (lane & 8) * 2);
      float sv = (fp[0] + fp[1]) + (fp[2] + fp[3]);
      sv = round_bf16(sv + dpp<XOR8>(sv));
      const float res = xv + round_bf16(sv * g);
      if (!(lane & 8)) __builtin_amdgcn_raw_buffer_store_b16((u16)pack_bf16(res, res), make_rsrc(out, NB * DM * 2), oi * 2, 0, 0);
      if (r == 0 && lane == 0) __builtin_amdgcn_raw_buffer_store_b32(tag, rws, 0, WS_EPOCH, AUX_SC1);
      STAMP(10);
    } else if (is_wo || is_red) {
      WG_BARRIER();
    }
    STAMP_OUT();
  } else {
    WG_BARRIER();
    STAMP(1);
    const rsrc_t rk = make_rsrc(kc + kv_base, 131072);
    u32x4 kr[16];
#pragma unroll
    for (int i = 0; i < 16; ++i) kr[i] = __builtin_amdgcn_raw_buffer_load_b128(rk, lane * 16, ((w8 * 16 + i + rot) & 127) << 10, KV_AUX);
    STAMP(2);
    WG_BARRIER();
    STAMP(3);
    const unsigned tag = __builtin_amdgcn_readfirstlane(lds_ld<unsigned>(lds, L_TAG));
    if (w8 < 4) {
      u32x4 a0, a1;
      _Pragma("nounroll") for (unsigned s = 0; s < SPIN_MAX; ++s) {
        a0 = ld_sc1(rws, lane * 16, WS_QP + hk * 32768 + b * 8192 + w8 * 1024);
        a1 = ld_sc1(rws, lane * 16, WS_QP + hk * 32768 + b * 8192 + 4096 + w8 * 1024);
        if (wave_all((a0[1] == tag) & (a0[3] == tag) & (a1[1] == tag) & (a1[3] == tag))) break;
        poll_pause();
      }
      const int col = (w8 * 2 + (lane >> 5)) * 64 + (lane & 15) * 4 + ((lane >> 4) & 1) * 2;
      lds_st<unsigned>(lds, L_Q + col * 2, pack_bf16(asf(a0[0]) + asf(a1[0]), asf(a0[2]) + asf(a1[2])));
      lds_arrive(lds, L_FLAG + 8, lane);
    } else if (w8 == 4 && is_down) {
      const int un = (idx - 9) * 8 + hk;
      u32x4 gv[4];
      _Pragma("nounroll") for (unsigned s = 0; s < SPIN_MAX; ++s) {
        bool ok = true;
#pragma unroll
        for (int u = 0; u < 4; ++u) gv[u] = ld_sc1(rws, lane * 16, WS_GP + un * 4096 + u * 1024);
#pragma unroll
        for (int u = 0; u < 4; ++u) ok &= (gv[u][1] == tag) & (gv[u][3] == tag);
        if (wave_all(ok)) break;
        poll_pause();
      }
      const unsigned gt = pack_bf16(asf(gv[0][0]) + asf(gv[1][0]), asf(gv[0][2]) + asf(gv[1][2]));
      const unsigned up = pack_bf16(asf(gv[2][0]) + asf(gv[3][0]), asf(gv[2][2]) + asf(gv[3][2]));
      const float g0 = bf_lo(gt), g1 = bf_hi(gt);
      const unsigned hv = pack_bf16(g0 * (g0 > 0.f ? 1.f : 0.f) * bf_lo(up), g1 * (g1 > 0.f ? 1.f : 0.f) * bf_hi(up));
      const int c8 = lane & 7;
      lds_st<unsigned>(lds, L_A + (lane >> 4) * 64 + (c8 & 3) * 16 + ((c8 >> 2) * 4 + ((lane >> 3) & 1) * 2) * 2, hv);
    }
    STAMP(4);
    lds_wait_eq(lds, L_FLAG + 8, 4u);
    STAMP(5);
    {
      const u32x4 q0 = lds_ld<u32x4>(lds, L_Q + (lane & 31) * 16), q1 = lds_ld<u32x4>(lds, L_Q + 512 + (lane & 31) * 16);
      float v[16];
#pragma unroll
      for (int i = 0; i < 16; ++i) {
        float d0 = 0.f, d1 = 0.f;
#pragma unroll
        for (int k = 0; k < 4; ++k) { d0 = dot2(kr[i][k], q0[k], d0); d1 = dot2(kr[i][k], q1[k], d1); }
        v[i] = fold16(d0, d1);
      }
#pragma unroll
      for (int i = 0; i < 8; ++i) v[i] = foldrow<XOR8>(v[i], v[i + 8], lane & 8);
#pragma unroll
      for (int i = 0; i < 4; ++i) v[i] = foldrow<XOR7>(v[i], v[i + 4], lane & 4);
#pragma unroll
      for (int i = 0; i < 2; ++i) v[i] = foldrow<XOR2>(v[i], v[i + 2], lane & 2);
      const float sc = round_bf16(foldrow<XOR1>(v[0], v[1], lane & 1)) * 0.0625f;
      const float m = max32(row_max(sc));
      const float p = __builtin_amdgcn_exp2f((sc - m) * LOG2E);
      float l = row_sum(p);
      l = fold32(l, l);
      lds_st<u16>(lds, L_P + w8 * 128 + lane * 2, (u16)pack_bf16(p, p));
      if ((lane & 47) == 0) lds_st<f32x2>(lds, L_ML + (lane >> 4) * 64 + w8 * 8, f32x2{m, l});
    }
    STAMP(6);
    WG_BARRIER();
    const bool late = (is_wo || is_down) && w8 < 7;
    const int prod = is_wo ? hk * 16 + (idx - 16) : 128 + (idx - 9) * 8 + hk;
    if (late) {
      const rsrc_t rw = make_rsrc(is_wo ? wo : wd, (is_wo ? QW : FF) * DM * 2);
      tile_fetch(rw, DM * 2, (is_wo ? prod : prod - 128) * 32 * DM * 2 + w8 * 128, lds + L_TILE + w8 * 4096, lane);
    }
    STAMP(7);
    WG_BARRIER();
    if (late) {
      asm volatile("s_waitcnt vmcnt(0)" ::: "memory");
      STAMP(8);
      bf16x8 wf[4];
      tile_frags(lds + L_TILE + w8 * 4096, lane, wf);
      if (is_wo) WG_BARRIER();
      STAMP(9);
      const u32x4 act = lds_ld<u32x4>(lds, L_A + (lane & 3) * 64 + (lane >> 4) * 16);
      const f32x4 acc = tile_mma<true>(act, wf, lane);
      st_sc1(tag4(acc, tag), rws, (lane >> 3) * (184 * 128) + (lane & 7) * 16, WS_RP + (w8 * 8 * 184 + prod) * 128);
      STAMP(10);
    } else if (is_red) {
      const int r = idx * 8 + hk;
      u32x4 a0, a1;
      _Pragma("nounroll") for (unsigned s = 0; s < SPIN_MAX; ++s) {
        a0 = ld_sc1(rws, lane * 16, WS_RP + r * 184 * 128 + w8 * 2048);
        a1 = ld_sc1(rws, lane * 16, WS_RP + r * 184 * 128 + w8 * 2048 + 1024);
        if (wave_all(tag4_ok(a0, tag) & tag4_ok(a1, tag))) break;
        poll_pause();
      }
      STAMP(8);
      const float sv = fold_hi(untag4(a0) + untag4(a1));
      if (!(lane & 8)) lds_st<float>(lds, L_FIN + ((lane >> 4) * 8 + (lane & 7)) * 32 + w8 * 4, sv);
      WG_BARRIER();
      STAMP(9);
    } else if (is_wo) {
      WG_BARRIER();
    }
    STAMP_OUT();
  }
}

}

extern "C" void kernel_launch(void* const* d_in, const int* in_sizes, int n_in, void* d_out, int out_size, void* d_ws,
                              size_t ws_size, hipStream_t stream) {
  fused_decode<<<256, 1024, 0, stream>>>((const u16*)d_in[0], (const u16*)d_in[1], (const u16*)d_in[2], (const u16*)d_in[3],
                                         (const u16*)d_in[4], (const u16*)d_in[6], (const u16*)d_in[7], (const u16*)d_in[8],
                                         (u16*)d_out, (char*)d_ws + WS_OFF);
}
```

```cpp
#include <hip/hip_runtime.h>
#include <hip/hip_bf16.h>
#include <stdint.h>

#define DIAG 0
#define KV_AUX 2
#define W_AUX 2
#define ABL 0
#define WN 12
#define WNK 63
#define HOLD 1
#define LATE 1
#define XMAP 1
#define PF 0
#define CP 10400
#define PRIO 0
#define MLAT 5
#define WS_OFF 0

namespace {

typedef unsigned short u16;
typedef unsigned long long u64;
typedef unsigned u32x2 __attribute__((ext_vector_type(2)));
typedef unsigned u32x4 __attribute__((ext_vector_type(4)));
typedef unsigned u32x16 __attribute__((ext_vector_type(16)));
typedef float f32x2 __attribute__((ext_vector_type(2)));
typedef float f32x4 __attribute__((ext_vector_type(4)));
typedef short s16x4 __attribute__((ext_vector_type(4)));
typedef __bf16 bf16x2 __attribute__((ext_vector_type(2)));
typedef __bf16 bf16x8 __attribute__((ext_vector_type(8)));
#define LAS __attribute__((address_space(3)))
typedef __amdgpu_buffer_rsrc_t rsrc_t;

constexpr int NB = 4, DM = 448, SK = 8192, WIN = 2048, FF = 1792, QW = 4096;
constexpr int AUX_SC1 = 16;
constexpr unsigned SPIN_MAX = 1u << 18;
constexpr float LOG2E = 1.4426950408889634f;

constexpr unsigned WS_EPOCH = 0;
constexpr unsigned WS_QP = 4096;
constexpr unsigned WS_GP = WS_QP + 8 * 4 * 8192;
constexpr unsigned WS_OP = WS_GP + 56 * 4096;
constexpr unsigned WS_ML = WS_OP + 128 * 4096;
constexpr unsigned WS_RP = WS_ML + 16 * 32 * 16;
constexpr unsigned WS_END = WS_RP + 56 * 184 * 128;
constexpr unsigned WS_DIAG = 8u << 20;

constexpr int L_V = 0;
constexpr int L_XN = L_V;
constexpr int L_TILE = 131072;
constexpr int L_FIN = L_TILE;
constexpr int L_Q = L_TILE + 7 * 4096;
constexpr int L_P = L_Q + 1024;
constexpr int L_A = L_P + 1024;
constexpr int L_SYNC = L_A + 256;
constexpr int L_ZERO = L_SYNC, F_XN = L_SYNC + 16, F_EP = L_SYNC + 32, F_Q = L_SYNC + 48, F_T = L_SYNC + 64, L_PROG = L_SYNC + 80;
constexpr int L_MX = L_SYNC + 96;
constexpr int L_LS = L_MX + 32;
constexpr int L_F = L_SYNC + 256;
constexpr int L_END = L_SYNC + 512;

#define SCHED_FENCE() __builtin_amdgcn_sched_barrier(0)
#define MEM_FENCE() asm volatile("" ::: "memory")
#define WG_BARRIER() do { SCHED_FENCE(); asm volatile("s_waitcnt lgkmcnt(0)\n\ts_barrier" ::: "memory"); SCHED_FENCE(); } while (0)
#define WG_BARRIER_BARE() do { SCHED_FENCE(); asm volatile("s_barrier" ::: "memory"); SCHED_FENCE(); } while (0)
#define VM_WAIT(n) do { SCHED_FENCE(); asm volatile("s_waitcnt vmcnt(%0)" :: "i"(n) : "memory"); SCHED_FENCE(); } while (0)
#define VM_DRAIN() do { SCHED_FENCE(); __builtin_amdgcn_s_waitcnt(0x0F70); SCHED_FENCE(); MEM_FENCE(); } while (0)
#define NAP() __builtin_amdgcn_s_sleep(1)
#define PIN_V(x) asm volatile("" : "+v"(x))
#define PIN_S(x) asm volatile("" : "+s"(x))
#define SPIN(limit) _Pragma("nounroll") for (unsigned s_ = 0; s_ < (limit); ++s_)

__device__ __forceinline__ float asf(unsigned u) { return __builtin_bit_cast(float, u); }
__device__ __forceinline__ unsigned asu(float f) { return __builtin_bit_cast(unsigned, f); }
__device__ __forceinline__ unsigned pack_bf16(float lo, float hi) {
  f32x2 f = {lo, hi};
  return __builtin_bit_cast(unsigned, __builtin_convertvector(f, bf16x2));
}
__device__ __forceinline__ float round_bf16(float v) { return asf(pack_bf16(0.f, v)); }
__device__ __forceinline__ float bf_lo(unsigned u) { return asf(u << 16); }
__device__ __forceinline__ float bf_hi(unsigned u) { return asf(u & 0xffff0000u); }
template <int CTRL> __device__ __forceinline__ float dpp(float x) {
  return asf((unsigned)__builtin_amdgcn_mov_dpp((int)asu(x), CTRL, 0xf, 0xf, true));
}
constexpr int XOR1 = 0xB1, XOR2 = 0x4E, XOR7 = 0x141, XOR8 = 0x128;
__device__ __forceinline__ float fold16(float a, float b) {
  auto s = __builtin_amdgcn_permlane16_swap(asu(a), asu(b), false, false);
  return asf(s[0]) + asf(s[1]);
}
__device__ __forceinline__ float fold32(float a, float b) {
  auto s = __builtin_amdgcn_permlane32_swap(asu(a), asu(b), false, false);
  return asf(s[0]) + asf(s[1]);
}
__device__ __forceinline__ float max16(float a, float b) {
  auto s = __builtin_amdgcn_permlane16_swap(asu(a), asu(b), false, false);
  return fmaxf(asf(s[0]), asf(s[1]));
}
__device__ __forceinline__ float max32(float a, float b) {
  auto s = __builtin_amdgcn_permlane32_swap(asu(a), asu(b), false, false);
  return fmaxf(asf(s[0]), asf(s[1]));
}
__device__ __forceinline__ float row_sum(float v) {
  v += dpp<XOR8>(v); v += dpp<XOR7>(v); v += dpp<XOR2>(v); v += dpp<XOR1>(v);
  return v;
}
__device__ __forceinline__ float row_max(float v) {
  v = fmaxf(v, dpp<XOR8>(v)); v = fmaxf(v, dpp<XOR7>(v)); v = fmaxf(v, dpp<XOR2>(v)); v = fmaxf(v, dpp<XOR1>(v));
  return v;
}
__device__ __forceinline__ float fold_hi(f32x4 v) {
  const float z = fold16(fold32(v[0], v[2]), fold32(v[1], v[3]));
  return z + dpp<XOR8>(z);
}
__device__ __forceinline__ rsrc_t make_rsrc(u64 p, unsigned bytes) {
  return __builtin_amdgcn_make_buffer_rsrc((void*)p, 0, bytes, 0x00020000);
}
__device__ __forceinline__ u32x4 tag4(f32x4 v, unsigned tag) {
  u32x4 r;
  r[0] = __builtin_amdgcn_perm(asu(v[0]), tag, 0x07060500u);
  r[1] = __builtin_amdgcn_perm(asu(v[1]), tag, 0x07060501u);
  r[2] = __builtin_amdgcn_perm(asu(v[2]), tag, 0x07060502u);
  r[3] = __builtin_amdgcn_perm(asu(v[3]), tag, 0x07060503u);
  return r;
}
__device__ __forceinline__ unsigned next_tag(unsigned epoch) {
  const unsigned e = __builtin_amdgcn_readfirstlane(epoch);
  return ((e & 0x7f7f7f7fu) + 0x01010101u) ^ (e & 0x80808080u);
}
__device__ __forceinline__ u32x4 tag_bytes(unsigned tag) { return u32x4{tag & 255u, (tag >> 8) & 255u, (tag >> 16) & 255u, tag >> 24}; }
__device__ __forceinline__ unsigned or4(u32x4 v) { return v[0] | v[1] | v[2] | v[3]; }
__device__ __forceinline__ f32x4 asf4(u32x4 v) { return __builtin_bit_cast(f32x4, v); }
__device__ __forceinline__ u32x4 ld_sc1(rsrc_t r, unsigned voff, unsigned soff) {
  return __builtin_amdgcn_raw_buffer_load_b128(r, voff, soff, AUX_SC1);
}
__device__ __forceinline__ void st_sc1(u32x4 v, rsrc_t r, unsigned voff, unsigned soff) {
  __builtin_amdgcn_raw_buffer_store_b128(v, r, voff, soff, AUX_SC1);
}
__device__ __forceinline__ bool wave_none(bool bad) { return __builtin_amdgcn_ballot_w64(bad) == 0ull; }

template <typename T> __device__ __forceinline__ T lds_ld(LAS char* l, int off) { return *(LAS T*)(l + off); }
template <typename T> __device__ __forceinline__ void lds_st(LAS char* l, int off, T v) { *(LAS T*)(l + off) = v; }
__device__ __forceinline__ void lds_flag(LAS char* l, int off, unsigned v) {
  MEM_FENCE();
  *(volatile LAS unsigned*)(l + off) = v;
  MEM_FENCE();
}
__device__ __forceinline__ u32x4 lds_peek(LAS char* l, int off) { return *(volatile LAS u32x4*)(l + off); }
__device__ __forceinline__ void lds_wait_all(LAS char* l, int off) {
  SPIN(SPIN_MAX) { const u32x4 f = lds_peek(l, off); if (wave_none((f[0] & f[1] & f[2] & f[3]) == 0u)) break; NAP(); }
  MEM_FENCE();
}
__device__ __forceinline__ bool unset4(u32x4 v) { return max(max(v[0], v[1]), max(v[2], v[3])) == 0xffffffffu; }

__device__ __forceinline__ void dma16(rsrc_t r, LAS char* dst, unsigned voff, unsigned soff) {
  __builtin_amdgcn_raw_ptr_buffer_load_lds(r, (LAS void*)dst, 16, voff, soff, 0, W_AUX);
}
__device__ __forceinline__ void tile_fetch(rsrc_t r, unsigned voff, unsigned soff, unsigned step, LAS char* img) {
#pragma unroll
  for (int i = 0; i < 4; ++i) dma16(r, img + i * 1024, voff, soff + i * step);
}
template <int GAP> __device__ __forceinline__ bf16x8 tr_frag(LAS char* a) {
  const s16x4 lo = __builtin_amdgcn_ds_read_tr16_b64_v4i16((LAS s16x4*)a);
  const s16x4 hi = __builtin_amdgcn_ds_read_tr16_b64_v4i16((LAS s16x4*)(a + GAP));
  return __builtin_bit_cast(bf16x8, __builtin_shufflevector(lo, hi, 0, 1, 2, 3, 4, 5, 6, 7));
}
__device__ __forceinline__ void tile_frags(LAS char* img, int lane, bf16x8 (&w)[4]) {
  const int row = lane >> 2, p = lane & 3;
#pragma unroll
  for (int t = 0; t < 4; ++t) w[t] = tr_frag<2048>(img + row * 128 + (((2 * t + (p >> 1)) ^ (row & 6)) << 4) + 8 * (p & 1));
}
__device__ __forceinline__ f32x4 mma(bf16x8 a, bf16x8 b, f32x4 c) { return __builtin_amdgcn_mfma_f32_16x16x32_bf16(a, b, c, 0, 0, 0); }
__device__ __forceinline__ bf16x8 asb(u32x4 v) { return __builtin_bit_cast(bf16x8, v); }

#define STAMP(k)
#define STAMP_OUT()

__global__ void __launch_bounds__(512, 2)
fused_decode(const u16* x, const u16* kc, const u16* vc, const u16* wq, const u16* wo, const u16* wg, const u16* wu,
             const u16* wd, u16* out, char* ws) {
  __shared__ __attribute__((aligned(16))) char lds_raw[L_END];
  LAS char* lds = (LAS char*)lds_raw;
  STAMP(0);
  const int bid = blockIdx.x, lane = threadIdx.x & 63, wave = __builtin_amdgcn_readfirstlane(threadIdx.x >> 6);
  const int hk = bid & 7, idx = bid >> 3;
  const int w4 = wave & 3;
  const bool loader = wave >= 4;
  unsigned cpf = 0;
  if (CP) cpf = __builtin_amdgcn_raw_buffer_load_b32(make_rsrc(__builtin_amdgcn_s_getpc() & ~127ull, CP), 0, (idx * 4 + w4) * 128, 0);
  const bool is_q = idx < 16, is_gate = idx < 23;
  const int half = hk & 1;
  int et_q = idx + 16 * ((idx + (hk >> 1)) & 3), et_f = (idx - (is_gate ? 16 : 23)) * 4 + (hk >> 1);
  asm("" : "+s"(et_q), "+s"(et_f));
  const int etile = is_q ? et_q : et_f;
  const int r8 = lane >> 3;
  const unsigned swz = ((lane & 7) ^ (r8 & 6)) << 4;
  LAS char* img = lds + L_TILE + w4 * 4096;
  unsigned stride = is_q ? QW * 2 : FF * 2;
  unsigned evo = r8 * stride + swz, eso = (half * 224 + w4 * 32) * stride + etile * 128, l16 = lane * 16;
  unsigned kvo = (lane & 7) * 512 + ((lane >> 3) & 1) * 64 + (lane >> 4) * 16;
  unsigned kv_off = (((idx >> 3) * 8 + hk) * SK + (SK - WIN) + (idx & 7) * 256) * 512, rot = idx + 3 * hk + w4;
  unsigned ebytes = idx < 30 ? DM * stride : 0;
  { const unsigned iv = lane < 12 ? 0u : 0xffffffffu; lds_st<u32x2>(lds, L_SYNC + lane * 8, u32x2{iv, iv}); }
  const u64 p_x = (u64)x, p_k = (u64)kc, p_v = (u64)vc, p_wq = (u64)wq, p_wo = (u64)wo, p_wg = (u64)wg, p_wu = (u64)wu, p_wd = (u64)wd;
  const u64 p_out = (u64)out, p_ws = (u64)ws;
  asm volatile("" : "+v"(evo), "+v"(l16), "+v"(kvo));
  asm volatile("s_waitcnt lgkmcnt(0)"
               : "+s"(eso), "+s"(stride), "+s"(kv_off), "+s"(rot), "+s"(ebytes)
               : "s"(p_x), "s"(p_k), "s"(p_v), "s"(p_wq), "s"(p_wo), "s"(p_wg), "s"(p_wu), "s"(p_wd), "s"(p_out), "s"(p_ws) : "memory");
  const rsrc_t rws = make_rsrc(p_ws, WS_END);
  u32x4 xr;
  unsigned epoch, tag, xv = 0;
  if (loader) {
    __builtin_amdgcn_s_setprio(2);
    xr = __builtin_amdgcn_raw_buffer_load_b128(make_rsrc(p_x, NB * DM * 2), l16, w4 * DM * 2, 0);
    epoch = __builtin_amdgcn_raw_buffer_load_b32(rws, 0, WS_EPOCH, 0);
    SCHED_FENCE();
    const rsrc_t rw = make_rsrc(is_q ? p_wq : (is_gate ? p_wg : p_wu), ebytes);
    tile_fetch(rw, evo, eso, 8 * stride, img);
    tile_fetch(rw, evo, w4 < 3 ? eso + 128 * stride : 0x40000000u, 8 * stride, w4 < 3 ? img + 4 * 4096 : lds + L_V + 8192);
    if (HOLD == 2) VM_WAIT(8);
  }
  WG_BARRIER_BARE();
  STAMP(1);
  const int j = idx & 7, b = idx >> 3;
  const bool is_wo = idx >= 16, late = idx >= 9, is_red = idx < 7;
  constexpr unsigned SPIN_TAIL = (ABL & 1) ? 1 : SPIN_MAX;
#define wo_un ((XMAP ? (hk + 1 + (idx & 3)) & 7 : hk) * 16 + (idx - 16))
#define dn_un (bid - 72)
#define red bid
#define oi ((lane >> 4) * DM + bid * 8 + (lane & 7))

  if (__builtin_expect(loader, 0)) {
    const bool has_early = idx < 30;
    {
      float v[8], s1 = 0.f, s2 = 0.f;
      const float x0 = bf_lo(__builtin_amdgcn_readfirstlane(xr[0]));
#pragma unroll
      for (int k = 0; k < 4; ++k) { v[2 * k] = bf_lo(xr[k]); v[2 * k + 1] = bf_hi(xr[k]); }
#pragma unroll
      for (int k = 0; k < 8; ++k) { const float d = v[k] - x0; s1 += d; s2 = fmaf(d, d, s2); }
      float z = fold32(lane < 56 ? s1 : 0.f, lane < 56 ? s2 : 0.f);
      z = row_sum(fold16(z, z)) * (1.0f / DM);
      const float md = asf(__builtin_amdgcn_readlane(asu(z), 0)), m2 = asf(__builtin_amdgcn_readlane(asu(z), 32));
      const float var = round_bf16(fmaxf(fmaf(-md, md, m2), 0.f)), mb = round_bf16(x0 + md);
      const float rc = __builtin_amdgcn_rcpf(round_bf16(__builtin_amdgcn_sqrtf(round_bf16(var + 1e-5f))));
      u32x4 xn;
#pragma unroll
      for (int k = 0; k < 4; ++k) {
        const unsigned d = pack_bf16(v[2 * k] - mb, v[2 * k + 1] - mb);
        xn[k] = pack_bf16(bf_lo(d) * rc, bf_hi(d) * rc);
      }
      lds_st<u32x4>(lds, L_XN + w4 * 1024 + lane * 16, xn);
      lds_flag(lds, F_XN + w4 * 4, 1u);
      lds_wait_all(lds, F_XN);
      tag = next_tag(epoch);
      asm volatile("" : "+s"(tag) : "v"(cpf));
      const int bt = lane & 3, g = lane >> 4, jj = (lane >> 2) & 3;
      const int xo = L_XN + bt * 1024 + (half * 224 + w4 * 32 + 4 * g) * 2;
      const u32x2 a0 = lds_ld<u32x2>(lds, xo), a1 = lds_ld<u32x2>(lds, xo + 32);
      const u32x2 a2 = lds_ld<u32x2>(lds, xo + 256), a3 = lds_ld<u32x2>(lds, xo + 288);
      const u32x4 act0 = {a0[0], a0[1], a1[0], a1[1]}, act1 = {a2[0], a2[1], a3[0], a3[1]}, zero = {0u, 0u, 0u, 0u};
      VM_WAIT(0);
      bf16x8 wf0[4], wf1[4];
      tile_frags(img, lane, wf0);
      f32x4 acc = {0.f, 0.f, 0.f, 0.f};
      if (w4 < 3) {
        tile_frags(img + 4 * 4096, lane, wf1);
#pragma unroll
        for (int t = 0; t < 4; ++t) acc = mma(wf1[t], asb(jj == t ? act1 : zero), acc);
      }
#pragma unroll
      for (int t = 0; t < 4; ++t) acc = mma(wf0[t], asb(jj == t ? act0 : zero), acc);
      lds_st<f32x4>(img, bt * 256 + ((lane & 12) + g) * 16, acc);
      lds_flag(lds, F_EP + w4 * 4, 1u);
      if (w4 == 3 && has_early) {
        const int pb = lane >> 4, cq = lane & 15;
        const unsigned voff = is_q ? pb * 8192 + cq * 16 + WS_QP + (etile >> 3) * 32768 + half * 4096 + (etile & 7) * 512
                                   : (cq >> 3) * 4096 + pb * 256 + (cq & 7) * 16 + WS_GP + etile * 8192 + (is_gate ? 0 : 2048) + half * 1024;
        lds_wait_all(lds, F_EP);
        f32x4 sum = lds_ld<f32x4>(lds, L_TILE + lane * 16);
#pragma unroll
        for (int p = 1; p < 4; ++p) sum += lds_ld<f32x4>(lds, L_TILE + p * 4096 + lane * 16);
        st_sc1(u32x4{asu(sum[0]), tag, asu(sum[1]), tag}, rws, voff, 0);
        st_sc1(u32x4{asu(sum[2]), tag, asu(sum[3]), tag}, rws, voff, is_q ? 256 : 128);
      }
    }
    STAMP(2);
    if (!PRIO) __builtin_amdgcn_s_setprio(0);
    const rsrc_t rv = make_rsrc(p_v + kv_off, 131072);
    const unsigned hf = lane >> 5;
    unsigned vo0 = (lane ^ hf) * 16, vo1 = (lane ^ (2 + hf)) * 16, vo2 = (lane ^ (4 + hf)) * 16, vo3 = (lane ^ (6 + hf)) * 16;
    PIN_V(vo0); PIN_V(vo1); PIN_V(vo2); PIN_V(vo3);
    WG_BARRIER();
    STAMP(3);
    constexpr int LAG = (WN + 3) / 4;
    _Pragma("nounroll") for (int g = 0; g < 8; ++g) {
      const int grp = g * 4 + w4;
      const unsigned so = ((g * 4 + rot) & 31) << 12;
      LAS void* dst = (LAS void*)(lds + L_V + grp * 4096);
      VM_WAIT(WN - 1); __builtin_amdgcn_raw_ptr_buffer_load_lds(rv, dst, 16, vo0, so, 0, KV_AUX);
      VM_WAIT(WN - 1); __builtin_amdgcn_raw_ptr_buffer_load_lds(rv, dst, 16, vo1, so, 1024, KV_AUX);
      VM_WAIT(WN - 1); __builtin_amdgcn_raw_ptr_buffer_load_lds(rv, dst, 16, vo2, so, 2048, KV_AUX);
      VM_WAIT(WN - 1); __builtin_amdgcn_raw_ptr_buffer_load_lds(rv, dst, 16, vo3, so, 3072, KV_AUX);
      lds_flag(lds, L_PROG + w4 * 4, g + 1);
    }
    STAMP(4);
    const rsrc_t rl = make_rsrc(is_wo ? p_wo : p_wd, (is_wo ? QW : FF) * DM * 2);
    const int row0 = is_wo ? (wo_un >> 3) * 256 + (wo_un & 6) * 32 + (wo_un & 1) * 4 : dn_un * 32;
    const unsigned lvo = (is_wo ? (r8 & 4) * 2 + (r8 & 3) : r8) * (DM * 2) + swz;
    const unsigned lso = row0 * (DM * 2) + w4 * 128, lstep = (is_wo ? 16 : 8) * DM * 2;
    VM_WAIT(8);
    lds_flag(lds, L_PROG + w4 * 4, 6 + LAG);
    if (LATE == 1 && late) {
      tile_fetch(rl, lvo, lso, lstep, img);
      VM_WAIT(8);
      lds_flag(lds, L_PROG + w4 * 4, 7 + LAG);
      if (w4 < 3) { tile_fetch(rl, lvo, lso + 512, lstep, img + 4 * 4096); VM_WAIT(8); } else VM_WAIT(4);
    } else {
      VM_WAIT(4);
      lds_flag(lds, L_PROG + w4 * 4, 7 + LAG);
      VM_WAIT(0);
    }
    STAMP(5);
    WG_BARRIER_BARE();
    if (late) {
      if (LATE == 2) {
        tile_fetch(rl, lvo, lso, lstep, img);
        if (w4 < 3) tile_fetch(rl, lvo, lso + 512, lstep, img + 4 * 4096);
      }
    }
    STAMP(6);
  } else {
    const rsrc_t rk = make_rsrc(p_k + kv_off, 131072);
    u32x4 kr[32];
#pragma unroll
    for (int g = 0; g < 8; ++g) {
      const unsigned so = ((g * 4 + rot) & 31) << 12;
#pragma unroll
      for (int i = 0; i < 4; ++i) {
        if (WNK < 32) VM_WAIT(WNK - 1);
        kr[g * 4 + i] = __builtin_amdgcn_raw_buffer_load_b128(rk, kvo + i * 128, so, KV_AUX);
      }
    }
    SCHED_FENCE();
    epoch = __builtin_amdgcn_raw_buffer_load_b32(rws, 0, WS_EPOCH, 0);
    const unsigned qso = WS_QP + hk * 32768 + b * 8192 + w4 * 1024;
    u32x4 a0 = {}, a1 = {};
    if (!(ABL & 8)) { a0 = ld_sc1(rws, l16, qso); a1 = ld_sc1(rws, l16, qso + 4096); }
    STAMP(2);
    if (WNK < 32) VM_WAIT(WNK / 2 + 3);
    WG_BARRIER_BARE();
    STAMP(3);
    tag = next_tag(epoch);
    asm volatile("" : "+s"(tag) : "v"(cpf));
    SPIN((ABL & 8) ? 0 : SPIN_MAX) {
      if (wave_none((a0[1] != tag) | (a0[3] != tag) | (a1[1] != tag) | (a1[3] != tag))) break;
      MEM_FENCE();
      a0 = ld_sc1(rws, l16, qso);
      a1 = ld_sc1(rws, l16, qso + 4096);
    }
    {
      const int col = (w4 * 2 + (lane >> 5)) * 64 + (lane & 15) * 4 + ((lane >> 4) & 1) * 2;
      lds_st<unsigned>(lds, L_Q + col * 2, pack_bf16(asf(a0[0]) + asf(a1[0]), asf(a0[2]) + asf(a1[2])));
      lds_flag(lds, F_Q + w4 * 4, 1u);
      lds_wait_all(lds, F_Q);
    }
    STAMP(4);
    float m0, m1;
    {
      const int jj = (lane >> 2) & 3;
      const int qa = L_Q + (lane & 1) * 512 + ((lane >> 1) & 1) * 64 + (lane >> 4) * 16;
      f32x4 c0 = {0.f, 0.f, 0.f, 0.f}, c1 = {0.f, 0.f, 0.f, 0.f};
      u32x4 qf[4];
#pragma unroll
      for (int s = 0; s < 4; ++s) qf[s] = lds_ld<u32x4>(lds, qa + s * 128);
#pragma unroll
      for (int t = 0; t < 4; ++t)
#pragma unroll
        for (int s = 0; s < 4; ++s) {
          const bf16x8 qm = asb(jj == t ? qf[s] : u32x4{0u, 0u, 0u, 0u});
          c0 = mma(qm, asb(kr[t * 4 + s]), c0);
          c1 = mma(qm, asb(kr[16 + t * 4 + s]), c1);
        }
      const bool cb = lane & 8;
      const float r0 = (cb ? c1[2] : c0[0]) + dpp<XOR8>(cb ? c0[2] : c1[0]);
      const float r1 = (cb ? c1[3] : c0[1]) + dpp<XOR8>(cb ? c0[3] : c1[1]);
      const unsigned sp = pack_bf16(r0, r1);
      const float s0 = bf_lo(sp) * 0.0625f, s1 = bf_hi(sp) * 0.0625f;
      float mw = max32(s0, s1);
      mw = row_max(max16(mw, mw));
      if ((lane & 31) == 0) *(volatile LAS float*)(lds + L_MX + (lane >> 5) * 16 + w4 * 4) = mw;
      u32x4 x0, x1;
      SPIN(SPIN_MAX) { x0 = lds_peek(lds, L_MX); x1 = lds_peek(lds, L_MX + 16); if (wave_none(unset4(x0) | unset4(x1))) break; }
      m0 = fmaxf(fmaxf(asf(x0[0]), asf(x0[1])), fmaxf(asf(x0[2]), asf(x0[3])));
      m1 = fmaxf(fmaxf(asf(x1[0]), asf(x1[1])), fmaxf(asf(x1[2]), asf(x1[3])));
      const float p0 = __builtin_amdgcn_exp2f((s0 - m0) * LOG2E), p1 = __builtin_amdgcn_exp2f((s1 - m1) * LOG2E);
      const unsigned pk = pack_bf16(p0, p1);
      const int po = L_P + (((lane >> 3) & 1) * 4 + (lane >> 4)) * 64 + ((w4 & 1) * 2 + ((lane >> 2) & 1)) * 16 + ((w4 >> 1) * 4 + (lane & 3)) * 2;
      lds_st<u16>(lds, po, (u16)pk);
      lds_st<u16>(lds, po + 512, (u16)(pk >> 16));
      float l = fold32(p0, p1);
      l = row_sum(fold16(l, l));
      MEM_FENCE();
      if ((lane & 31) == 0) *(volatile LAS float*)(lds + L_LS + (lane >> 5) * 16 + w4 * 4) = l;
    }
    u32x4 ls;
    SPIN(SPIN_MAX) { ls = lds_peek(lds, L_LS + (lane >> 5) * 16); if (wave_none(unset4(ls))) break; }
    MEM_FENCE();
    STAMP(5);
    if (w4 < 2) {
      const int g = lane >> 4, row = lane >> 2, p = lane & 3, tl = lane & 7;
      LAS char* va[8];
#pragma unroll
      for (int t = 0; t < 8; ++t) va[t] = lds + L_V + row * 512 + ((w4 * 16 + (p >> 1) * 8 + (t ^ (row & 7))) << 4) + 8 * (p & 1);
      LAS char* pa = lds + L_P + ((lane >> 3) & 1) * 512 + g * 16;
      f32x4 acc[8];
#pragma unroll
      for (int t = 0; t < 8; ++t) acc[t] = f32x4{0.f, 0.f, 0.f, 0.f};
      const u32x4 pl = lds_ld<u32x4>(pa, 7 * 64);
      u32x4 pm[8];
#pragma unroll
      for (int t = 0; t < 8; ++t) { pm[t] = tl == t ? pl : u32x4{0u, 0u, 0u, 0u}; PIN_V(pm[t]); }
      unsigned ovo = (((lane >> 3) & 1) * 8 + g) * 4096 + tl * 16, oso = WS_OP + (hk * 16 + w4 * 4) * 4096 + (b * 8 + j) * 128;
      PIN_V(ovo); PIN_S(oso);
      constexpr int LAG = (WN + 3) / 4;
      _Pragma("nounroll") for (int ks = 0; ks < 7; ++ks) {
        SPIN(SPIN_MAX) {
          const u32x4 pr = lds_peek(lds, L_PROG);
          if (wave_none(min(min(pr[0], pr[1]), min(pr[2], pr[3])) <= (unsigned)(ks + LAG))) break;
          NAP();
        }
        MEM_FENCE();
        const bf16x8 pf = asb(lds_ld<u32x4>(pa, 0));
        bf16x8 vf[8];
#pragma unroll
        for (int t = 0; t < 8; ++t) vf[t] = tr_frag<8192>(va[t]);
        SCHED_FENCE();
#pragma unroll
        for (int t = 0; t < 8; ++t) { acc[t] = mma(vf[t], pf, acc[t]); va[t] += 16384; }
        pa += 64;
      }
      f32x4 o = acc[0];
#pragma unroll
      for (int t = 1; t < 8; ++t) o = tl == t ? acc[t] : o;
      PIN_V(o);
      STAMP(6);
      WG_BARRIER_BARE();
      bf16x8 vf[8];
#pragma unroll
      for (int t = 0; t < 8; ++t) vf[t] = tr_frag<8192>(va[t]);
      SCHED_FENCE();
#pragma unroll
      for (int t = 0; t < 8; ++t) o = mma(vf[t], asb(pm[t]), o);
      st_sc1(tag4(o, tag), rws, ovo, oso);
      STAMP(7);
    } else {
      if (w4 == 3) {
        const float lsum = (asf(ls[0]) + asf(ls[1])) + (asf(ls[2]) + asf(ls[3]));
        if ((lane & 31) == 0)
          st_sc1(tag4(f32x4{lane ? m1 : m0, lsum, 0.f, 0.f}, tag), rws, lane * 16, WS_ML + (hk * 64 + b * 8 + j) * 16);
        if (is_wo) {
          if (PF) {
            const unsigned t0 = __builtin_amdgcn_raw_buffer_load_b32(rws, (lane & 31) * 128, WS_OP + wo_un * 4096, AUX_SC1);
            asm volatile("" :: "v"(t0));
          }
          SPIN(SPIN_MAX) {
            if (wave_none(*(volatile LAS unsigned*)(lds + L_PROG) < (unsigned)(MLAT + (WN + 3) / 4))) break;
            __builtin_amdgcn_s_sleep(4);
          }
          u32x4 mv = ld_sc1(rws, (lane & 31) * 16, WS_ML + (wo_un >> 3) * 512);
          WG_BARRIER_BARE();
        SPIN(SPIN_TAIL) {
          mv ^= tag_bytes(tag);
          if (wave_none((or4(mv) & 255u) != 0u)) break;
          MEM_FENCE();
          mv = ld_sc1(rws, (lane & 31) * 16, WS_ML + (wo_un >> 3) * 512);
        }
        const float m = asf(mv[0]);
        float mx = fmaxf(m, dpp<XOR7>(m));
        mx = fmaxf(mx, dpp<XOR2>(mx));
        mx = fmaxf(mx, dpp<XOR1>(mx));
        const float e = __builtin_amdgcn_exp2f((m - mx) * LOG2E);
        float lt = e * asf(mv[1]);
        lt += dpp<XOR7>(lt);
        lt += dpp<XOR2>(lt);
        lt += dpp<XOR1>(lt);
        *(volatile LAS unsigned*)(lds + L_F + (lane & 31) * 4) = asu(e * __builtin_amdgcn_rcpf(lt)) & 0xfffffffeu;
        }
      } else if (late && !is_wo) {
        u32x4 gv[4];
        SPIN(SPIN_MAX) {
          bool bad = false;
#pragma unroll
          for (int u = 0; u < 4; ++u) gv[u] = ld_sc1(rws, l16 + u * 1024, WS_GP + dn_un * 4096);
#pragma unroll
          for (int u = 0; u < 4; ++u) bad |= (gv[u][1] != tag) | (gv[u][3] != tag);
          if (wave_none(bad)) break;
          MEM_FENCE();
        }
        const unsigned gt = pack_bf16(asf(gv[0][0]) + asf(gv[1][0]), asf(gv[0][2]) + asf(gv[1][2]));
        const unsigned up = pack_bf16(asf(gv[2][0]) + asf(gv[3][0]), asf(gv[2][2]) + asf(gv[3][2]));
        const float g0 = bf_lo(gt), g1 = bf_hi(gt);
        const unsigned hv = pack_bf16(g0 * (g0 > 0.f ? 1.f : 0.f) * bf_lo(up), g1 * (g1 > 0.f ? 1.f : 0.f) * bf_hi(up));
        const int c8 = lane & 7;
        lds_st<unsigned>(lds, L_A + (lane >> 4) * 64 + (c8 & 3) * 16 + ((c8 >> 2) * 4 + ((lane >> 3) & 1) * 2) * 2, hv);
      } else if (is_red) {
        xv = __builtin_amdgcn_raw_buffer_load_b16(make_rsrc(p_x, NB * DM * 2), oi * 2, 0, 0);
        PIN_V(xv);
      }
      STAMP(6);
      if (!(w4 == 3 && is_wo)) WG_BARRIER();
    }
  }

  const u32x4 tb = tag_bytes(tag);
  if (late) {
    const int tile = loader ? w4 : 3 + w4;
    const bool gather = is_wo && !loader;
    u32x4 ov;
    float f;
    unsigned oso = WS_OP + wo_un * 4096 + w4 * 1024;
    if (gather) {
      ov = ld_sc1(rws, l16, oso);
    }
    bf16x8 wf[4];
    LAS char* aa[4];
    unsigned rvo = (lane >> 3) * (184 * 128) + (lane & 7) * 16, rso = WS_RP + (tile * 8 * 184 + (is_wo ? wo_un : 128 + dn_un)) * 128;
    LAS char* ta[4];
    LAS char* ad = lds + L_A + w4 * 64 + (lane & 3) * 16 + ((lane & 4) + (lane >> 4)) * 2;
#pragma unroll
    for (int t = 0; t < 4; ++t) {
      aa[t] = lds + (((lane >> 2) & 3) == t ? L_A + (lane & 3) * 64 + (lane >> 4) * 16 : L_ZERO);
      ta[t] = lds + L_TILE + tile * 4096 + (lane >> 2) * 128 + (((2 * t + ((lane >> 1) & 1)) ^ ((lane >> 2) & 6)) << 4) + 8 * (lane & 1);
      PIN_V(aa[t]); PIN_V(ta[t]);
    }
    PIN_V(rvo); PIN_S(rso); PIN_V(ad);
    if (loader) {
      VM_DRAIN();
      lds_flag(lds, F_T + w4 * 4, 1u);
    } else if (wave != 0) {
      SPIN(SPIN_MAX) { if (wave_none(*(volatile LAS unsigned*)(lds + F_T + (w4 - 1) * 4) == 0u)) break; }
      MEM_FENCE();
    }
    if (wave != 0) {
#pragma unroll
      for (int t = 0; t < 4; ++t) wf[t] = tr_frag<2048>(ta[t]);
      MEM_FENCE();
    }
    STAMP(8);
    if (is_wo) {
      if (gather) {
        SPIN(SPIN_MAX) {
          f = *(volatile LAS float*)(lds + L_F + w4 * 32 + (lane >> 3) * 4);
          if (wave_none(asu(f) == 0xffffffffu)) break;
        }
        SPIN(SPIN_TAIL) {
          ov ^= tb;
          if (wave_none((or4(ov) & 255u) != 0u)) break;
          MEM_FENCE();
          ov = ld_sc1(rws, l16, oso);
        }
        STAMP(9);
        const float o = fold_hi(asf4(ov) * f);
        lds_st<u16>(ad, 0, (u16)pack_bf16(o, o));
      }
      WG_BARRIER();
    }
    STAMP(10);
    if (wave != 0) {
      u32x4 act[4];
#pragma unroll
      for (int t = 0; t < 4; ++t) act[t] = lds_ld<u32x4>(aa[t], 0);
      SCHED_FENCE();
      f32x4 acc = {0.f, 0.f, 0.f, 0.f};
#pragma unroll
      for (int t = 0; t < 4; ++t) acc = mma(asb(act[t]), wf[t], acc);
      st_sc1(tag4(acc, tag), rws, rvo, rso);
    }
    STAMP(11);
  } else if (is_red) {
    LAS char* fa = lds + L_FIN + ((lane >> 4) * 8 + (lane & 7)) * 64;
    LAS char* fw = fa + wave * 4 + (lane & 8) * 4;
    LAS char* fr = fa + (lane & 8) * 2;
    unsigned oo = (lane & 8) ? 0x7fffffffu : oi * 2;
    float xf = bf_lo(xv);
    PIN_V(fw); PIN_V(fr); PIN_V(oo); PIN_V(xf);
    const rsrc_t ro = make_rsrc(p_out, NB * DM * 2);
    const unsigned so = WS_RP + red * 184 * 128, sd = wave < 7 ? so + 16384 + wave * 1024 : so + wave * 2048;
    const float dw = wave < 7 ? 1.f : 0.f;
    u32x4 a0, a1, a2;
    STAMP(8);
    SPIN(SPIN_TAIL) {
      a2 = ld_sc1(rws, l16, sd) ^ tb;
      a0 = ld_sc1(rws, l16, so + wave * 2048) ^ tb;
      a1 = ld_sc1(rws, l16, so + wave * 2048 + 1024) ^ tb;
      if (wave_none(((or4(a0) | or4(a1) | or4(a2)) & 255u) != 0u)) break;
      MEM_FENCE();
    }
    STAMP(9);
    {
      const f32x4 u = asf4(a0) + asf4(a1), d = asf4(a2);
      const float zs = fold16(fold32(u[0], u[2]), fold32(u[1], u[3])), zd = fold16(fold32(d[0], d[2]), fold32(d[1], d[3])) * dw;
      const bool hi = lane & 8;
      lds_st<float>(fw, 0, (hi ? zd : zs) + dpp<XOR8>(hi ? zs : zd));
    }
    WG_BARRIER();
    STAMP(10);
    if (wave == 2) {
      const f32x4 fd = lds_ld<f32x4>(fr, 32), fp = lds_ld<f32x4>(fr, 0);
      float dn = (fd[0] + fd[1]) + (fd[2] + fd[3]);
      dn = round_bf16(dn + dpp<XOR8>(dn));
      const float g = round_bf16(dn * __builtin_amdgcn_rcpf(1.0f + __builtin_amdgcn_exp2f(-dn * LOG2E)));
      float sv = (fp[0] + fp[1]) + (fp[2] + fp[3]);
      sv = round_bf16(sv + dpp<XOR8>(sv));
      const float res = xf + round_bf16(sv * g);
      __builtin_amdgcn_raw_buffer_store_b16((u16)pack_bf16(res, res), ro, oo, 0, 0);
      if (red == 0 && lane == 0) __builtin_amdgcn_raw_buffer_store_b32(tag, rws, 0, WS_EPOCH, AUX_SC1);
      STAMP(11);
    }
  }
  STAMP_OUT();
}

}

extern "C" void kernel_launch(void* const* d_in, const int* in_sizes, int n_in, void* d_out, int out_size, void* d_ws,
                              size_t ws_size, hipStream_t stream) {
  fused_decode<<<256, 512, 0, stream>>>((const u16*)d_in[0], (const u16*)d_in[1], (const u16*)d_in[2], (const u16*)d_in[3],
                                        (const u16*)d_in[4], (const u16*)d_in[6], (const u16*)d_in[7], (const u16*)d_in[8],
                                        (u16*)d_out, (char*)d_ws + WS_OFF);
}
```

```cpp
#include <hip/hip_runtime.h>
#include <hip/hip_bf16.h>
#include <stdint.h>

#define DIAG 0
#define KV_AUX 2
#define W_AUX 2
#define ABL 0
#define WN 12
#define WNK 63
#define HOLD 1
#define LATE 1
#define XMAP 1
#define PF 0
#define CP 0
#define PRIO 0
#define MLAT 8
#define PT 0
#define P0 0
#define DT 54
#define STG 0
#define PLT 30
#define LAG 2
#define RM 0
#define EM 0
#define FAST 0
#define PD 0
#define WS_OFF 0

namespace {

typedef unsigned short u16;
typedef unsigned long long u64;
typedef unsigned u32x2 __attribute__((ext_vector_type(2)));
typedef unsigned u32x4 __attribute__((ext_vector_type(4)));
typedef unsigned u32x16 __attribute__((ext_vector_type(16)));
typedef float f32x2 __attribute__((ext_vector_type(2)));
typedef float f32x4 __attribute__((ext_vector_type(4)));
typedef short s16x4 __attribute__((ext_vector_type(4)));
typedef __bf16 bf16x2 __attribute__((ext_vector_type(2)));
typedef __bf16 bf16x8 __attribute__((ext_vector_type(8)));
#define LAS __attribute__((address_space(3)))
typedef __amdgpu_buffer_rsrc_t rsrc_t;

constexpr int NB = 4, DM = 448, SK = 8192, WIN = 2048, FF = 1792, QW = 4096;
constexpr int AUX_SC1 = 16;
constexpr unsigned SPIN_MAX = 1u << 18;
constexpr float LOG2E = 1.4426950408889634f;

constexpr unsigned WS_EPOCH = 0;
constexpr unsigned WS_QP = 4096;
constexpr unsigned WS_GP = WS_QP + 8 * 4 * 8192;
constexpr unsigned WS_OP = WS_GP + 56 * 4096;
constexpr unsigned WS_ML = WS_OP + 128 * 4096;
constexpr unsigned WS_RP = WS_ML + 16 * 32 * 16;
constexpr unsigned WS_END = WS_RP + 56 * 184 * 128;
constexpr unsigned WS_DIAG = 8u << 20;

constexpr int L_V = 0;
constexpr int L_XN = L_V;
constexpr int L_TILE = 131072;
constexpr int L_FIN = L_TILE;
constexpr int L_Q = L_TILE + 7 * 4096;
constexpr int L_P = L_Q + 1024;
constexpr int L_A = L_P + 1024;
constexpr int L_SYNC = L_A + 256;
constexpr int L_ZERO = L_SYNC, F_XN = L_SYNC + 16, F_EP = L_SYNC + 32, F_Q = L_SYNC + 48, F_T = L_SYNC + 64, L_PROG = L_SYNC + 80;
constexpr int L_MX = L_SYNC + 96;
constexpr int L_LS = L_MX + 32;
constexpr int L_F = L_SYNC + 256;
constexpr int L_END = L_SYNC + 512;

#define SCHED_FENCE() __builtin_amdgcn_sched_barrier(0)
#define MEM_FENCE() asm volatile("" ::: "memory")
#define WG_BARRIER() do { SCHED_FENCE(); asm volatile("s_waitcnt lgkmcnt(0)\n\ts_barrier" ::: "memory"); SCHED_FENCE(); } while (0)
#define WG_BARRIER_BARE() do { SCHED_FENCE(); asm volatile("s_barrier" ::: "memory"); SCHED_FENCE(); } while (0)
#define VM_WAIT(n) do { SCHED_FENCE(); asm volatile("s_waitcnt vmcnt(%0)" :: "i"(n) : "memory"); SCHED_FENCE(); } while (0)
#define VM_DRAIN() do { SCHED_FENCE(); __builtin_amdgcn_s_waitcnt(0x0F70); SCHED_FENCE(); MEM_FENCE(); } while (0)
#define NAP() __builtin_amdgcn_s_sleep(1)
#define PACE(g) do { if (P0) { SCHED_FENCE(); while ((int)((unsigned)__builtin_amdgcn_s_memrealtime() - t_go) < (int)(g)) __builtin_amdgcn_s_sleep(1); SCHED_FENCE(); } } while (0)
#define PIN_V(x) asm volatile("" : "+v"(x))
#define PIN_S(x) asm volatile("" : "+s"(x))
#define SPIN(limit) _Pragma("nounroll") for (unsigned s_ = 0; s_ < (limit); ++s_)

__device__ __forceinline__ float asf(unsigned u) { return __builtin_bit_cast(float, u); }
__device__ __forceinline__ unsigned asu(float f) { return __builtin_bit_cast(unsigned, f); }
__device__ __forceinline__ unsigned pack_bf16(float lo, float hi) {
  f32x2 f = {lo, hi};
  return __builtin_bit_cast(unsigned, __builtin_convertvector(f, bf16x2));
}
__device__ __forceinline__ float round_bf16(float v) { return asf(pack_bf16(0.f, v)); }
__device__ __forceinline__ float bf_lo(unsigned u) { return asf(u << 16); }
__device__ __forceinline__ float bf_hi(unsigned u) { return asf(u & 0xffff0000u); }
template <int CTRL> __device__ __forceinline__ float dpp(float x) {
  return asf((unsigned)__builtin_amdgcn_mov_dpp((int)asu(x), CTRL, 0xf, 0xf, true));
}
constexpr int XOR1 = 0xB1, XOR2 = 0x4E, XOR7 = 0x141, XOR8 = 0x128;
__device__ __forceinline__ float fold16(float a, float b) {
  auto s = __builtin_amdgcn_permlane16_swap(asu(a), asu(b), false, false);
  return asf(s[0]) + asf(s[1]);
}
__device__ __forceinline__ float fold32(float a, float b) {
  auto s = __builtin_amdgcn_permlane32_swap(asu(a), asu(b), false, false);
  return asf(s[0]) + asf(s[1]);
}
__device__ __forceinline__ float max16(float a, float b) {
  auto s = __builtin_amdgcn_permlane16_swap(asu(a), asu(b), false, false);
  return fmaxf(asf(s[0]), asf(s[1]));
}
__device__ __forceinline__ float max32(float a, float b) {
  auto s = __builtin_amdgcn_permlane32_swap(asu(a), asu(b), false, false);
  return fmaxf(asf(s[0]), asf(s[1]));
}
__device__ __forceinline__ float row_sum(float v) {
  v += dpp<XOR8>(v); v += dpp<XOR7>(v); v += dpp<XOR2>(v); v += dpp<XOR1>(v);
  return v;
}
__device__ __forceinline__ float row_max(float v) {
  v = fmaxf(v, dpp<XOR8>(v)); v = fmaxf(v, dpp<XOR7>(v)); v = fmaxf(v, dpp<XOR2>(v)); v = fmaxf(v, dpp<XOR1>(v));
  return v;
}
__device__ __forceinline__ float fold_hi(f32x4 v) {
  const float z = fold16(fold32(v[0], v[2]), fold32(v[1], v[3]));
  return z + dpp<XOR8>(z);
}
__device__ __forceinline__ rsrc_t make_rsrc(u64 p, unsigned bytes) {
  return __builtin_amdgcn_make_buffer_rsrc((void*)p, 0, bytes, 0x00020000);
}
__device__ __forceinline__ u32x4 tag4(f32x4 v, unsigned tag) {
  u32x4 r;
  r[0] = __builtin_amdgcn_perm(asu(v[0]), tag, 0x07060500u);
  r[1] = __builtin_amdgcn_perm(asu(v[1]), tag, 0x07060501u);
  r[2] = __builtin_amdgcn_perm(asu(v[2]), tag, 0x07060502u);
  r[3] = __builtin_amdgcn_perm(asu(v[3]), tag, 0x07060503u);
  return r;
}
__device__ __forceinline__ unsigned next_tag(unsigned epoch) {
  const unsigned e = __builtin_amdgcn_readfirstlane(epoch);
  return ((e & 0x7f7f7f7fu) + 0x01010101u) ^ (e & 0x80808080u);
}
__device__ __forceinline__ u32x4 tag_bytes(unsigned tag) { return u32x4{tag & 255u, (tag >> 8) & 255u, (tag >> 16) & 255u, tag >> 24}; }
__device__ __forceinline__ unsigned or4(u32x4 v) { return v[0] | v[1] | v[2] | v[3]; }
__device__ __forceinline__ f32x4 asf4(u32x4 v) { return __builtin_bit_cast(f32x4, v); }
__device__ __forceinline__ u32x4 ld_sc1(rsrc_t r, unsigned voff, unsigned soff) {
  return __builtin_amdgcn_raw_buffer_load_b128(r, voff, soff, AUX_SC1);
}
__device__ __forceinline__ void st_sc1(u32x4 v, rsrc_t r, unsigned voff, unsigned soff) {
  __builtin_amdgcn_raw_buffer_store_b128(v, r, voff, soff, AUX_SC1);
}
__device__ __forceinline__ bool wave_none(bool bad) { return __builtin_amdgcn_ballot_w64(bad) == 0ull; }

template <typename T> __device__ __forceinline__ T lds_ld(LAS char* l, int off) { return *(LAS T*)(l + off); }
template <typename T> __device__ __forceinline__ void lds_st(LAS char* l, int off, T v) { *(LAS T*)(l + off) = v; }
__device__ __forceinline__ void lds_flag(LAS char* l, int off, unsigned v) {
  MEM_FENCE();
  *(volatile LAS unsigned*)(l + off) = v;
  MEM_FENCE();
}
__device__ __forceinline__ u32x4 lds_peek(LAS char* l, int off) { return *(volatile LAS u32x4*)(l + off); }
__device__ __forceinline__ void lds_wait_all(LAS char* l, int off) {
  SPIN(SPIN_MAX) { const u32x4 f = lds_peek(l, off); if (wave_none((f[0] & f[1] & f[2] & f[3]) == 0u)) break; NAP(); }
  MEM_FENCE();
}
__device__ __forceinline__ bool unset4(u32x4 v) { return max(max(v[0], v[1]), max(v[2], v[3])) == 0xffffffffu; }

__device__ __forceinline__ void dma16(rsrc_t r, LAS char* dst, unsigned voff, unsigned soff) {
  __builtin_amdgcn_raw_ptr_buffer_load_lds(r, (LAS void*)dst, 16, voff, soff, 0, W_AUX);
}
__device__ __forceinline__ void tile_fetch(rsrc_t r, unsigned voff, unsigned soff, unsigned step, LAS char* img) {
#pragma unroll
  for (int i = 0; i < 4; ++i) dma16(r, img + i * 1024, voff, soff + i * step);
}
template <int GAP> __device__ __forceinline__ bf16x8 tr_frag(LAS char* a) {
  const s16x4 lo = __builtin_amdgcn_ds_read_tr16_b64_v4i16((LAS s16x4*)a);
  const s16x4 hi = __builtin_amdgcn_ds_read_tr16_b64_v4i16((LAS s16x4*)(a + GAP));
  return __builtin_bit_cast(bf16x8, __builtin_shufflevector(lo, hi, 0, 1, 2, 3, 4, 5, 6, 7));
}
__device__ __forceinline__ void tile_frags(LAS char* img, int lane, bf16x8 (&w)[4]) {
  const int row = lane >> 2, p = lane & 3;
#pragma unroll
  for (int t = 0; t < 4; ++t) w[t] = tr_frag<2048>(img + row * 128 + (((2 * t + (p >> 1)) ^ (row & 6)) << 4) + 8 * (p & 1));
}
__device__ __forceinline__ f32x4 mma(bf16x8 a, bf16x8 b, f32x4 c) { return __builtin_amdgcn_mfma_f32_16x16x32_bf16(a, b, c, 0, 0, 0); }
__device__ __forceinline__ bf16x8 asb(u32x4 v) { return __builtin_bit_cast(bf16x8, v); }

#define STAMP(k)
#define STAMP_OUT()

__global__ void __launch_bounds__(512, 2)
fused_decode(const u16* x, const u16* kc, const u16* vc, const u16* wq, const u16* wo, const u16* wg, const u16* wu,
             const u16* wd, u16* out, char* ws) {
  __shared__ __attribute__((aligned(16))) char lds_raw[L_END];
  LAS char* lds = (LAS char*)lds_raw;
  STAMP(0);
  const int bid = blockIdx.x, lane = threadIdx.x & 63, wave = __builtin_amdgcn_readfirstlane(threadIdx.x >> 6);
  const int hk = bid & 7, idx = bid >> 3;
  const int w4 = wave & 3;
  const bool loader = wave >= 4;
  unsigned cpf = 0;
  unsigned t_go = 0;
  if (P0) t_go = (unsigned)__builtin_amdgcn_s_memrealtime() + P0 + w4 * STG;
  const bool fast = (hk & 1) == FAST;
  const int fu = EM ? (hk >> 1) * 28 + (fast ? idx - 16 : idx) : (idx - 16) * 8 + hk;
  const bool is_q = idx < 16, is_gate = fu < 56, has_early = idx < (EM ? (fast ? 32 : 28) : 30);
  const int half = (is_q ? hk : fu) & 1;
  int et_q = idx + 16 * ((idx + (hk >> 1)) & 3), et_f = (fu - (is_gate ? 0 : 56)) >> 1;
  asm("" : "+s"(et_q), "+s"(et_f));
  const int etile = is_q ? et_q : et_f;
  const int r8 = lane >> 3;
  const unsigned swz = ((lane & 7) ^ (r8 & 6)) << 4;
  LAS char* img = lds + L_TILE + w4 * 4096;
  unsigned stride = is_q ? QW * 2 : FF * 2;
  unsigned evo = r8 * stride + swz, eso = (half * 224 + w4 * 32) * stride + etile * 128, l16 = lane * 16;
  unsigned kvo = (lane & 7) * 512 + ((lane >> 3) & 1) * 64 + (lane >> 4) * 16;
  unsigned kv_off = (((idx >> 3) * 8 + hk) * SK + (SK - WIN) + (idx & 7) * 256) * 512, rot = idx + 3 * hk + w4;
  unsigned ebytes = has_early ? DM * stride : 0;
  { const unsigned iv = lane < 12 ? 0u : 0xffffffffu; lds_st<u32x2>(lds, L_SYNC + lane * 8, u32x2{iv, iv}); }
  const u64 p_x = (u64)x, p_k = (u64)kc, p_v = (u64)vc, p_wq = (u64)wq, p_wo = (u64)wo, p_wg = (u64)wg, p_wu = (u64)wu, p_wd = (u64)wd;
  const u64 p_out = (u64)out, p_ws = (u64)ws;
  asm volatile("" : "+v"(evo), "+v"(l16), "+v"(kvo));
  asm volatile("s_waitcnt lgkmcnt(0)"
               : "+s"(eso), "+s"(stride), "+s"(kv_off), "+s"(rot), "+s"(ebytes)
               : "s"(p_x), "s"(p_k), "s"(p_v), "s"(p_wq), "s"(p_wo), "s"(p_wg), "s"(p_wu), "s"(p_wd), "s"(p_out), "s"(p_ws) : "memory");
  const rsrc_t rws = make_rsrc(p_ws, WS_END);
  u32x4 xr;
  unsigned epoch, tag, xv = 0;
  if (loader) {
    __builtin_amdgcn_s_setprio(2);
    xr = __builtin_amdgcn_raw_buffer_load_b128(make_rsrc(p_x, NB * DM * 2), l16, w4 * DM * 2, 0);
    epoch = __builtin_amdgcn_raw_buffer_load_b32(rws, 0, WS_EPOCH, 0);
    SCHED_FENCE();
    const rsrc_t rw = make_rsrc(is_q ? p_wq : (is_gate ? p_wg : p_wu), ebytes);
    tile_fetch(rw, evo, eso, 8 * stride, img);
    tile_fetch(rw, evo, w4 < 3 ? eso + 128 * stride : 0x40000000u, 8 * stride, w4 < 3 ? img + 4 * 4096 : lds + L_V + 8192);
    if (HOLD == 2) VM_WAIT(8);
  } else if (CP) {
    cpf = __builtin_amdgcn_raw_buffer_load_b32(make_rsrc(__builtin_amdgcn_s_getpc() & ~127ull, CP), 0, (idx * 4 + w4) * 128, 0);
  }
  WG_BARRIER_BARE();
  STAMP(1);
  const int j = idx & 7, b = idx >> 3;
  const bool is_wo = RM ? fast : idx >= 16, late = RM ? fast || idx >= 18 : idx >= 9, is_red = RM ? !fast && idx < 14 : idx < 7;
  constexpr unsigned SPIN_TAIL = (ABL & 1) ? 1 : SPIN_MAX;
#define wo_un (RM ? ((((hk >> 1) + 1 + ((idx & 3) == 3 ? 0 : idx & 3)) & 3) * 32 + idx) : ((hk + 1 + (idx & 3)) & 7) * 16 + (idx - 16))
#define dn_un (RM ? (idx - 18) * 4 + (hk >> 1) : bid - 72)
#define red (RM ? idx * 4 + (hk >> 1) : bid)
#define oi ((lane >> 4) * DM + red * 8 + (lane & 7))

  if (__builtin_expect(loader, 0)) {
    if (ABL & 32) { tag = next_tag(epoch); asm volatile("" :: "v"(xr)); VM_WAIT(0); } else {
      float v[8], s1 = 0.f, s2 = 0.f;
      const float x0 = bf_lo(__builtin_amdgcn_readfirstlane(xr[0]));
#pragma unroll
      for (int k = 0; k < 4; ++k) { v[2 * k] = bf_lo(xr[k]); v[2 * k + 1] = bf_hi(xr[k]); }
#pragma unroll
      for (int k = 0; k < 8; ++k) { const float d = v[k] - x0; s1 += d; s2 = fmaf(d, d, s2); }
      float z = fold32(lane < 56 ? s1 : 0.f, lane < 56 ? s2 : 0.f);
      z = row_sum(fold16(z, z)) * (1.0f / DM);
      const float md = asf(__builtin_amdgcn_readlane(asu(z), 0)), m2 = asf(__builtin_amdgcn_readlane(asu(z), 32));
      const float var = round_bf16(fmaxf(fmaf(-md, md, m2), 0.f)), mb = round_bf16(x0 + md);
      const float rc = __builtin_amdgcn_rcpf(round_bf16(__builtin_amdgcn_sqrtf(round_bf16(var + 1e-5f))));
      u32x4 xn;
#pragma unroll
      for (int k = 0; k < 4; ++k) {
        const unsigned d = pack_bf16(v[2 * k] - mb, v[2 * k + 1] - mb);
        xn[k] = pack_bf16(bf_lo(d) * rc, bf_hi(d) * rc);
      }
      lds_st<u32x4>(lds, L_XN + w4 * 1024 + lane * 16, xn);
      lds_flag(lds, F_XN + w4 * 4, 1u);
      lds_wait_all(lds, F_XN);
      tag = next_tag(epoch);
      PIN_S(tag);
      const int bt = lane & 3, g = lane >> 4, jj = (lane >> 2) & 3;
      const int xo = L_XN + bt * 1024 + (half * 224 + w4 * 32 + 4 * g) * 2;
      const u32x2 a0 = lds_ld<u32x2>(lds, xo), a1 = lds_ld<u32x2>(lds, xo + 32);
      const u32x2 a2 = lds_ld<u32x2>(lds, xo + 256), a3 = lds_ld<u32x2>(lds, xo + 288);
      const u32x4 act0 = {a0[0], a0[1], a1[0], a1[1]}, act1 = {a2[0], a2[1], a3[0], a3[1]}, zero = {0u, 0u, 0u, 0u};
      VM_WAIT(0);
      bf16x8 wf0[4], wf1[4];
      tile_frags(img, lane, wf0);
      f32x4 acc = {0.f, 0.f, 0.f, 0.f};
      if (w4 < 3) {
        tile_frags(img + 4 * 4096, lane, wf1);
#pragma unroll
        for (int t = 0; t < 4; ++t) acc = mma(wf1[t], asb(jj == t ? act1 : zero), acc);
      }
#pragma unroll
      for (int t = 0; t < 4; ++t) acc = mma(wf0[t], asb(jj == t ? act0 : zero), acc);
      lds_st<f32x4>(img, bt * 256 + ((lane & 12) + g) * 16, acc);
      lds_flag(lds, F_EP + w4 * 4, 1u);
      if (w4 == 3 && has_early && !(ABL & 64)) {
        const int pb = lane >> 4, cq = lane & 15;
        const unsigned voff = is_q ? pb * 8192 + cq * 16 + WS_QP + (etile >> 3) * 32768 + half * 4096 + (etile & 7) * 512
                                   : (cq >> 3) * 4096 + pb * 256 + (cq & 7) * 16 + WS_GP + etile * 8192 + (is_gate ? 0 : 2048) + half * 1024;
        lds_wait_all(lds, F_EP);
        f32x4 sum = lds_ld<f32x4>(lds, L_TILE + lane * 16);
#pragma unroll
        for (int p = 1; p < 4; ++p) sum += lds_ld<f32x4>(lds, L_TILE + p * 4096 + lane * 16);
        st_sc1(u32x4{asu(sum[0]), tag, asu(sum[1]), tag}, rws, voff, 0);
        st_sc1(u32x4{asu(sum[2]), tag, asu(sum[3]), tag}, rws, voff, is_q ? 256 : 128);
      }
    }
    if ((PT & 2) && w4 == 0) {
      const unsigned t0 = __builtin_amdgcn_raw_buffer_load_b32(make_rsrc(p_v + kv_off, 4), 0, 0, KV_AUX);
      const unsigned t1 = __builtin_amdgcn_raw_buffer_load_b32(make_rsrc(is_wo ? p_wo + (wo_un >> 3) * 256 * DM * 2 : p_wd + dn_un * 32 * DM * 2, late ? 4 : 0), 0, 0, KV_AUX);
      asm volatile("" :: "v"(t0), "v"(t1));
    }
    STAMP(2);
    if (!PRIO) __builtin_amdgcn_s_setprio(0);
    const rsrc_t rv = make_rsrc(p_v + kv_off, 131072);
    const unsigned hf = lane >> 5;
    unsigned vo0 = (lane ^ hf) * 16, vo1 = (lane ^ (2 + hf)) * 16, vo2 = (lane ^ (4 + hf)) * 16, vo3 = (lane ^ (6 + hf)) * 16;
    PIN_V(vo0); PIN_V(vo1); PIN_V(vo2); PIN_V(vo3);
    WG_BARRIER();
    STAMP(3);
    _Pragma("nounroll") for (int g = 0; g < 8; ++g) {
      const int grp = g * 4 + w4;
      PACE((8 + g) * DT);
      const unsigned so = ((g * 4 + rot) & 31) << 12;
      LAS void* dst = (LAS void*)(lds + L_V + grp * 4096);
      __builtin_amdgcn_raw_ptr_buffer_load_lds(rv, dst, 16, vo0, so, 0, KV_AUX);
      __builtin_amdgcn_raw_ptr_buffer_load_lds(rv, dst, 16, vo1, so, 1024, KV_AUX);
      __builtin_amdgcn_raw_ptr_buffer_load_lds(rv, dst, 16, vo2, so, 2048, KV_AUX);
      __builtin_amdgcn_raw_ptr_buffer_load_lds(rv, dst, 16, vo3, so, 3072, KV_AUX);
      VM_WAIT(4 * LAG);
      lds_flag(lds, L_PROG + w4 * 4, g + 1);
    }
    STAMP(4);
    const rsrc_t rl = make_rsrc(is_wo ? p_wo : p_wd, (is_wo ? QW : FF) * DM * 2);
    const int row0 = is_wo ? (wo_un >> 3) * 256 + (wo_un & 6) * 32 + (wo_un & 1) * 4 : dn_un * 32;
    const unsigned lvo = (is_wo ? (r8 & 4) * 2 + (r8 & 3) : r8) * (DM * 2) + swz;
    const unsigned lso = row0 * (DM * 2) + w4 * 128, lstep = (is_wo ? 16 : 8) * DM * 2;
    if (LAG > 2) { VM_WAIT(8); lds_flag(lds, L_PROG + w4 * 4, 6 + LAG); }
    if (LATE == 1 && late) {
      tile_fetch(rl, lvo, lso, lstep, img);
      VM_WAIT(8);
      lds_flag(lds, L_PROG + w4 * 4, 7 + LAG);
      if (w4 < 3) { tile_fetch(rl, lvo, lso + 512, lstep, img + 4 * 4096); VM_WAIT(8); } else VM_WAIT(4);
    } else {
      VM_WAIT(4);
      lds_flag(lds, L_PROG + w4 * 4, 7 + LAG);
      if (LATE == 3 && late) {
        PACE(15 * DT + PLT);
        tile_fetch(rl, lvo, lso, lstep, img);
        if (w4 < 3) { tile_fetch(rl, lvo, lso + 512, lstep, img + 4 * 4096); VM_WAIT(8); } else VM_WAIT(4);
      } else VM_WAIT(0);
    }
    STAMP(5);
    WG_BARRIER_BARE();
    if (late) {
      if (LATE == 2) {
        tile_fetch(rl, lvo, lso, lstep, img);
        if (w4 < 3) tile_fetch(rl, lvo, lso + 512, lstep, img + 4 * 4096);
      }
    }
    STAMP(6);
  } else {
    const rsrc_t rk = make_rsrc(p_k + kv_off, 131072);
    u32x4 kr[32];
#pragma unroll
    for (int g = 0; g < 8; ++g) {
      const unsigned so = ((g * 4 + rot) & 31) << 12;
      PACE(g * DT);
#pragma unroll
      for (int i = 0; i < 4; ++i) {
        if (WNK < 32) VM_WAIT(WNK - 1);
        kr[g * 4 + i] = __builtin_amdgcn_raw_buffer_load_b128(rk, kvo + i * 128, so, KV_AUX);
      }
    }
    SCHED_FENCE();
    epoch = __builtin_amdgcn_raw_buffer_load_b32(rws, 0, WS_EPOCH, 0);
    const unsigned qso = WS_QP + hk * 32768 + b * 8192 + w4 * 1024;
    u32x4 a0 = {}, a1 = {};
    if (!(ABL & 8)) { a0 = ld_sc1(rws, l16, qso); a1 = ld_sc1(rws, l16, qso + 4096); }
    STAMP(2);
    if (WNK < 32) VM_WAIT(WNK / 2 + 3);
    WG_BARRIER_BARE();
    STAMP(3);
    tag = next_tag(epoch);
    asm volatile("" : "+s"(tag) : "v"(cpf));
    if (ABL & 512) {
#pragma unroll
      for (int i = 0; i < 32; ++i) asm volatile("" :: "v"(kr[i]));
      WG_BARRIER_BARE();
      STAMP_OUT();
      return;
    }
    SPIN((ABL & 8) ? 0 : SPIN_MAX) {
      if (wave_none((a0[1] != tag) | (a0[3] != tag) | (a1[1] != tag) | (a1[3] != tag))) break;
      MEM_FENCE();
      a0 = ld_sc1(rws, l16, qso);
      a1 = ld_sc1(rws, l16, qso + 4096);
    }
    {
      const int col = (w4 * 2 + (lane >> 5)) * 64 + (lane & 15) * 4 + ((lane >> 4) & 1) * 2;
      lds_st<unsigned>(lds, L_Q + col * 2, pack_bf16(asf(a0[0]) + asf(a1[0]), asf(a0[2]) + asf(a1[2])));
      lds_flag(lds, F_Q + w4 * 4, 1u);
      lds_wait_all(lds, F_Q);
    }
    STAMP(4);
    float m0, m1;
    {
      const int jj = (lane >> 2) & 3;
      const int qa = L_Q + (lane & 1) * 512 + ((lane >> 1) & 1) * 64 + (lane >> 4) * 16;
      f32x4 c0 = {0.f, 0.f, 0.f, 0.f}, c1 = {0.f, 0.f, 0.f, 0.f};
      u32x4 qf[4];
#pragma unroll
      for (int s = 0; s < 4; ++s) qf[s] = lds_ld<u32x4>(lds, qa + s * 128);
#pragma unroll
      for (int t = 0; t < 4; ++t)
#pragma unroll
        for (int s = 0; s < 4; ++s) {
          const bf16x8 qm = asb(jj == t ? qf[s] : u32x4{0u, 0u, 0u, 0u});
          c0 = mma(qm, asb(kr[t * 4 + s]), c0);
          c1 = mma(qm, asb(kr[16 + t * 4 + s]), c1);
        }
      const bool cb = lane & 8;
      const float r0 = (cb ? c1[2] : c0[0]) + dpp<XOR8>(cb ? c0[2] : c1[0]);
      const float r1 = (cb ? c1[3] : c0[1]) + dpp<XOR8>(cb ? c0[3] : c1[1]);
      const unsigned sp = pack_bf16(r0, r1);
      const float s0 = bf_lo(sp) * 0.0625f, s1 = bf_hi(sp) * 0.0625f;
      float mw = max32(s0, s1);
      mw = row_max(max16(mw, mw));
      if ((lane & 31) == 0) *(volatile LAS float*)(lds + L_MX + (lane >> 5) * 16 + w4 * 4) = mw;
      u32x4 x0, x1;
      SPIN(SPIN_MAX) { x0 = lds_peek(lds, L_MX); x1 = lds_peek(lds, L_MX + 16); if (wave_none(unset4(x0) | unset4(x1))) break; }
      m0 = fmaxf(fmaxf(asf(x0[0]), asf(x0[1])), fmaxf(asf(x0[2]), asf(x0[3])));
      m1 = fmaxf(fmaxf(asf(x1[0]), asf(x1[1])), fmaxf(asf(x1[2]), asf(x1[3])));
      const float p0 = __builtin_amdgcn_exp2f((s0 - m0) * LOG2E), p1 = __builtin_amdgcn_exp2f((s1 - m1) * LOG2E);
      const unsigned pk = pack_bf16(p0, p1);
      const int po = L_P + (((lane >> 3) & 1) * 4 + (lane >> 4)) * 64 + ((w4 & 1) * 2 + ((lane >> 2) & 1)) * 16 + ((w4 >> 1) * 4 + (lane & 3)) * 2;
      lds_st<u16>(lds, po, (u16)pk);
      lds_st<u16>(lds, po + 512, (u16)(pk >> 16));
      float l = fold32(p0, p1);
      l = row_sum(fold16(l, l));
      MEM_FENCE();
      if ((lane & 31) == 0) *(volatile LAS float*)(lds + L_LS + (lane >> 5) * 16 + w4 * 4) = l;
    }
    u32x4 ls;
    SPIN(SPIN_MAX) { ls = lds_peek(lds, L_LS + (lane >> 5) * 16); if (wave_none(unset4(ls))) break; }
    MEM_FENCE();
    STAMP(5);
    if (ABL & 128) { WG_BARRIER_BARE(); STAMP_OUT(); return; }
    if ((ABL & 256) && w4 >= 2) { WG_BARRIER_BARE(); STAMP_OUT(); return; }
    if (w4 < 2) {
      const int g = lane >> 4, row = lane >> 2, p = lane & 3, tl = lane & 7;
      LAS char* va[8];
#pragma unroll
      for (int t = 0; t < 8; ++t) va[t] = lds + L_V + row * 512 + ((w4 * 16 + (p >> 1) * 8 + (t ^ (row & 7))) << 4) + 8 * (p & 1);
      LAS char* pa = lds + L_P + ((lane >> 3) & 1) * 512 + g * 16;
      f32x4 acc[8];
#pragma unroll
      for (int t = 0; t < 8; ++t) acc[t] = f32x4{0.f, 0.f, 0.f, 0.f};
      const u32x4 pl = lds_ld<u32x4>(pa, 7 * 64);
      u32x4 pm[8];
#pragma unroll
      for (int t = 0; t < 8; ++t) { pm[t] = tl == t ? pl : u32x4{0u, 0u, 0u, 0u}; PIN_V(pm[t]); }
      unsigned ovo = (((lane >> 3) & 1) * 8 + g) * 4096 + tl * 16, oso = WS_OP + (hk * 16 + w4 * 4) * 4096 + (b * 8 + j) * 128;
      PIN_V(ovo); PIN_S(oso);
      _Pragma("nounroll") for (int ks = 0; ks < 7; ++ks) {
        SPIN(SPIN_MAX) {
          const u32x4 pr = lds_peek(lds, L_PROG);
          if (wave_none(min(min(pr[0], pr[1]), min(pr[2], pr[3])) <= (unsigned)(ks + LAG))) break;
          NAP();
        }
        MEM_FENCE();
        const bf16x8 pf = asb(lds_ld<u32x4>(pa, 0));
        bf16x8 vf[8];
#pragma unroll
        for (int t = 0; t < 8; ++t) vf[t] = tr_frag<8192>(va[t]);
        SCHED_FENCE();
#pragma unroll
        for (int t = 0; t < 8; ++t) { acc[t] = mma(vf[t], pf, acc[t]); va[t] += 16384; }
        pa += 64;
      }
      f32x4 o = acc[0];
#pragma unroll
      for (int t = 1; t < 8; ++t) o = tl == t ? acc[t] : o;
      PIN_V(o);
      STAMP(6);
      WG_BARRIER_BARE();
      bf16x8 vf[8];
#pragma unroll
      for (int t = 0; t < 8; ++t) vf[t] = tr_frag<8192>(va[t]);
      SCHED_FENCE();
#pragma unroll
      for (int t = 0; t < 8; ++t) o = mma(vf[t], asb(pm[t]), o);
      st_sc1(tag4(o, tag), rws, ovo, oso);
      STAMP(7);
    } else {
      if ((PT & 1) && w4 == 2) {
        const unsigned t0 = __builtin_amdgcn_raw_buffer_load_b32(make_rsrc(p_ws, WS_END + 4096), 0, WS_END, 0);
        const unsigned t1 = __builtin_amdgcn_raw_buffer_load_b32(make_rsrc(p_out, is_red ? 4 : 0), 0, 0, 0);
        asm volatile("" :: "v"(t0), "v"(t1));
      }
      if (w4 == 3) {
        const float lsum = (asf(ls[0]) + asf(ls[1])) + (asf(ls[2]) + asf(ls[3]));
        const float l1 = asf(__builtin_amdgcn_readlane(asu(lsum), 32));
        if (lane == 0) st_sc1(tag4(f32x4{m0, lsum, m1, l1}, tag), rws, 0, WS_ML + (hk * 32 + idx) * 16);
        if (is_wo) {
          if (PF) {
            const unsigned t0 = __builtin_amdgcn_raw_buffer_load_b32(rws, (lane & 31) * 128, WS_OP + wo_un * 4096, AUX_SC1);
            asm volatile("" :: "v"(t0));
          }
          SPIN(SPIN_MAX) {
            if (wave_none(*(volatile LAS unsigned*)(lds + L_PROG) < (unsigned)MLAT)) break;
            __builtin_amdgcn_s_sleep(4);
          }
          u32x4 mv = ld_sc1(rws, (lane & 31) * 16, WS_ML + (wo_un >> 4) * 512);
          WG_BARRIER_BARE();
        SPIN(SPIN_TAIL) {
          mv ^= tag_bytes(tag);
          if (wave_none((or4(mv) & 255u) != 0u)) break;
          MEM_FENCE();
          mv = ld_sc1(rws, (lane & 31) * 16, WS_ML + (wo_un >> 4) * 512);
        }
        const float m = asf((idx & 8) ? mv[2] : mv[0]);
        float mx = fmaxf(m, dpp<XOR7>(m));
        mx = fmaxf(mx, dpp<XOR2>(mx));
        mx = fmaxf(mx, dpp<XOR1>(mx));
        const float e = __builtin_amdgcn_exp2f((m - mx) * LOG2E);
        float lt = e * asf((idx & 8) ? mv[3] : mv[1]);
        lt += dpp<XOR7>(lt);
        lt += dpp<XOR2>(lt);
        lt += dpp<XOR1>(lt);
        *(volatile LAS unsigned*)(lds + L_F + (lane & 31) * 4) = asu(e * __builtin_amdgcn_rcpf(lt)) & 0xfffffffeu;
        }
      } else if (late && !is_wo) {
        u32x4 gv[4];
        SPIN(SPIN_MAX) {
          bool bad = false;
#pragma unroll
          for (int u = 0; u < 4; ++u) gv[u] = ld_sc1(rws, l16 + u * 1024, WS_GP + dn_un * 4096);
#pragma unroll
          for (int u = 0; u < 4; ++u) bad |= (gv[u][1] != tag) | (gv[u][3] != tag);
          if (wave_none(bad)) break;
          MEM_FENCE();
        }
        const unsigned gt = pack_bf16(asf(gv[0][0]) + asf(gv[1][0]), asf(gv[0][2]) + asf(gv[1][2]));
        const unsigned up = pack_bf16(asf(gv[2][0]) + asf(gv[3][0]), asf(gv[2][2]) + asf(gv[3][2]));
        const float g0 = bf_lo(gt), g1 = bf_hi(gt);
        const unsigned hv = pack_bf16(g0 * (g0 > 0.f ? 1.f : 0.f) * bf_lo(up), g1 * (g1 > 0.f ? 1.f : 0.f) * bf_hi(up));
        const int c8 = lane & 7;
        lds_st<unsigned>(lds, L_A + (lane >> 4) * 64 + (c8 & 3) * 16 + ((c8 >> 2) * 4 + ((lane >> 3) & 1) * 2) * 2, hv);
      } else if (is_red) {
        xv = __builtin_amdgcn_raw_buffer_load_b16(make_rsrc(p_x, NB * DM * 2), oi * 2, 0, 0);
        PIN_V(xv);
      }
      STAMP(6);
      if (!(w4 == 3 && is_wo)) WG_BARRIER();
    }
  }

  if (ABL & 16) { VM_WAIT(0); STAMP(8); STAMP_OUT(); return; }
  const u32x4 tb = tag_bytes(tag);
  if (late) {
    const int tile = loader ? w4 : 3 + w4;
    const bool gather = is_wo && !loader;
    u32x4 ov;
    float f;
    unsigned oso = WS_OP + wo_un * 4096 + w4 * 1024;
    if (gather) {
      if (PD) __builtin_amdgcn_s_sleep(PD);
      ov = ld_sc1(rws, l16, oso);
    }
    bf16x8 wf[4];
    LAS char* aa[4];
    unsigned rvo = (lane >> 3) * (184 * 128) + (lane & 7) * 16, rso = WS_RP + (tile * 8 * 184 + (is_wo ? wo_un : 128 + dn_un)) * 128;
    LAS char* ta[4];
    LAS char* ad = lds + L_A + w4 * 64 + (lane & 3) * 16 + ((lane & 4) + (lane >> 4)) * 2;
#pragma unroll
    for (int t = 0; t < 4; ++t) {
      aa[t] = lds + (((lane >> 2) & 3) == t ? L_A + (lane & 3) * 64 + (lane >> 4) * 16 : L_ZERO);
      ta[t] = lds + L_TILE + tile * 4096 + (lane >> 2) * 128 + (((2 * t + ((lane >> 1) & 1)) ^ ((lane >> 2) & 6)) << 4) + 8 * (lane & 1);
      PIN_V(aa[t]); PIN_V(ta[t]);
    }
    PIN_V(rvo); PIN_S(rso); PIN_V(ad);
    if (loader) {
      VM_DRAIN();
      lds_flag(lds, F_T + w4 * 4, 1u);
    } else if (wave != 0) {
      SPIN(SPIN_MAX) { if (wave_none(*(volatile LAS unsigned*)(lds + F_T + (w4 - 1) * 4) == 0u)) break; }
      MEM_FENCE();
    }
    if (wave != 0) {
#pragma unroll
      for (int t = 0; t < 4; ++t) wf[t] = tr_frag<2048>(ta[t]);
      MEM_FENCE();
    }
    STAMP(8);
    if (is_wo) {
      if (gather) {
        SPIN(SPIN_MAX) {
          f = *(volatile LAS float*)(lds + L_F + w4 * 32 + (lane >> 3) * 4);
          if (wave_none(asu(f) == 0xffffffffu)) break;
        }
        SPIN(SPIN_TAIL) {
          ov ^= tb;
          if (wave_none((or4(ov) & 255u) != 0u)) break;
          MEM_FENCE();
          ov = ld_sc1(rws, l16, oso);
        }
        STAMP(9);
        const float o = fold_hi(asf4(ov) * f);
        lds_st<u16>(ad, 0, (u16)pack_bf16(o, o));
      }
      WG_BARRIER();
    }
    STAMP(10);
    if (wave != 0) {
      u32x4 act[4];
#pragma unroll
      for (int t = 0; t < 4; ++t) act[t] = lds_ld<u32x4>(aa[t], 0);
      SCHED_FENCE();
      f32x4 acc = {0.f, 0.f, 0.f, 0.f};
#pragma unroll
      for (int t = 0; t < 4; ++t) acc = mma(asb(act[t]), wf[t], acc);
      st_sc1(tag4(acc, tag), rws, rvo, rso);
    }
    STAMP(11);
  } else if (is_red) {
    LAS char* fa = lds + L_FIN + ((lane >> 4) * 8 + (lane & 7)) * 64;
    LAS char* fw = fa + wave * 4 + (lane & 8) * 4;
    LAS char* fr = fa + (lane & 8) * 2;
    unsigned oo = (lane & 8) ? 0x7fffffffu : oi * 2;
    float xf = bf_lo(xv);
    PIN_V(fw); PIN_V(fr); PIN_V(oo); PIN_V(xf);
    const rsrc_t ro = make_rsrc(p_out, NB * DM * 2);
    const unsigned so = WS_RP + red * 184 * 128, sd = wave < 7 ? so + 16384 + wave * 1024 : so + wave * 2048;
    const float dw = wave < 7 ? 1.f : 0.f;
    u32x4 a0, a1, a2;
    STAMP(8);
    SPIN(SPIN_TAIL) {
      a2 = ld_sc1(rws, l16, sd) ^ tb;
      a0 = ld_sc1(rws, l16, so + wave * 2048) ^ tb;
      a1 = ld_sc1(rws, l16, so + wave * 2048 + 1024) ^ tb;
      if (wave_none(((or4(a0) | or4(a1) | or4(a2)) & 255u) != 0u)) break;
      MEM_FENCE();
    }
    STAMP(9);
    {
      const f32x4 u = asf4(a0) + asf4(a1), d = asf4(a2);
      const float zs = fold16(fold32(u[0], u[2]), fold32(u[1], u[3])), zd = fold16(fold32(d[0], d[2]), fold32(d[1], d[3])) * dw;
      const bool hi = lane & 8;
      lds_st<float>(fw, 0, (hi ? zd : zs) + dpp<XOR8>(hi ? zs : zd));
    }
    WG_BARRIER();
    STAMP(10);
    if (wave == 2) {
      const f32x4 fd = lds_ld<f32x4>(fr, 32), fp = lds_ld<f32x4>(fr, 0);
      float dn = (fd[0] + fd[1]) + (fd[2] + fd[3]);
      dn = round_bf16(dn + dpp<XOR8>(dn));
      const float g = round_bf16(dn * __builtin_amdgcn_rcpf(1.0f + __builtin_amdgcn_exp2f(-dn * LOG2E)));
      float sv = (fp[0] + fp[1]) + (fp[2] + fp[3]);
      sv = round_bf16(sv + dpp<XOR8>(sv));
      const float res = xf + round_bf16(sv * g);
      __builtin_amdgcn_raw_buffer_store_b16((u16)pack_bf16(res, res), ro, oo, 0, 0);
      if (red == 0 && lane == 0) __builtin_amdgcn_raw_buffer_store_b32(tag, rws, 0, WS_EPOCH, AUX_SC1);
      STAMP(11);
    }
  }
  STAMP_OUT();
}

}

extern "C" void kernel_launch(void* const* d_in, const int* in_sizes, int n_in, void* d_out, int out_size, void* d_ws,
                              size_t ws_size, hipStream_t stream) {
  fused_decode<<<256, 512, 0, stream>>>((const u16*)d_in[0], (const u16*)d_in[1], (const u16*)d_in[2], (const u16*)d_in[3],
                                        (const u16*)d_in[4], (const u16*)d_in[6], (const u16*)d_in[7], (const u16*)d_in[8],
                                        (u16*)d_out, (char*)d_ws + WS_OFF);
}
```

```cpp
#include <hip/hip_runtime.h>
#include <hip/hip_bf16.h>
#include <stdint.h>

#define DIAG 0
#define ABL 0
#define CP 11000
#define CPPOS 0
#define MLAT 7
#define OAT 8
#define OUT_AUX 0
#define QLATE 0
#define RD 0
#define RMAP 0
#define QKV 1
#define OVP 0
#define MLS 16
#define ORI 1
#define PKO 0
#define EPO 1
#define FPW 4
#define P0 32
#define DT 57
#define PL 60
#define PAT 1
#define LPA 8
#define LPB 8
#define GA 39
#define GB 29
#define OD 0
#define B0W 8
#define XL 0
#define STG 10
#define CAN 0
#define CAND 0
#define HALF 0
#define SREV 1
#define DTV DT
#define KG 0
#define GI 3
#define PT 0
#define WS_OFF 0
#define KV_AUX 2
#define W_AUX 2

namespace {

typedef unsigned short u16;
typedef unsigned long long u64;
typedef unsigned u32x2 __attribute__((ext_vector_type(2)));
typedef unsigned u32x4 __attribute__((ext_vector_type(4)));
typedef unsigned u32x16 __attribute__((ext_vector_type(16)));
typedef float f32x2 __attribute__((ext_vector_type(2)));
typedef float f32x4 __attribute__((ext_vector_type(4)));
typedef short s16x4 __attribute__((ext_vector_type(4)));
typedef __bf16 bf16x2 __attribute__((ext_vector_type(2)));
typedef __bf16 bf16x8 __attribute__((ext_vector_type(8)));
#define LAS __attribute__((address_space(3)))
typedef __amdgpu_buffer_rsrc_t rsrc_t;

constexpr int NB = 4, DM = 448, SK = 8192, WIN = 2048, FF = 1792, QW = 4096;
constexpr int AUX_SC1 = 16;
constexpr unsigned SPIN_MAX = 1u << 18;
constexpr float LOG2E = 1.4426950408889634f;

constexpr unsigned WS_EPOCH = 0;
constexpr unsigned WS_QP = 4096;
constexpr unsigned WS_GP = WS_QP + 8 * 4 * 4096;
constexpr unsigned WS_OP = WS_GP + 56 * 2048;
constexpr unsigned WS_ML = WS_OP + 128 * 4096;
constexpr unsigned ML_I = MLS ? MLS : 128, ML_H = MLS ? 32 * MLS : 16;
constexpr unsigned WS_RP = WS_ML + 8 * 32 * (MLS ? MLS : 16);
constexpr unsigned WS_END = WS_RP + 56 * 184 * 128;
constexpr unsigned WS_DIAG = 8u << 20;

constexpr int L_V = 0;
constexpr int L_XN = L_V;
constexpr int L_TILE = 131072;
constexpr int L_FIN = L_TILE;
constexpr int L_Q = L_TILE + 7 * 4096;
constexpr int L_P = L_Q + 1024;
constexpr int L_A = L_P + 1024;
constexpr int L_SYNC = L_A + 256;
constexpr int L_ZERO = L_SYNC, F_XN = L_SYNC + 16, F_EP = L_SYNC + 32, F_Q = L_SYNC + 48, F_T = L_SYNC + 64, L_PROG = L_SYNC + 80;
constexpr int L_MX = L_SYNC + 96;
constexpr int L_LS = L_MX + 32;
constexpr int L_F = L_SYNC + 256;
constexpr int L_END = L_SYNC + 512;

#define SCHED_FENCE() __builtin_amdgcn_sched_barrier(0)
#define MEM_FENCE() asm volatile("" ::: "memory")
#define WG_BARRIER() do { SCHED_FENCE(); asm volatile("s_waitcnt lgkmcnt(0)\n\ts_barrier" ::: "memory"); SCHED_FENCE(); } while (0)
#define WG_BARRIER_BARE() do { SCHED_FENCE(); asm volatile("s_barrier" ::: "memory"); SCHED_FENCE(); } while (0)
#define VM_WAIT(n) do { SCHED_FENCE(); asm volatile("s_waitcnt vmcnt(%0)" :: "i"(n) : "memory"); SCHED_FENCE(); } while (0)
#define VM_DRAIN() do { SCHED_FENCE(); __builtin_amdgcn_s_waitcnt(0x0F70); SCHED_FENCE(); MEM_FENCE(); } while (0)
#define NAP() __builtin_amdgcn_s_sleep(1)
#define PACE(t) do { if (P0) { SCHED_FENCE(); pace_until(t_go + (t)); SCHED_FENCE(); } } while (0)
#define PIN_V(x) asm volatile("" : "+v"(x))
#define PIN_S(x) asm volatile("" : "+s"(x))
#define SPIN(limit) _Pragma("nounroll") for (unsigned s_ = 0; s_ < (limit); ++s_)

__device__ __forceinline__ void pace_until(unsigned when) {
  asm volatile("1:\n\ts_memrealtime vcc\n\ts_waitcnt lgkmcnt(0)\n\ts_sub_i32 vcc_lo, vcc_lo, %0\n\ts_cmp_lt_i32 vcc_lo, 0\n\ts_cbranch_scc1 1b"
               :: "s"(when) : "vcc", "scc", "memory");
}
__device__ __forceinline__ float asf(unsigned u) { return __builtin_bit_cast(float, u); }
__device__ __forceinline__ unsigned asu(float f) { return __builtin_bit_cast(unsigned, f); }
__device__ __forceinline__ unsigned pack_bf16(float lo, float hi) {
  f32x2 f = {lo, hi};
  return __builtin_bit_cast(unsigned, __builtin_convertvector(f, bf16x2));
}
__device__ __forceinline__ float round_bf16(float v) { return asf(pack_bf16(0.f, v)); }
__device__ __forceinline__ float bf_lo(unsigned u) { return asf(u << 16); }
__device__ __forceinline__ float bf_hi(unsigned u) { return asf(u & 0xffff0000u); }
template <int CTRL> __device__ __forceinline__ float dpp(float x) {
  return asf((unsigned)__builtin_amdgcn_mov_dpp((int)asu(x), CTRL, 0xf, 0xf, true));
}
constexpr int XOR1 = 0xB1, XOR2 = 0x4E, XOR7 = 0x141, XOR8 = 0x128;
__device__ __forceinline__ float fold16(float a, float b) {
  auto s = __builtin_amdgcn_permlane16_swap(asu(a), asu(b), false, false);
  return asf(s[0]) + asf(s[1]);
}
__device__ __forceinline__ float fold32(float a, float b) {
  auto s = __builtin_amdgcn_permlane32_swap(asu(a), asu(b), false, false);
  return asf(s[0]) + asf(s[1]);
}
__device__ __forceinline__ float max16(float a, float b) {
  auto s = __builtin_amdgcn_permlane16_swap(asu(a), asu(b), false, false);
  return fmaxf(asf(s[0]), asf(s[1]));
}
__device__ __forceinline__ float max32(float a, float b) {
  auto s = __builtin_amdgcn_permlane32_swap(asu(a), asu(b), false, false);
  return fmaxf(asf(s[0]), asf(s[1]));
}
__device__ __forceinline__ float row_sum(float v) {
  v += dpp<XOR8>(v); v += dpp<XOR7>(v); v += dpp<XOR2>(v); v += dpp<XOR1>(v);
  return v;
}
__device__ __forceinline__ float row_max(float v) {
  v = fmaxf(v, dpp<XOR8>(v)); v = fmaxf(v, dpp<XOR7>(v)); v = fmaxf(v, dpp<XOR2>(v)); v = fmaxf(v, dpp<XOR1>(v));
  return v;
}
__device__ __forceinline__ float fold_hi(f32x4 v) {
  const float z = fold16(fold32(v[0], v[2]), fold32(v[1], v[3]));
  return z + dpp<XOR8>(z);
}
__device__ __forceinline__ rsrc_t make_rsrc(u64 p, unsigned bytes) {
  return __builtin_amdgcn_make_buffer_rsrc((void*)p, 0, bytes, 0x00020000);
}
__device__ __forceinline__ u32x4 tag4(f32x4 v, unsigned tag) {
  u32x4 r;
  r[0] = __builtin_amdgcn_perm(asu(v[0]), tag, 0x07060500u);
  r[1] = __builtin_amdgcn_perm(asu(v[1]), tag, 0x07060501u);
  r[2] = __builtin_amdgcn_perm(asu(v[2]), tag, 0x07060502u);
  r[3] = __builtin_amdgcn_perm(asu(v[3]), tag, 0x07060503u);
  return r;
}
__device__ __forceinline__ unsigned next_tag(unsigned epoch) {
  const unsigned e = __builtin_amdgcn_readfirstlane(epoch);
  return ((e & 0x7f7f7f7fu) + 0x01010101u) ^ (e & 0x80808080u);
}
__device__ __forceinline__ u32x4 tag_bytes(unsigned tag) { return u32x4{tag & 255u, (tag >> 8) & 255u, (tag >> 16) & 255u, tag >> 24}; }
__device__ __forceinline__ u32x4 tag_nibbles(unsigned tag) { return u32x4{tag & 15u, (tag >> 8) & 15u, (tag >> 16) & 15u, (tag >> 24) & 15u}; }
__device__ __forceinline__ u32x4 tag4_fine(f32x4 v, unsigned tag) {
  return ((__builtin_bit_cast(u32x4, v) + 8u) & 0xfffffff0u) | tag_nibbles(tag);
}
__device__ __forceinline__ unsigned or4(u32x4 v) { return v[0] | v[1] | v[2] | v[3]; }
__device__ __forceinline__ f32x4 asf4(u32x4 v) { return __builtin_bit_cast(f32x4, v); }
__device__ __forceinline__ u32x4 ld_sc1(rsrc_t r, unsigned voff, unsigned soff) {
  return __builtin_amdgcn_raw_buffer_load_b128(r, voff, soff, AUX_SC1);
}
__device__ __forceinline__ void st_sc1(u32x4 v, rsrc_t r, unsigned voff, unsigned soff) {
  __builtin_amdgcn_raw_buffer_store_b128(v, r, voff, soff, AUX_SC1);
}
__device__ __forceinline__ bool wave_none(bool bad) { return __builtin_amdgcn_ballot_w64(bad) == 0ull; }

template <typename T> __device__ __forceinline__ T lds_ld(LAS char* l, int off) { return *(LAS T*)(l + off); }
template <typename T> __device__ __forceinline__ void lds_st(LAS char* l, int off, T v) { *(LAS T*)(l + off) = v; }
__device__ __forceinline__ void lds_flag(LAS char* l, int off, unsigned v) {
  MEM_FENCE();
  *(volatile LAS unsigned*)(l + off) = v;
  MEM_FENCE();
}
__device__ __forceinline__ u32x4 lds_peek(LAS char* l, int off) { return *(volatile LAS u32x4*)(l + off); }
__device__ __forceinline__ void lds_wait_all(LAS char* l, int off) {
  SPIN(SPIN_MAX) { const u32x4 f = lds_peek(l, off); if (wave_none((f[0] & f[1] & f[2] & f[3]) == 0u)) break; NAP(); }
  MEM_FENCE();
}
__device__ __forceinline__ bool unset4(u32x4 v) { return max(max(v[0], v[1]), max(v[2], v[3])) == 0xffffffffu; }

__device__ __forceinline__ void dma16(rsrc_t r, LAS char* dst, unsigned voff, unsigned soff) {
  __builtin_amdgcn_raw_ptr_buffer_load_lds(r, (LAS void*)dst, 16, voff, soff, 0, W_AUX);
}
__device__ __forceinline__ void tile_fetch(rsrc_t r, unsigned voff, unsigned soff, unsigned step, LAS char* img) {
#pragma unroll
  for (int i = 0; i < 4; ++i) dma16(r, img + i * 1024, voff, soff + i * step);
}
template <int GAP> __device__ __forceinline__ bf16x8 tr_frag(LAS char* a) {
  const s16x4 lo = __builtin_amdgcn_ds_read_tr16_b64_v4i16((LAS s16x4*)a);
  const s16x4 hi = __builtin_amdgcn_ds_read_tr16_b64_v4i16((LAS s16x4*)(a + GAP));
  return __builtin_bit_cast(bf16x8, __builtin_shufflevector(lo, hi, 0, 1, 2, 3, 4, 5, 6, 7));
}
__device__ __forceinline__ void tile_frags(LAS char* img, int lane, bf16x8 (&w)[4]) {
  const int row = lane >> 2, p = lane & 3;
#pragma unroll
  for (int t = 0; t < 4; ++t) w[t] = tr_frag<2048>(img + row * 128 + (((2 * t + (p >> 1)) ^ (row & 6)) << 4) + 8 * (p & 1));
}
__device__ __forceinline__ f32x4 mma(bf16x8 a, bf16x8 b, f32x4 c) { return __builtin_amdgcn_mfma_f32_16x16x32_bf16(a, b, c, 0, 0, 0); }
__device__ __forceinline__ bf16x8 asb(u32x4 v) { return __builtin_bit_cast(bf16x8, v); }

#define STAMP(k)
#define STAMP_OUT()

__global__ void __launch_bounds__(512, 2)
fused_decode(const u16* x, const u16* kc, const u16* vc, const u16* wq, const u16* wo, const u16* wg, const u16* wu,
             const u16* wd, u16* out, char* ws) {
  __shared__ __attribute__((aligned(16))) char lds_raw[L_END];
  LAS char* lds = (LAS char*)lds_raw;
  STAMP(0);
  const int bid = blockIdx.x, lane = threadIdx.x & 63, wave = __builtin_amdgcn_readfirstlane(threadIdx.x >> 6);
  const int hk = bid & 7, idx = bid >> 3, w4 = wave & 3;
  const bool loader = wave >= 4;
  const u64 p_x = (u64)x, p_k = (u64)kc, p_v = (u64)vc, p_wq = (u64)wq, p_wo = (u64)wo, p_wg = (u64)wg, p_wu = (u64)wu, p_wd = (u64)wd;
  const u64 p_out = (u64)out, p_ws = (u64)ws;
#define ARGS_IN "s"(p_x), "s"(p_k), "s"(p_v), "s"(p_wq), "s"(p_wo), "s"(p_wg), "s"(p_wu), "s"(p_wd), "s"(p_out), "s"(p_ws)
#define KV_OFF ((((idx >> 3) * 8 + hk) * SK + (SK - WIN) + (idx & 7) * 256) * 512)
#define ROT (idx + 3 * hk + w4)
#define SEQ ((unsigned)(0x0564312075643120ull >> (((idx + 3 * hk) & 7) * 4)))
#define is_wo (idx >= 16)
#define late (idx >= 9)
#define is_red (idx < 7)
#define wo_un (((hk + 1 + (idx & 3)) & 7) * 16 + (idx - 16))
#define dn_un (bid - 72)
#define red (RMAP ? hk * 7 + idx : bid)
#define oi (ORI ? (bid & 3) * DM + (bid >> 2) * 32 + (lane & 31) : (lane >> 4) * DM + red * 8 + (lane & 7))
#define rws make_rsrc(p_ws, WS_END)
  constexpr unsigned SPIN_TAIL = (ABL & 1) ? 1 : SPIN_MAX;
  const unsigned l16 = lane * 16;
  unsigned tag, xv = 0;

  unsigned cpf = 0, ptf = 0, t_k;
#define PAGE_TOUCH() ptf = __builtin_amdgcn_raw_buffer_load_b32(make_rsrc(w4 == 0 ? p_k + KV_OFF : w4 == 1 ? p_v + KV_OFF : w4 == 3 ? p_out : is_wo ? p_wo + (wo_un >> 3) * 256 * DM * 2 : p_wd + (late ? dn_un : 0) * 32 * DM * 2, 4), 0, 0, KV_AUX)
  if (!loader) {
    const unsigned iv = lane < 12 ? 0u : 0xffffffffu;
    lds_st<u32x2>(lds, L_SYNC + lane * 8, u32x2{iv, iv});
#define CODE_PREFETCH() cpf = __builtin_amdgcn_raw_buffer_load_b32(make_rsrc(__builtin_amdgcn_s_getpc(), CP), 0, (idx * 4 + w4) * 128, 0)
    if (CP && CPPOS == 0) CODE_PREFETCH();
    asm volatile("" :: ARGS_IN);
    if (PT == 1) PAGE_TOUCH();
    WG_BARRIER();
    t_k = (unsigned)__builtin_amdgcn_s_memrealtime();
    if (CP && CPPOS == 1) CODE_PREFETCH();
    if (PT == 2) PAGE_TOUCH();
    STAMP(1);
  }
  MEM_FENCE();
  if (loader) {
    __builtin_amdgcn_s_setprio(2);
    const bool is_q = idx < 16, is_gate = idx < 23, has_early = idx < 30;
    const int half = hk & 1;
    int et_q = idx + 16 * ((idx + (hk >> 1)) & 3), et_f = (idx - (is_gate ? 16 : 23)) * 4 + (hk >> 1);
    asm("" : "+s"(et_q), "+s"(et_f));
    const int etile = is_q ? et_q : et_f;
    const int r8 = lane >> 3;
    const unsigned swz = ((lane & 7) ^ (r8 & 6)) << 4;
    LAS char* img = lds + L_TILE + w4 * 4096;
    unsigned stride = is_q ? QW * 2 : FF * 2;
    unsigned evo = r8 * stride + swz, eso = (half * 224 + w4 * 32) * stride + etile * 128, xvo = l16;
    unsigned ebytes = has_early ? DM * stride : 0, eso2 = w4 < 3 ? eso + 128 * stride : 0x40000000u, xso = w4 * DM * 2;
    unsigned estep = 8 * stride, img2 = w4 < 3 ? L_TILE + (w4 + 4) * 4096 : L_V + 8192;
    asm volatile("" : "+v"(evo), "+v"(xvo));
    asm volatile("" : "+s"(eso), "+s"(estep), "+s"(ebytes), "+s"(eso2), "+s"(xso), "+s"(img2));
    SCHED_FENCE();
    asm volatile("" :: ARGS_IN : "memory");
    u32x4 xr;
    unsigned epoch;
    if (XL == 0) {
      xr = __builtin_amdgcn_raw_buffer_load_b128(make_rsrc(p_x, NB * DM * 2), xvo, xso, 0);
      epoch = __builtin_amdgcn_raw_buffer_load_b32(rws, 0, WS_EPOCH, 0);
    }
    SCHED_FENCE();
    {
      const rsrc_t rw = make_rsrc(is_q ? p_wq : (is_gate ? p_wg : p_wu), ebytes);
      tile_fetch(rw, evo, eso, estep, img);
      tile_fetch(rw, evo, eso2, estep, lds + img2);
    }
    SCHED_FENCE();
    if (XL == 1) {
      xr = __builtin_amdgcn_raw_buffer_load_b128(make_rsrc(p_x, NB * DM * 2), xvo, xso, 0);
      epoch = __builtin_amdgcn_raw_buffer_load_b32(rws, 0, WS_EPOCH, 0);
    }
    if (B0W < 10) VM_WAIT(B0W);
    WG_BARRIER_BARE();
    const unsigned t_go = (unsigned)__builtin_amdgcn_s_memrealtime() + P0 + (SREV ? 3 - (idx >> 3) : idx >> 3) * STG;
    SCHED_FENCE();
    STAMP(1);
    {
      float v[8], s1 = 0.f, s2 = 0.f;
      const float x0 = bf_lo(__builtin_amdgcn_readfirstlane(xr[0]));
#pragma unroll
      for (int k = 0; k < 4; ++k) { v[2 * k] = bf_lo(xr[k]); v[2 * k + 1] = bf_hi(xr[k]); }
#pragma unroll
      for (int k = 0; k < 8; ++k) { const float d = v[k] - x0; s1 += d; s2 = fmaf(d, d, s2); }
      float z = fold32(lane < 56 ? s1 : 0.f, lane < 56 ? s2 : 0.f);
      z = row_sum(fold16(z, z)) * (1.0f / DM);
      const float md = asf(__builtin_amdgcn_readlane(asu(z), 0)), m2 = asf(__builtin_amdgcn_readlane(asu(z), 32));
      const float var = round_bf16(fmaxf(fmaf(-md, md, m2), 0.f)), mb = round_bf16(x0 + md);
      const float rc = __builtin_amdgcn_rcpf(round_bf16(__builtin_amdgcn_sqrtf(round_bf16(var + 1e-5f))));
      u32x4 xn;
#pragma unroll
      for (int k = 0; k < 4; ++k) {
        const unsigned d = pack_bf16(v[2 * k] - mb, v[2 * k + 1] - mb);
        xn[k] = pack_bf16(bf_lo(d) * rc, bf_hi(d) * rc);
      }
      lds_st<u32x4>(lds, L_XN + w4 * 1024 + lane * 16, xn);
      lds_flag(lds, F_XN + w4 * 4, 1u);
      lds_wait_all(lds, F_XN);
      tag = next_tag(epoch);
      PIN_S(tag);
      const int bt = lane & 3, g = lane >> 4, jj = (lane >> 2) & 3;
      const int xo = L_XN + bt * 1024 + (half * 224 + w4 * 32 + 4 * g) * 2;
      const u32x2 a0 = lds_ld<u32x2>(lds, xo), a1 = lds_ld<u32x2>(lds, xo + 32);
      const u32x2 a2 = lds_ld<u32x2>(lds, xo + 256), a3 = lds_ld<u32x2>(lds, xo + 288);
      const u32x4 act0 = {a0[0], a0[1], a1[0], a1[1]}, act1 = {a2[0], a2[1], a3[0], a3[1]}, zero = {0u, 0u, 0u, 0u};
      VM_DRAIN();
      bf16x8 wf0[4], wf1[4];
      tile_frags(img, lane, wf0);
      f32x4 acc = {0.f, 0.f, 0.f, 0.f};
      if (w4 < 3) {
        tile_frags(img + 4 * 4096, lane, wf1);
#pragma unroll
        for (int t = 0; t < 4; ++t) acc = mma(wf1[t], asb(jj == t ? act1 : zero), acc);
      }
#pragma unroll
      for (int t = 0; t < 4; ++t) acc = mma(wf0[t], asb(jj == t ? act0 : zero), acc);
      lds_st<f32x4>(img, bt * 256 + ((lane & 12) + g) * 16, acc);
      lds_flag(lds, F_EP + w4 * 4, 1u);
      if (w4 == 3 && has_early) {
        const int pb = lane >> 4, cq = lane & 15;
        const unsigned voff = is_q ? WS_QP + (etile >> 3) * 16384 + pb * 4096 + (etile & 6) * 512 + half * 512 + (etile & 1) * 256 + cq * 16
                                   : WS_GP + (etile * 2 + (cq >> 3)) * 2048 + (is_gate ? 0 : 1024) + half * 512 + pb * 128 + (cq & 7) * 16;
        lds_wait_all(lds, F_EP);
        f32x4 sum = lds_ld<f32x4>(lds, L_TILE + lane * 16);
#pragma unroll
        for (int p = 1; p < 4; ++p) sum += lds_ld<f32x4>(lds, L_TILE + p * 4096 + lane * 16);
        st_sc1(tag4_fine(sum, tag), rws, voff, 0);
      }
    }
    STAMP(2);
    __builtin_amdgcn_s_setprio(0);
    const rsrc_t rv = make_rsrc(p_v + KV_OFF + (PAT ? w4 * 32768 : 0), PAT ? 32768 : 131072);
    const unsigned hf = lane >> 5;
    unsigned vo0 = (lane ^ hf) * 16, vo1 = (lane ^ (2 + hf)) * 16, vo2 = (lane ^ (4 + hf)) * 16, vo3 = (lane ^ (6 + hf)) * 16;
    const rsrc_t rl = make_rsrc(is_wo ? p_wo : p_wd, (is_wo ? QW : FF) * DM * 2);
    const int row0 = is_wo ? (wo_un >> 3) * 256 + (wo_un & 6) * 32 + (wo_un & 1) * 4 : dn_un * 32;
    unsigned lvo = (is_wo ? (r8 & 4) * 2 + (r8 & 3) : r8) * (DM * 2) + (((lane & 7) ^ (r8 >> 1)) << 4);
    unsigned lso = row0 * (DM * 2) + w4 * 128, lstep = (is_wo ? 16 : 8) * DM * 2;
    PIN_V(vo0); PIN_V(vo1); PIN_V(vo2); PIN_V(vo3); PIN_V(lvo); PIN_S(lso); PIN_S(lstep);
    WG_BARRIER();
    STAMP(3);
    unsigned t_v = 8 * DT + KG;
    _Pragma("nounroll") for (int g = 0; g < 8; ++g) {
      const unsigned so = PAT ? ((SEQ >> (4 * g)) & 7) << 12 : ((g * 4 + ROT) & 31) << 12;
      if (LPA < 8 && g == LPA) { if (late) { PACE(t_v); tile_fetch(rl, lvo, lso, lstep, img); } t_v += GA; }
      if (LPB < 8 && g == LPB) { if (late && w4 < 3) { PACE(t_v); tile_fetch(rl, lvo, lso + 512, lstep, img + 4 * 4096); } t_v += GB; }
      PACE(t_v);
      t_v += DTV;
      LAS void* dst = (LAS void*)(lds + L_V + (g * 4 + w4) * 4096);
      __builtin_amdgcn_raw_ptr_buffer_load_lds(rv, dst, 16, vo0, so, 0, KV_AUX);
      __builtin_amdgcn_raw_ptr_buffer_load_lds(rv, dst, 16, vo1, so, 1024, KV_AUX);
      __builtin_amdgcn_raw_ptr_buffer_load_lds(rv, dst, 16, vo2, so, 2048, KV_AUX);
      __builtin_amdgcn_raw_ptr_buffer_load_lds(rv, dst, 16, vo3, so, 3072, KV_AUX);
      VM_WAIT(8);
      lds_flag(lds, L_PROG + w4 * 4, g + 1);
    }
    STAMP(4);
    VM_WAIT(4);
    lds_flag(lds, L_PROG + w4 * 4, 9);
    if (late && LPA == 8) {
      PACE(t_v - DTV + PL);
      tile_fetch(rl, lvo, lso, lstep, img);
      if (LPB == 8 && w4 < 3) { tile_fetch(rl, lvo, lso + 512, lstep, img + 4 * 4096); VM_WAIT(8); } else VM_WAIT(4);
    } else VM_WAIT(0);
    STAMP(5);
    WG_BARRIER_BARE();
  } else {
    u32x4 kr[32], qv;
    unsigned kepoch;
#define qso (WS_QP + hk * 16384 + (idx >> 3) * 4096 + w4 * 1024)
    const unsigned kvo = (lane & 7) * 512 + ((lane >> 3) & 1) * 64 + (lane >> 4) * 16, rot = PAT ? SEQ : ROT << 12;
    const rsrc_t rk = make_rsrc(p_k + KV_OFF + (PAT ? w4 * 32768 : 0), PAT ? 32768 : 131072);
    const unsigned t_go = t_k + P0 + (SREV ? 3 - (idx >> 3) : idx >> 3) * STG;
#pragma unroll
    for (int g = 0; g < 8; ++g) {
      const unsigned so = PAT ? ((rot >> (4 * g)) & 7) << 12 : (g * 16384 + rot) & 0x1f000;
      PACE(g * DT + (g >= GI ? KG : 0));
#pragma unroll
      for (int i = 0; i < 4; ++i) kr[g * 4 + i] = __builtin_amdgcn_raw_buffer_load_b128(rk, kvo + i * 128, so, KV_AUX);
    }
    SCHED_FENCE();
    kepoch = __builtin_amdgcn_raw_buffer_load_b32(rws, 0, WS_EPOCH, 0);
    if (!QLATE) qv = ld_sc1(rws, l16, qso);
    STAMP(2);
    WG_BARRIER_BARE();
    STAMP(3);
    const int j = idx & 7, b = idx >> 3;
    if (QLATE) { VM_WAIT(QLATE - 1); qv = ld_sc1(rws, l16, qso); }
    tag = next_tag(kepoch);
    asm volatile("" : "+s"(tag) : "v"(cpf), "v"(ptf));
    SPIN(SPIN_MAX) {
      qv ^= tag_nibbles(tag);
      if (wave_none((or4(qv) & 15u) != 0u)) break;
      MEM_FENCE();
      qv = ld_sc1(rws, l16, qso);
    }
    lds_st<unsigned>(lds, L_Q + w4 * 256 + (lane & 31) * 8 + (lane >> 5) * 4,
                     pack_bf16(fold32(asf(qv[0]), asf(qv[2])), fold32(asf(qv[1]), asf(qv[3]))));
    lds_flag(lds, F_Q + w4 * 4, 1u);
    lds_wait_all(lds, F_Q);
    STAMP(4);
    float m0, m1;
    {
      const int jj = (lane >> 2) & 3;
      const int qa = L_Q + (lane & 1) * 512 + ((lane >> 1) & 1) * 64 + (lane >> 4) * 16;
      f32x4 c0 = {0.f, 0.f, 0.f, 0.f}, c1 = {0.f, 0.f, 0.f, 0.f};
      if (QKV == 0) {
      u32x4 qf[4];
#pragma unroll
      for (int s = 0; s < 4; ++s) qf[s] = lds_ld<u32x4>(lds, qa + s * 128);
#pragma unroll
      for (int t = 0; t < 4; ++t)
#pragma unroll
        for (int s = 0; s < 4; ++s) {
          const bf16x8 qm = asb(jj == t ? qf[s] : u32x4{0u, 0u, 0u, 0u});
          c0 = mma(qm, asb(kr[t * 4 + s]), c0);
          c1 = mma(qm, asb(kr[16 + t * 4 + s]), c1);
        }
      } else {
        u32x4 qf[4];
#pragma unroll
        for (int s = 0; s < 4; ++s) qf[s] = lds_ld<u32x4>(lds, qa + s * 128);
        f32x4 c[8];
#pragma unroll
        for (int t = 0; t < 8; ++t) c[t] = mma(asb(qf[0]), asb(kr[t * 4]), f32x4{0.f, 0.f, 0.f, 0.f});
#pragma unroll
        for (int s = 1; s < 4; ++s)
#pragma unroll
          for (int t = 0; t < 8; ++t) c[t] = mma(asb(qf[s]), asb(kr[t * 4 + s]), c[t]);
        const bool g1 = lane & 16, g2 = lane & 32;
        c0 = g2 ? (g1 ? c[3] : c[2]) : (g1 ? c[1] : c[0]);
        c1 = g2 ? (g1 ? c[7] : c[6]) : (g1 ? c[5] : c[4]);
      }
      const bool cb = lane & 8;
      const float r0 = (cb ? c1[2] : c0[0]) + dpp<XOR8>(cb ? c0[2] : c1[0]);
      const float r1 = (cb ? c1[3] : c0[1]) + dpp<XOR8>(cb ? c0[3] : c1[1]);
      const unsigned sp = pack_bf16(r0, r1);
      const float s0 = bf_lo(sp) * 0.0625f, s1 = bf_hi(sp) * 0.0625f;
      float mw = max32(s0, s1);
      mw = row_max(max16(mw, mw));
      *(volatile LAS float*)(lds + L_MX + (lane >> 5) * 16 + w4 * 4) = mw;
      u32x4 x0, x1;
      SPIN(SPIN_MAX) { x0 = lds_peek(lds, L_MX); x1 = lds_peek(lds, L_MX + 16); if (wave_none(unset4(x0) | unset4(x1))) break; }
      m0 = fmaxf(fmaxf(asf(x0[0]), asf(x0[1])), fmaxf(asf(x0[2]), asf(x0[3])));
      m1 = fmaxf(fmaxf(asf(x1[0]), asf(x1[1])), fmaxf(asf(x1[2]), asf(x1[3])));
      const float p0 = __builtin_amdgcn_exp2f((s0 - m0) * LOG2E), p1 = __builtin_amdgcn_exp2f((s1 - m1) * LOG2E);
      const unsigned pk = pack_bf16(p0, p1);
      const int po = L_P + (((lane >> 3) & 1) * 4 + (lane >> 4)) * 64 + ((w4 & 1) * 2 + ((lane >> 2) & 1)) * 16 + ((w4 >> 1) * 4 + (lane & 3)) * 2;
      lds_st<u16>(lds, po, (u16)pk);
      lds_st<u16>(lds, po + 512, (u16)(pk >> 16));
      float l = fold32(p0, p1);
      l = row_sum(fold16(l, l));
      MEM_FENCE();
      *(volatile LAS float*)(lds + L_LS + (lane >> 5) * 16 + w4 * 4) = l;
    }
    u32x4 ls;
    SPIN(SPIN_MAX) { ls = lds_peek(lds, L_LS + (lane >> 5) * 16); if (wave_none(unset4(ls))) break; }
    MEM_FENCE();
    STAMP(5);
    if (w4 < 2) {
      const int g = lane >> 4, row = lane >> 2, p = lane & 3, tl = lane & 7;
      LAS char* va[8];
#pragma unroll
      for (int t = 0; t < 8; ++t) va[t] = lds + L_V + row * 512 + ((w4 * 16 + (p >> 1) * 8 + (t ^ (row & 7))) << 4) + 8 * (p & 1);
      LAS char* pa = lds + L_P + ((lane >> 3) & 1) * 512 + g * 16;
      unsigned ovo = (((lane >> 3) & 1) * 8 + g) * 4096 + tl * 16, oso = WS_OP + (hk * 16 + w4 * 4) * 4096 + (b * 8 + j) * 128;
      PIN_V(ovo); PIN_S(oso);
      f32x4 o = {0.f, 0.f, 0.f, 0.f};
      u32x4 pm[8];
      {
        const u32x4 pl = lds_ld<u32x4>(pa, 0);
#pragma unroll
        for (int t = 0; t < 8; ++t) pm[t] = tl == t ? pl : u32x4{0u, 0u, 0u, 0u};
      }
      _Pragma("nounroll") for (int ks = 0; ks < 7; ++ks) {
        SPIN(SPIN_MAX) {
          const u32x4 pr = lds_peek(lds, L_PROG);
          if (wave_none(min(min(pr[0], pr[1]), min(pr[2], pr[3])) <= (unsigned)(ks + 2))) break;
        }
        MEM_FENCE();
        pa += 64;
        const u32x4 pl = lds_ld<u32x4>(pa, 0);
        bf16x8 vf[8];
#pragma unroll
        for (int t = 0; t < 8; ++t) vf[t] = tr_frag<8192>(va[t]);
        SCHED_FENCE();
#pragma unroll
        for (int t = 0; t < 8; ++t) {
          o = mma(vf[t], asb(pm[t]), o);
          SCHED_FENCE();
          pm[t] = tl == t ? pl : u32x4{0u, 0u, 0u, 0u};
          va[t] += 16384;
          PIN_V(pm[t]); PIN_V(va[t]);
          SCHED_FENCE();
        }
      }
      STAMP(6);
      WG_BARRIER_BARE();
      {
        bf16x8 vf[8];
#pragma unroll
        for (int t = 0; t < 8; ++t) vf[t] = tr_frag<8192>(va[t]);
        SCHED_FENCE();
#pragma unroll
        for (int t = 0; t < 8; ++t) o = mma(vf[t], asb(pm[t]), o);
      }
      st_sc1(tag4(o, tag), rws, ovo, oso);
      STAMP(7);
    } else {
      if (w4 == 3) {
        const float lsum = (asf(ls[0]) + asf(ls[1])) + (asf(ls[2]) + asf(ls[3]));
        const float l1 = asf(__builtin_amdgcn_readlane(asu(lsum), 32));
        if (lane == 0) st_sc1(tag4(f32x4{m0, lsum, m1, l1}, tag), rws, 0, WS_ML + hk * ML_H + idx * ML_I);
      } else if (late && !is_wo) {
        u32x4 gg, gu;
        SPIN(SPIN_MAX) {
          gg = ld_sc1(rws, l16, WS_GP + dn_un * 2048) ^ tag_nibbles(tag);
          gu = ld_sc1(rws, l16, WS_GP + dn_un * 2048 + 1024) ^ tag_nibbles(tag);
          if (wave_none(((or4(gg) | or4(gu)) & 15u) != 0u)) break;
          MEM_FENCE();
        }
        const unsigned gt = pack_bf16(fold32(asf(gg[0]), asf(gg[2])), fold32(asf(gg[1]), asf(gg[3])));
        const unsigned up = pack_bf16(fold32(asf(gu[0]), asf(gu[2])), fold32(asf(gu[1]), asf(gu[3])));
        const float g0 = bf_lo(gt), g1 = bf_hi(gt);
        const unsigned hv = pack_bf16(g0 * (g0 > 0.f ? 1.f : 0.f) * bf_lo(up), g1 * (g1 > 0.f ? 1.f : 0.f) * bf_hi(up));
        lds_st<unsigned>(lds, L_A + ((lane >> 3) & 3) * 64 + (lane & 3) * 16 + (lane & 4) * 2 + (lane >> 5) * 4, hv);
      } else if (is_red) {
        xv = __builtin_amdgcn_raw_buffer_load_b16(make_rsrc(p_x, NB * DM * 2), oi * 2, 0, 0);
        PIN_V(xv);
      }
      const bool mlw = w4 == 3 && is_wo;
      const unsigned mso = mlw ? WS_ML + (wo_un >> 4) * ML_H : WS_END;
      SPIN(SPIN_MAX) { if (wave_none(*(volatile LAS unsigned*)(lds + L_PROG) < (unsigned)MLAT)) break; __builtin_amdgcn_s_sleep(4); }
      u32x4 mv = ld_sc1(rws, (lane & 31) * ML_I, mso);
      STAMP(6);
      WG_BARRIER();
      mv ^= tag_bytes(tag);
      PIN_V(mv);
      if (mlw) {
        SPIN(SPIN_TAIL) {
          if (wave_none((or4(mv) & 255u) != 0u)) break;
          MEM_FENCE();
          mv = ld_sc1(rws, (lane & 31) * ML_I, mso) ^ tag_bytes(tag);
        }
        const float m = asf((idx & 8) ? mv[2] : mv[0]);
        float mx = fmaxf(m, dpp<XOR7>(m));
        mx = fmaxf(mx, dpp<XOR2>(mx));
        mx = fmaxf(mx, dpp<XOR1>(mx));
        const float e = __builtin_amdgcn_exp2f((m - mx) * LOG2E);
        float lt = e * asf((idx & 8) ? mv[3] : mv[1]);
        lt += dpp<XOR7>(lt);
        lt += dpp<XOR2>(lt);
        lt += dpp<XOR1>(lt);
        *(volatile LAS unsigned*)(lds + L_F + (lane & 31) * 4) = asu(e * __builtin_amdgcn_rcpf(lt)) & 0xfffffffeu;
      }
    }
  }

  if (ABL & 16) { VM_WAIT(0); STAMP(8); STAMP_OUT(); return; }
  const u32x4 tb = tag_bytes(tag);
  if (late) {
    const int tile = loader ? w4 : 3 + w4;
    const bool gather = is_wo && !loader;
    float f;
    u32x4 ov;
    unsigned oso = WS_OP + wo_un * 4096 + w4 * 1024;
    bf16x8 wf[4];
    LAS char* aa[4];
    unsigned rvo = (((lane >> 3) & 1) * 4 + (lane >> 4)) * (184 * 128) + (lane & 7) * 16, rso = WS_RP + (tile * 8 * 184 + (is_wo ? wo_un : 128 + dn_un)) * 128;
    LAS char* ta[4];
    LAS char* ad = lds + L_A + w4 * 64 + (lane & 3) * 16 + ((lane & 4) + (lane >> 4)) * 2;
#pragma unroll
    for (int t = 0; t < 4; ++t) {
      aa[t] = lds + ((lane & 3) == t ? L_A + ((lane >> 2) & 3) * 64 + (lane >> 4) * 16 : L_ZERO);
      ta[t] = lds + L_TILE + tile * 4096 + (lane >> 2) * 128 + (((lane & 2) * 2 + (t ^ ((lane >> 3) & 3))) << 4) + 8 * (lane & 1);
      PIN_V(aa[t]); PIN_V(ta[t]);
    }
    PIN_V(rvo); PIN_S(rso); PIN_V(ad);
    if (loader) {
      VM_WAIT(FPW);
      lds_flag(lds, F_T + w4 * 4, 1u);
      VM_DRAIN();
      lds_flag(lds, F_T + w4 * 4, 2u);
    } else {
      LAS char* ft = lds + F_T + (w4 ? w4 - 1 : 0) * 4;
      if (is_wo) {
        SPIN(SPIN_MAX) { if (wave_none(*(volatile LAS unsigned*)ft == 0u)) break; }
        MEM_FENCE();
        if (OD) __builtin_amdgcn_s_sleep(OD);
        ov = ld_sc1(rws, l16, oso);
        SPIN(SPIN_MAX) {
          f = *(volatile LAS float*)(lds + L_F + w4 * 32 + (lane >> 3) * 4);
          if (wave_none(asu(f) == 0xffffffffu)) break;
        }
      }
      if (wave != 0) SPIN(SPIN_MAX) { if (wave_none(*(volatile LAS unsigned*)ft != 2u)) break; }
      MEM_FENCE();
    }
    if (wave != 0) {
#pragma unroll
      for (int t = 0; t < 4; ++t) wf[t] = tr_frag<2048>(ta[t]);
      MEM_FENCE();
    }
    STAMP(8);
    if (is_wo) {
      if (gather) {
        SPIN(SPIN_TAIL) {
          ov ^= tb;
          if (wave_none((or4(ov) & 255u) != 0u)) break;
          MEM_FENCE();
          ov = ld_sc1(rws, l16, oso);
        }
        STAMP(9);
        const float o = fold_hi(asf4(ov) * f);
        lds_st<u16>(ad, 0, (u16)pack_bf16(o, o));
      }
      WG_BARRIER();
    }
    STAMP(10);
    if (wave != 0) {
      u32x4 act[4];
#pragma unroll
      for (int t = 0; t < 4; ++t) act[t] = lds_ld<u32x4>(aa[t], 0);
      SCHED_FENCE();
      f32x4 acc = {0.f, 0.f, 0.f, 0.f};
#pragma unroll
      for (int t = 0; t < 4; ++t) acc = mma(asb(act[t]), wf[t], acc);
      st_sc1(tag4(acc, tag), rws, rvo, HALF && ((((is_wo ? wo_un : dn_un) >> 3) ^ (tile >> 2)) & 1) ? WS_END : rso);
    }
    STAMP(11);
  } else if (is_red) {
    LAS char* fw = lds + L_FIN + ((lane >> 4) * 8 + (lane & 7)) * 64 + wave * 4 + (lane & 8) * 4;
    LAS char* fr = lds + L_FIN + (ORI ? (lane & 31) * 64 + (lane >> 5) * 32 : ((lane >> 4) * 8 + (lane & 7)) * 64 + (lane & 8) * 2);
    unsigned oo = (lane & (ORI ? 32 + PKO : 8)) ? 0x7fffffffu : oi * 2;
    float xf = bf_lo(xv);
    PIN_V(fw); PIN_V(fr); PIN_V(oo); PIN_V(xf);
    const rsrc_t ro = make_rsrc(p_out, NB * DM * 2);
    const unsigned so = WS_RP + red * 184 * 128, sd = wave < 7 && !(HALF && ((wave ^ (bid >> 5)) & 1)) ? so + 16384 + wave * 1024 : so + wave * 2048 + (HALF ? (bid >> 5) * 1024 : 0);
    const float dw = wave < 7 ? 1.f : 0.f;
    u32x4 a0, a1, a2;
    if (RD) __builtin_amdgcn_s_sleep(RD);
    STAMP(8);
    if (CAN) {
      const unsigned co = (lane & (CAN - 1)) * 640;
      SPIN(SPIN_TAIL) {
        const unsigned c = __builtin_amdgcn_raw_buffer_load_b32(rws, co, so + wave * 2048, AUX_SC1) ^ tb[0];
        if (wave_none((c & 255u) != 0u)) break;
        MEM_FENCE();
      }
      if (CAND) __builtin_amdgcn_s_sleep(CAND);
    }
    SPIN(SPIN_TAIL) {
      a2 = ld_sc1(rws, l16, sd) ^ tb;
      a0 = ld_sc1(rws, l16, so + wave * 2048 + (HALF ? (bid >> 5) * 1024 : 0)) ^ tb;
      a1 = HALF ? a0 : ld_sc1(rws, l16, so + wave * 2048 + 1024) ^ tb;
      if (wave_none(((or4(a0) | or4(a1) | or4(a2)) & 255u) != 0u)) break;
      MEM_FENCE();
    }
    STAMP(9);
    {
      const f32x4 u = asf4(a0) + asf4(a1), d = asf4(a2);
      const float zs = fold16(fold32(u[0], u[2]), fold32(u[1], u[3])), zd = fold16(fold32(d[0], d[2]), fold32(d[1], d[3])) * dw;
      const bool hi = lane & 8;
      lds_st<float>(fw, 0, (hi ? zd : zs) + dpp<XOR8>(hi ? zs : zd));
    }
    WG_BARRIER();
    STAMP(10);
    if (wave == 2) {
      float dn, sv;
      if (ORI) {
        const f32x4 t = lds_ld<f32x4>(fr, 0) + lds_ld<f32x4>(fr, 16);
        const float u = round_bf16((t[0] + t[1]) + (t[2] + t[3]));
        auto sw = __builtin_amdgcn_permlane32_swap(asu(u), asu(u), false, false);
        sv = asf(sw[0]);
        dn = asf(sw[1]);
      } else {
        const f32x4 fd = lds_ld<f32x4>(fr, 32), fp = lds_ld<f32x4>(fr, 0);
        dn = (fd[0] + fd[1]) + (fd[2] + fd[3]);
        dn = round_bf16(dn + dpp<XOR8>(dn));
        sv = (fp[0] + fp[1]) + (fp[2] + fp[3]);
        sv = round_bf16(sv + dpp<XOR8>(sv));
      }
      const float g = round_bf16(dn * __builtin_amdgcn_rcpf(1.0f + __builtin_amdgcn_exp2f(-dn * LOG2E)));
      const float res = xf + round_bf16(sv * g);
      if (PKO) __builtin_amdgcn_raw_buffer_store_b32(pack_bf16(res, dpp<XOR1>(res)), ro, oo, 0, OUT_AUX);
      else __builtin_amdgcn_raw_buffer_store_b16((u16)pack_bf16(res, res), ro, oo, 0, OUT_AUX);
      if (!EPO && red == 0 && lane == 0) __builtin_amdgcn_raw_buffer_store_b32(tag, rws, 0, WS_EPOCH, AUX_SC1);
      STAMP(11);
    }
  } else if (EPO && bid == 56 && wave == 3) {
    SPIN(SPIN_TAIL) {
      unsigned bad = 0;
#pragma unroll
      for (int u = 0; u < 4; ++u) bad |= or4(ld_sc1(rws, l16 + u * 1024, WS_ML) ^ tb);
      if (wave_none((bad & 255u) != 0u)) break;
      MEM_FENCE();
    }
    if (lane == 0) __builtin_amdgcn_raw_buffer_store_b32(tag, rws, 0, WS_EPOCH, AUX_SC1);
  }
  STAMP_OUT();
}

}

extern "C" void kernel_launch(void* const* d_in, const int* in_sizes, int n_in, void* d_out, int out_size, void* d_ws,
                              size_t ws_size, hipStream_t stream) {
  fused_decode<<<256, 512, 0, stream>>>((const u16*)d_in[0], (const u16*)d_in[1], (const u16*)d_in[2], (const u16*)d_in[3],
                                        (const u16*)d_in[4], (const u16*)d_in[6], (const u16*)d_in[7], (const u16*)d_in[8],
                                        (u16*)d_out, (char*)d_ws + WS_OFF);
}
```

```cpp
#define PROBE 0
#define PROBE_GRP 1
#define PROBE_X 0
#include <hip/hip_runtime.h>
#include <hip/hip_bf16.h>
#include <stdint.h>

#define DIAG 0
#define ABL 0
#define CP 11000
#define CPPOS 0
#define MLAT 7
#define OAT 8
#define OUT_AUX 0
#define QLATE 0
#define RD 0
#define RMAP 0
#define QKV 1
#define OVP 0
#define MLS 16
#define ORI 1
#define PKO 0
#define EPO 1
#define FPW 4
#define P0 32
#define DT 56
#define PL 60
#define PAT 1
#define LPA 8
#define LPB 8
#define GA 39
#define GB 29
#define OD 0
#define B0W 8
#define XL 0
#define STG 10
#define CAN 0
#define CAND 0
#define HALF 0
#define SREV 1
#define DTV DT
#define KG 0
#define GI 3
#define PT 0
#define WS_OFF 0
#define KV_AUX 2
#define W_AUX 2

namespace {

typedef unsigned short u16;
typedef unsigned long long u64;
typedef unsigned u32x2 __attribute__((ext_vector_type(2)));
typedef unsigned u32x4 __attribute__((ext_vector_type(4)));
typedef unsigned u32x16 __attribute__((ext_vector_type(16)));
typedef float f32x2 __attribute__((ext_vector_type(2)));
typedef float f32x4 __attribute__((ext_vector_type(4)));
typedef short s16x4 __attribute__((ext_vector_type(4)));
typedef __bf16 bf16x2 __attribute__((ext_vector_type(2)));
typedef __bf16 bf16x8 __attribute__((ext_vector_type(8)));
#define LAS __attribute__((address_space(3)))
typedef __amdgpu_buffer_rsrc_t rsrc_t;

constexpr int NB = 4, DM = 448, SK = 8192, WIN = 2048, FF = 1792, QW = 4096;
constexpr int AUX_SC1 = 16;
constexpr unsigned SPIN_MAX = 1u << 18;
constexpr float LOG2E = 1.4426950408889634f;

constexpr unsigned WS_EPOCH = 0;
constexpr unsigned WS_QP = 4096;
constexpr unsigned WS_GP = WS_QP + 8 * 4 * 4096;
constexpr unsigned WS_OP = WS_GP + 56 * 2048;
constexpr unsigned WS_ML = WS_OP + 128 * 4096;
constexpr unsigned ML_I = MLS ? MLS : 128, ML_H = MLS ? 32 * MLS : 16;
constexpr unsigned WS_RP = WS_ML + 8 * 32 * (MLS ? MLS : 16);
constexpr unsigned WS_END = WS_RP + 56 * 184 * 128;
constexpr unsigned WS_DIAG = 8u << 20;

constexpr int L_V = 0;
constexpr int L_XN = L_V;
constexpr int L_TILE = 131072;
constexpr int L_FIN = L_TILE;
constexpr int L_Q = L_TILE + 7 * 4096;
constexpr int L_P = L_Q + 1024;
constexpr int L_A = L_P + 1024;
constexpr int L_SYNC = L_A + 256;
constexpr int L_ZERO = L_SYNC, F_XN = L_SYNC + 16, F_EP = L_SYNC + 32, F_Q = L_SYNC + 48, F_T = L_SYNC + 64, L_PROG = L_SYNC + 80;
constexpr int L_MX = L_SYNC + 96;
constexpr int L_LS = L_MX + 32;
constexpr int L_F = L_SYNC + 256;
constexpr int L_END = L_SYNC + 512;

#define SCHED_FENCE() __builtin_amdgcn_sched_barrier(0)
#define MEM_FENCE() asm volatile("" ::: "memory")
#define WG_BARRIER() do { SCHED_FENCE(); asm volatile("s_waitcnt lgkmcnt(0)\n\ts_barrier" ::: "memory"); SCHED_FENCE(); } while (0)
#define WG_BARRIER_BARE() do { SCHED_FENCE(); asm volatile("s_barrier" ::: "memory"); SCHED_FENCE(); } while (0)
#define VM_WAIT(n) do { SCHED_FENCE(); asm volatile("s_waitcnt vmcnt(%0)" :: "i"(n) : "memory"); SCHED_FENCE(); } while (0)
#define VM_DRAIN() do { SCHED_FENCE(); __builtin_amdgcn_s_waitcnt(0x0F70); SCHED_FENCE(); MEM_FENCE(); } while (0)
#define NAP() __builtin_amdgcn_s_sleep(1)
#define PACE(t) do { if (P0) { SCHED_FENCE(); pace_until(t_go + (t)); SCHED_FENCE(); } } while (0)
#define PIN_V(x) asm volatile("" : "+v"(x))
#define PIN_S(x) asm volatile("" : "+s"(x))
#define SPIN(limit) _Pragma("nounroll") for (unsigned s_ = 0; s_ < (limit); ++s_)

__device__ __forceinline__ void pace_until(unsigned when) {
  asm volatile("1:\n\ts_memrealtime vcc\n\ts_waitcnt lgkmcnt(0)\n\ts_sub_i32 vcc_lo, vcc_lo, %0\n\ts_cmp_lt_i32 vcc_lo, 0\n\ts_cbranch_scc1 1b"
               :: "s"(when) : "vcc", "scc", "memory");
}
__device__ __forceinline__ float asf(unsigned u) { return __builtin_bit_cast(float, u); }
__device__ __forceinline__ unsigned asu(float f) { return __builtin_bit_cast(unsigned, f); }
__device__ __forceinline__ unsigned pack_bf16(float lo, float hi) {
  f32x2 f = {lo, hi};
  return __builtin_bit_cast(unsigned, __builtin_convertvector(f, bf16x2));
}
__device__ __forceinline__ float round_bf16(float v) { return asf(pack_bf16(0.f, v)); }
__device__ __forceinline__ float bf_lo(unsigned u) { return asf(u << 16); }
__device__ __forceinline__ float bf_hi(unsigned u) { return asf(u & 0xffff0000u); }
template <int CTRL> __device__ __forceinline__ float dpp(float x) {
  return asf((unsigned)__builtin_amdgcn_mov_dpp((int)asu(x), CTRL, 0xf, 0xf, true));
}
constexpr int XOR1 = 0xB1, XOR2 = 0x4E, XOR7 = 0x141, XOR8 = 0x128;
__device__ __forceinline__ float fold16(float a, float b) {
  auto s = __builtin_amdgcn_permlane16_swap(asu(a), asu(b), false, false);
  return asf(s[0]) + asf(s[1]);
}
__device__ __forceinline__ float fold32(float a, float b) {
  auto s = __builtin_amdgcn_permlane32_swap(asu(a), asu(b), false, false);
  return asf(s[0]) + asf(s[1]);
}
__device__ __forceinline__ float max16(float a, float b) {
  auto s = __builtin_amdgcn_permlane16_swap(asu(a), asu(b), false, false);
  return fmaxf(asf(s[0]), asf(s[1]));
}
__device__ __forceinline__ float max32(float a, float b) {
  auto s = __builtin_amdgcn_permlane32_swap(asu(a), asu(b), false, false);
  return fmaxf(asf(s[0]), asf(s[1]));
}
__device__ __forceinline__ float row_sum(float v) {
  v += dpp<XOR8>(v); v += dpp<XOR7>(v); v += dpp<XOR2>(v); v += dpp<XOR1>(v);
  return v;
}
__device__ __forceinline__ float row_max(float v) {
  v = fmaxf(v, dpp<XOR8>(v)); v = fmaxf(v, dpp<XOR7>(v)); v = fmaxf(v, dpp<XOR2>(v)); v = fmaxf(v, dpp<XOR1>(v));
  return v;
}
__device__ __forceinline__ float fold_hi(f32x4 v) {
  const float z = fold16(fold32(v[0], v[2]), fold32(v[1], v[3]));
  return z + dpp<XOR8>(z);
}
__device__ __forceinline__ rsrc_t make_rsrc(u64 p, unsigned bytes) {
  return __builtin_amdgcn_make_buffer_rsrc((void*)p, 0, bytes, 0x00020000);
}
__device__ __forceinline__ u32x4 tag4(f32x4 v, unsigned tag) {
  u32x4 r;
  r[0] = __builtin_amdgcn_perm(asu(v[0]), tag, 0x07060500u);
  r[1] = __builtin_amdgcn_perm(asu(v[1]), tag, 0x07060501u);
  r[2] = __builtin_amdgcn_perm(asu(v[2]), tag, 0x07060502u);
  r[3] = __builtin_amdgcn_perm(asu(v[3]), tag, 0x07060503u);
  return r;
}
__device__ __forceinline__ unsigned next_tag(unsigned epoch) {
  const unsigned e = __builtin_amdgcn_readfirstlane(epoch);
  return ((e & 0x7f7f7f7fu) + 0x01010101u) ^ (e & 0x80808080u);
}
__device__ __forceinline__ u32x4 tag_bytes(unsigned tag) { return u32x4{tag & 255u, (tag >> 8) & 255u, (tag >> 16) & 255u, tag >> 24}; }
__device__ __forceinline__ u32x4 tag_nibbles(unsigned tag) { return u32x4{tag & 15u, (tag >> 8) & 15u, (tag >> 16) & 15u, (tag >> 24) & 15u}; }
__device__ __forceinline__ u32x4 tag4_fine(f32x4 v, unsigned tag) {
  return ((__builtin_bit_cast(u32x4, v) + 8u) & 0xfffffff0u) | tag_nibbles(tag);
}
__device__ __forceinline__ unsigned or4(u32x4 v) { return v[0] | v[1] | v[2] | v[3]; }
__device__ __forceinline__ f32x4 asf4(u32x4 v) { return __builtin_bit_cast(f32x4, v); }
__device__ __forceinline__ u32x4 ld_sc1(rsrc_t r, unsigned voff, unsigned soff) {
  return __builtin_amdgcn_raw_buffer_load_b128(r, voff, soff, AUX_SC1);
}
__device__ __forceinline__ void st_sc1(u32x4 v, rsrc_t r, unsigned voff, unsigned soff) {
  __builtin_amdgcn_raw_buffer_store_b128(v, r, voff, soff, AUX_SC1);
}
__device__ __forceinline__ bool wave_none(bool bad) { return __builtin_amdgcn_ballot_w64(bad) == 0ull; }

template <typename T> __device__ __forceinline__ T lds_ld(LAS char* l, int off) { return *(LAS T*)(l + off); }
template <typename T> __device__ __forceinline__ void lds_st(LAS char* l, int off, T v) { *(LAS T*)(l + off) = v; }
__device__ __forceinline__ void lds_flag(LAS char* l, int off, unsigned v) {
  MEM_FENCE();
  *(volatile LAS unsigned*)(l + off) = v;
  MEM_FENCE();
}
__device__ __forceinline__ u32x4 lds_peek(LAS char* l, int off) { return *(volatile LAS u32x4*)(l + off); }
__device__ __forceinline__ void lds_wait_all(LAS char* l, int off) {
  SPIN(SPIN_MAX) { const u32x4 f = lds_peek(l, off); if (wave_none((f[0] & f[1] & f[2] & f[3]) == 0u)) break; NAP(); }
  MEM_FENCE();
}
__device__ __forceinline__ bool unset4(u32x4 v) { return max(max(v[0], v[1]), max(v[2], v[3])) == 0xffffffffu; }

__device__ __forceinline__ void dma16(rsrc_t r, LAS char* dst, unsigned voff, unsigned soff) {
  __builtin_amdgcn_raw_ptr_buffer_load_lds(r, (LAS void*)dst, 16, voff, soff, 0, W_AUX);
}
__device__ __forceinline__ void tile_fetch(rsrc_t r, unsigned voff, unsigned soff, unsigned step, LAS char* img) {
#pragma unroll
  for (int i = 0; i < 4; ++i) dma16(r, img + i * 1024, voff, soff + i * step);
}
template <int GAP> __device__ __forceinline__ bf16x8 tr_frag(LAS char* a) {
  const s16x4 lo = __builtin_amdgcn_ds_read_tr16_b64_v4i16((LAS s16x4*)a);
  const s16x4 hi = __builtin_amdgcn_ds_read_tr16_b64_v4i16((LAS s16x4*)(a + GAP));
  return __builtin_bit_cast(bf16x8, __builtin_shufflevector(lo, hi, 0, 1, 2, 3, 4, 5, 6, 7));
}
__device__ __forceinline__ void tile_frags(LAS char* img, int lane, bf16x8 (&w)[4]) {
  const int row = lane >> 2, p = lane & 3;
#pragma unroll
  for (int t = 0; t < 4; ++t) w[t] = tr_frag<2048>(img + row * 128 + (((2 * t + (p >> 1)) ^ (row & 6)) << 4) + 8 * (p & 1));
}
__device__ __forceinline__ f32x4 mma(bf16x8 a, bf16x8 b, f32x4 c) { return __builtin_amdgcn_mfma_f32_16x16x32_bf16(a, b, c, 0, 0, 0); }
__device__ __forceinline__ bf16x8 asb(u32x4 v) { return __builtin_bit_cast(bf16x8, v); }

#define STAMP(k) do { if ((k) == PROBE) { SCHED_FENCE(); pst = __builtin_amdgcn_s_memrealtime(); SCHED_FENCE(); } } while (0)
#define STAMP_OUT() do { if ((wave >> 2) == PROBE_GRP && pst && (PROBE_X < 0 || hk == PROBE_X)) while (__builtin_amdgcn_s_memrealtime() < pst + 3000) __builtin_amdgcn_s_sleep(2); } while (0)

__global__ void __launch_bounds__(512, 2)
fused_decode(const u16* x, const u16* kc, const u16* vc, const u16* wq, const u16* wo, const u16* wg, const u16* wu,
             const u16* wd, u16* out, char* ws) {
  __shared__ __attribute__((aligned(16))) char lds_raw[L_END];
  LAS char* lds = (LAS char*)lds_raw;
  u64 pst = 0;
  STAMP(0);
  const int bid = blockIdx.x, lane = threadIdx.x & 63, wave = __builtin_amdgcn_readfirstlane(threadIdx.x >> 6);
  const int hk = bid & 7, idx = bid >> 3, w4 = wave & 3;
  const bool loader = wave >= 4;
  const u64 p_x = (u64)x, p_k = (u64)kc, p_v = (u64)vc, p_wq = (u64)wq, p_wo = (u64)wo, p_wg = (u64)wg, p_wu = (u64)wu, p_wd = (u64)wd;
  const u64 p_out = (u64)out, p_ws = (u64)ws;
#define ARGS_IN "s"(p_x), "s"(p_k), "s"(p_v), "s"(p_wq), "s"(p_wo), "s"(p_wg), "s"(p_wu), "s"(p_wd), "s"(p_out), "s"(p_ws)
#define KV_OFF ((((idx >> 3) * 8 + hk) * SK + (SK - WIN) + (idx & 7) * 256) * 512)
#define ROT (idx + 3 * hk + w4)
#define SEQ ((unsigned)(0x0564312075643120ull >> (((idx + 3 * hk) & 7) * 4)))
#define is_wo (idx >= 16)
#define late (idx >= 9)
#define is_red (idx < 7)
#define wo_un (((hk + 1 + (idx & 3)) & 7) * 16 + (idx - 16))
#define dn_un (bid - 72)
#define red (RMAP ? hk * 7 + idx : bid)
#define oi (ORI ? (bid & 3) * DM + (bid >> 2) * 32 + (lane & 31) : (lane >> 4) * DM + red * 8 + (lane & 7))
#define rws make_rsrc(p_ws, WS_END)
  constexpr unsigned SPIN_TAIL = (ABL & 1) ? 1 : SPIN_MAX;
  const unsigned l16 = lane * 16;
  unsigned tag, xv = 0;

  unsigned cpf = 0, ptf = 0, t_k;
#define PAGE_TOUCH() ptf = __builtin_amdgcn_raw_buffer_load_b32(make_rsrc(w4 == 0 ? p_k + KV_OFF : w4 == 1 ? p_v + KV_OFF : w4 == 3 ? p_out : is_wo ? p_wo + (wo_un >> 3) * 256 * DM * 2 : p_wd + (late ? dn_un : 0) * 32 * DM * 2, 4), 0, 0, KV_AUX)
  if (!loader) {
    const unsigned iv = lane < 12 ? 0u : 0xffffffffu;
    lds_st<u32x2>(lds, L_SYNC + lane * 8, u32x2{iv, iv});
#define CODE_PREFETCH() cpf = __builtin_amdgcn_raw_buffer_load_b32(make_rsrc(__builtin_amdgcn_s_getpc(), CP), 0, (idx * 4 + w4) * 128, 0)
    if (CP && CPPOS == 0) CODE_PREFETCH();
    asm volatile("" :: ARGS_IN);
    if (PT == 1) PAGE_TOUCH();
    WG_BARRIER();
    t_k = (unsigned)__builtin_amdgcn_s_memrealtime();
    if (CP && CPPOS == 1) CODE_PREFETCH();
    if (PT == 2) PAGE_TOUCH();
    STAMP(1);
  }
  MEM_FENCE();
  if (loader) {
    __builtin_amdgcn_s_setprio(2);
    const bool is_q = idx < 16, is_gate = idx < 23, has_early = idx < 30;
    const int half = hk & 1;
    int et_q = idx + 16 * ((idx + (hk >> 1)) & 3), et_f = (idx - (is_gate ? 16 : 23)) * 4 + (hk >> 1);
    asm("" : "+s"(et_q), "+s"(et_f));
    const int etile = is_q ? et_q : et_f;
    const int r8 = lane >> 3;
    const unsigned swz = ((lane & 7) ^ (r8 & 6)) << 4;
    LAS char* img = lds + L_TILE + w4 * 4096;
    unsigned stride = is_q ? QW * 2 : FF * 2;
    unsigned evo = r8 * stride + swz, eso = (half * 224 + w4 * 32) * stride + etile * 128, xvo = l16;
    unsigned ebytes = has_early ? DM * stride : 0, eso2 = w4 < 3 ? eso + 128 * stride : 0x40000000u, xso = w4 * DM * 2;
    unsigned estep = 8 * stride, img2 = w4 < 3 ? L_TILE + (w4 + 4) * 4096 : L_V + 8192;
    asm volatile("" : "+v"(evo), "+v"(xvo));
    asm volatile("" : "+s"(eso), "+s"(estep), "+s"(ebytes), "+s"(eso2), "+s"(xso), "+s"(img2));
    SCHED_FENCE();
    asm volatile("" :: ARGS_IN : "memory");
    u32x4 xr;
    unsigned epoch;
    if (XL == 0) {
      xr = __builtin_amdgcn_raw_buffer_load_b128(make_rsrc(p_x, NB * DM * 2), xvo, xso, 0);
      epoch = __builtin_amdgcn_raw_buffer_load_b32(rws, 0, WS_EPOCH, 0);
    }
    SCHED_FENCE();
    {
      const rsrc_t rw = make_rsrc(is_q ? p_wq : (is_gate ? p_wg : p_wu), ebytes);
      tile_fetch(rw, evo, eso, estep, img);
      tile_fetch(rw, evo, eso2, estep, lds + img2);
    }
    SCHED_FENCE();
    if (XL == 1) {
      xr = __builtin_amdgcn_raw_buffer_load_b128(make_rsrc(p_x, NB * DM * 2), xvo, xso, 0);
      epoch = __builtin_amdgcn_raw_buffer_load_b32(rws, 0, WS_EPOCH, 0);
    }
    if (B0W < 10) VM_WAIT(B0W);
    WG_BARRIER_BARE();
    const unsigned t_go = (unsigned)__builtin_amdgcn_s_memrealtime() + P0 + (SREV ? 3 - (idx >> 3) : idx >> 3) * STG;
    SCHED_FENCE();
    STAMP(1);
    {
      float v[8], s1 = 0.f, s2 = 0.f;
      const float x0 = bf_lo(__builtin_amdgcn_readfirstlane(xr[0]));
#pragma unroll
      for (int k = 0; k < 4; ++k) { v[2 * k] = bf_lo(xr[k]); v[2 * k + 1] = bf_hi(xr[k]); }
#pragma unroll
      for (int k = 0; k < 8; ++k) { const float d = v[k] - x0; s1 += d; s2 = fmaf(d, d, s2); }
      float z = fold32(lane < 56 ? s1 : 0.f, lane < 56 ? s2 : 0.f);
      z = row_sum(fold16(z, z)) * (1.0f / DM);
      const float md = asf(__builtin_amdgcn_readlane(asu(z), 0)), m2 = asf(__builtin_amdgcn_readlane(asu(z), 32));
      const float var = round_bf16(fmaxf(fmaf(-md, md, m2), 0.f)), mb = round_bf16(x0 + md);
      const float rc = __builtin_amdgcn_rcpf(round_bf16(__builtin_amdgcn_sqrtf(round_bf16(var + 1e-5f))));
      u32x4 xn;
#pragma unroll
      for (int k = 0; k < 4; ++k) {
        const unsigned d = pack_bf16(v[2 * k] - mb, v[2 * k + 1] - mb);
        xn[k] = pack_bf16(bf_lo(d) * rc, bf_hi(d) * rc);
      }
      lds_st<u32x4>(lds, L_XN + w4 * 1024 + lane * 16, xn);
      lds_flag(lds, F_XN + w4 * 4, 1u);
      lds_wait_all(lds, F_XN);
      tag = next_tag(epoch);
      PIN_S(tag);
      const int bt = lane & 3, g = lane >> 4, jj = (lane >> 2) & 3;
      const int xo = L_XN + bt * 1024 + (half * 224 + w4 * 32 + 4 * g) * 2;
      const u32x2 a0 = lds_ld<u32x2>(lds, xo), a1 = lds_ld<u32x2>(lds, xo + 32);
      const u32x2 a2 = lds_ld<u32x2>(lds, xo + 256), a3 = lds_ld<u32x2>(lds, xo + 288);
      const u32x4 act0 = {a0[0], a0[1], a1[0], a1[1]}, act1 = {a2[0], a2[1], a3[0], a3[1]}, zero = {0u, 0u, 0u, 0u};
      VM_DRAIN();
      bf16x8 wf0[4], wf1[4];
      tile_frags(img, lane, wf0);
      f32x4 acc = {0.f, 0.f, 0.f, 0.f};
      if (w4 < 3) {
        tile_frags(img + 4 * 4096, lane, wf1);
#pragma unroll
        for (int t = 0; t < 4; ++t) acc = mma(wf1[t], asb(jj == t ? act1 : zero), acc);
      }
#pragma unroll
      for (int t = 0; t < 4; ++t) acc = mma(wf0[t], asb(jj == t ? act0 : zero), acc);
      lds_st<f32x4>(img, bt * 256 + ((lane & 12) + g) * 16, acc);
      lds_flag(lds, F_EP + w4 * 4, 1u);
      if (w4 == 3 && has_early) {
        const int pb = lane >> 4, cq = lane & 15;
        const unsigned voff = is_q ? WS_QP + (etile >> 3) * 16384 + pb * 4096 + (etile & 6) * 512 + half * 512 + (etile & 1) * 256 + cq * 16
                                   : WS_GP + (etile * 2 + (cq >> 3)) * 2048 + (is_gate ? 0 : 1024) + half * 512 + pb * 128 + (cq & 7) * 16;
        lds_wait_all(lds, F_EP);
        f32x4 sum = lds_ld<f32x4>(lds, L_TILE + lane * 16);
#pragma unroll
        for (int p = 1; p < 4; ++p) sum += lds_ld<f32x4>(lds, L_TILE + p * 4096 + lane * 16);
        st_sc1(tag4_fine(sum, tag), rws, voff, 0);
      }
    }
    STAMP(2);
    __builtin_amdgcn_s_setprio(0);
    const rsrc_t rv = make_rsrc(p_v + KV_OFF + (PAT ? w4 * 32768 : 0), PAT ? 32768 : 131072);
    const unsigned hf = lane >> 5;
    unsigned vo0 = (lane ^ hf) * 16, vo1 = (lane ^ (2 + hf)) * 16, vo2 = (lane ^ (4 + hf)) * 16, vo3 = (lane ^ (6 + hf)) * 16;
    const rsrc_t rl = make_rsrc(is_wo ? p_wo : p_wd, (is_wo ? QW : FF) * DM * 2);
    const int row0 = is_wo ? (wo_un >> 3) * 256 + (wo_un & 6) * 32 + (wo_un & 1) * 4 : dn_un * 32;
    unsigned lvo = (is_wo ? (r8 & 4) * 2 + (r8 & 3) : r8) * (DM * 2) + (((lane & 7) ^ (r8 >> 1)) << 4);
    unsigned lso = row0 * (DM * 2) + w4 * 128, lstep = (is_wo ? 16 : 8) * DM * 2;
    PIN_V(vo0); PIN_V(vo1); PIN_V(vo2); PIN_V(vo3); PIN_V(lvo); PIN_S(lso); PIN_S(lstep);
    WG_BARRIER();
    STAMP(3);
    unsigned t_v = 8 * DT + KG;
    _Pragma("nounroll") for (int g = 0; g < 8; ++g) {
      const unsigned so = PAT ? ((SEQ >> (4 * g)) & 7) << 12 : ((g * 4 + ROT) & 31) << 12;
      if (LPA < 8 && g == LPA) { if (late) { PACE(t_v); tile_fetch(rl, lvo, lso, lstep, img); } t_v += GA; }
      if (LPB < 8 && g == LPB) { if (late && w4 < 3) { PACE(t_v); tile_fetch(rl, lvo, lso + 512, lstep, img + 4 * 4096); } t_v += GB; }
      PACE(t_v);
      t_v += DTV;
      LAS void* dst = (LAS void*)(lds + L_V + (g * 4 + w4) * 4096);
      __builtin_amdgcn_raw_ptr_buffer_load_lds(rv, dst, 16, vo0, so, 0, KV_AUX);
      __builtin_amdgcn_raw_ptr_buffer_load_lds(rv, dst, 16, vo1, so, 1024, KV_AUX);
      __builtin_amdgcn_raw_ptr_buffer_load_lds(rv, dst, 16, vo2, so, 2048, KV_AUX);
      __builtin_amdgcn_raw_ptr_buffer_load_lds(rv, dst, 16, vo3, so, 3072, KV_AUX);
      VM_WAIT(8);
      lds_flag(lds, L_PROG + w4 * 4, g + 1);
    }
    STAMP(4);
    VM_WAIT(4);
    lds_flag(lds, L_PROG + w4 * 4, 9);
    if (late && LPA == 8) {
      PACE(t_v - DTV + PL);
      tile_fetch(rl, lvo, lso, lstep, img);
      if (LPB == 8 && w4 < 3) { tile_fetch(rl, lvo, lso + 512, lstep, img + 4 * 4096); VM_WAIT(8); } else VM_WAIT(4);
    } else VM_WAIT(0);
    STAMP(5);
    WG_BARRIER_BARE();
  } else {
    u32x4 kr[32], qv;
    unsigned kepoch;
#define qso (WS_QP + hk * 16384 + (idx >> 3) * 4096 + w4 * 1024)
    const unsigned kvo = (lane & 7) * 512 + ((lane >> 3) & 1) * 64 + (lane >> 4) * 16, rot = PAT ? SEQ : ROT << 12;
    const rsrc_t rk = make_rsrc(p_k + KV_OFF + (PAT ? w4 * 32768 : 0), PAT ? 32768 : 131072);
    const unsigned t_go = t_k + P0 + (SREV ? 3 - (idx >> 3) : idx >> 3) * STG;
#pragma unroll
    for (int g = 0; g < 8; ++g) {
      const unsigned so = PAT ? ((rot >> (4 * g)) & 7) << 12 : (g * 16384 + rot) & 0x1f000;
      PACE(g * DT + (g >= GI ? KG : 0));
#pragma unroll
      for (int i = 0; i < 4; ++i) kr[g * 4 + i] = __builtin_amdgcn_raw_buffer_load_b128(rk, kvo + i * 128, so, KV_AUX);
    }
    SCHED_FENCE();
    kepoch = __builtin_amdgcn_raw_buffer_load_b32(rws, 0, WS_EPOCH, 0);
    if (!QLATE) qv = ld_sc1(rws, l16, qso);
    STAMP(2);
    WG_BARRIER_BARE();
    STAMP(3);
    const int j = idx & 7, b = idx >> 3;
    if (QLATE) { VM_WAIT(QLATE - 1); qv = ld_sc1(rws, l16, qso); }
    tag = next_tag(kepoch);
    asm volatile("" : "+s"(tag) : "v"(cpf), "v"(ptf));
    SPIN(SPIN_MAX) {
      qv ^= tag_nibbles(tag);
      if (wave_none((or4(qv) & 15u) != 0u)) break;
      MEM_FENCE();
      qv = ld_sc1(rws, l16, qso);
    }
    lds_st<unsigned>(lds, L_Q + w4 * 256 + (lane & 31) * 8 + (lane >> 5) * 4,
                     pack_bf16(fold32(asf(qv[0]), asf(qv[2])), fold32(asf(qv[1]), asf(qv[3]))));
    lds_flag(lds, F_Q + w4 * 4, 1u);
    lds_wait_all(lds, F_Q);
    STAMP(4);
    float m0, m1;
    {
      const int jj = (lane >> 2) & 3;
      const int qa = L_Q + (lane & 1) * 512 + ((lane >> 1) & 1) * 64 + (lane >> 4) * 16;
      f32x4 c0 = {0.f, 0.f, 0.f, 0.f}, c1 = {0.f, 0.f, 0.f, 0.f};
      if (QKV == 0) {
      u32x4 qf[4];
#pragma unroll
      for (int s = 0; s < 4; ++s) qf[s] = lds_ld<u32x4>(lds, qa + s * 128);
#pragma unroll
      for (int t = 0; t < 4; ++t)
#pragma unroll
        for (int s = 0; s < 4; ++s) {
          const bf16x8 qm = asb(jj == t ? qf[s] : u32x4{0u, 0u, 0u, 0u});
          c0 = mma(qm, asb(kr[t * 4 + s]), c0);
          c1 = mma(qm, asb(kr[16 + t * 4 + s]), c1);
        }
      } else {
        u32x4 qf[4];
#pragma unroll
        for (int s = 0; s < 4; ++s) qf[s] = lds_ld<u32x4>(lds, qa + s * 128);
        f32x4 c[8];
#pragma unroll
        for (int t = 0; t < 8; ++t) c[t] = mma(asb(qf[0]), asb(kr[t * 4]), f32x4{0.f, 0.f, 0.f, 0.f});
#pragma unroll
        for (int s = 1; s < 4; ++s)
#pragma unroll
          for (int t = 0; t < 8; ++t) c[t] = mma(asb(qf[s]), asb(kr[t * 4 + s]), c[t]);
        const bool g1 = lane & 16, g2 = lane & 32;
        c0 = g2 ? (g1 ? c[3] : c[2]) : (g1 ? c[1] : c[0]);
        c1 = g2 ? (g1 ? c[7] : c[6]) : (g1 ? c[5] : c[4]);
      }
      const bool cb = lane & 8;
      const float r0 = (cb ? c1[2] : c0[0]) + dpp<XOR8>(cb ? c0[2] : c1[0]);
      const float r1 = (cb ? c1[3] : c0[1]) + dpp<XOR8>(cb ? c0[3] : c1[1]);
      const unsigned sp = pack_bf16(r0, r1);
      const float s0 = bf_lo(sp) * 0.0625f, s1 = bf_hi(sp) * 0.0625f;
      float mw = max32(s0, s1);
      mw = row_max(max16(mw, mw));
      *(volatile LAS float*)(lds + L_MX + (lane >> 5) * 16 + w4 * 4) = mw;
      u32x4 x0, x1;
      SPIN(SPIN_MAX) { x0 = lds_peek(lds, L_MX); x1 = lds_peek(lds, L_MX + 16); if (wave_none(unset4(x0) | unset4(x1))) break; }
      m0 = fmaxf(fmaxf(asf(x0[0]), asf(x0[1])), fmaxf(asf(x0[2]), asf(x0[3])));
      m1 = fmaxf(fmaxf(asf(x1[0]), asf(x1[1])), fmaxf(asf(x1[2]), asf(x1[3])));
      const float p0 = __builtin_amdgcn_exp2f((s0 - m0) * LOG2E), p1 = __builtin_amdgcn_exp2f((s1 - m1) * LOG2E);
      const unsigned pk = pack_bf16(p0, p1);
      const int po = L_P + (((lane >> 3) & 1) * 4 + (lane >> 4)) * 64 + ((w4 & 1) * 2 + ((lane >> 2) & 1)) * 16 + ((w4 >> 1) * 4 + (lane & 3)) * 2;
      lds_st<u16>(lds, po, (u16)pk);
      lds_st<u16>(lds, po + 512, (u16)(pk >> 16));
      float l = fold32(p0, p1);
      l = row_sum(fold16(l, l));
      MEM_FENCE();
      *(volatile LAS float*)(lds + L_LS + (lane >> 5) * 16 + w4 * 4) = l;
    }
    u32x4 ls;
    SPIN(SPIN_MAX) { ls = lds_peek(lds, L_LS + (lane >> 5) * 16); if (wave_none(unset4(ls))) break; }
    MEM_FENCE();
    STAMP(5);
    if (w4 < 2) {
      const int g = lane >> 4, row = lane >> 2, p = lane & 3, tl = lane & 7;
      LAS char* va[8];
#pragma unroll
      for (int t = 0; t < 8; ++t) va[t] = lds + L_V + row * 512 + ((w4 * 16 + (p >> 1) * 8 + (t ^ (row & 7))) << 4) + 8 * (p & 1);
      LAS char* pa = lds + L_P + ((lane >> 3) & 1) * 512 + g * 16;
      unsigned ovo = (((lane >> 3) & 1) * 8 + g) * 4096 + tl * 16, oso = WS_OP + (hk * 16 + w4 * 4) * 4096 + (b * 8 + j) * 128;
      PIN_V(ovo); PIN_S(oso);
      f32x4 o = {0.f, 0.f, 0.f, 0.f};
      u32x4 pm[8];
      {
        const u32x4 pl = lds_ld<u32x4>(pa, 0);
#pragma unroll
        for (int t = 0; t < 8; ++t) pm[t] = tl == t ? pl : u32x4{0u, 0u, 0u, 0u};
      }
      _Pragma("nounroll") for (int ks = 0; ks < 7; ++ks) {
        SPIN(SPIN_MAX) {
          const u32x4 pr = lds_peek(lds, L_PROG);
          if (wave_none(min(min(pr[0], pr[1]), min(pr[2], pr[3])) <= (unsigned)(ks + 2))) break;
        }
        MEM_FENCE();
        pa += 64;
        const u32x4 pl = lds_ld<u32x4>(pa, 0);
        bf16x8 vf[8];
#pragma unroll
        for (int t = 0; t < 8; ++t) vf[t] = tr_frag<8192>(va[t]);
        SCHED_FENCE();
#pragma unroll
        for (int t = 0; t < 8; ++t) {
          o = mma(vf[t], asb(pm[t]), o);
          SCHED_FENCE();
          pm[t] = tl == t ? pl : u32x4{0u, 0u, 0u, 0u};
          va[t] += 16384;
          PIN_V(pm[t]); PIN_V(va[t]);
          SCHED_FENCE();
        }
      }
      STAMP(6);
      WG_BARRIER_BARE();
      {
        bf16x8 vf[8];
#pragma unroll
        for (int t = 0; t < 8; ++t) vf[t] = tr_frag<8192>(va[t]);
        SCHED_FENCE();
#pragma unroll
        for (int t = 0; t < 8; ++t) o = mma(vf[t], asb(pm[t]), o);
      }
      st_sc1(tag4(o, tag), rws, ovo, oso);
      STAMP(7);
    } else {
      if (w4 == 3) {
        const float lsum = (asf(ls[0]) + asf(ls[1])) + (asf(ls[2]) + asf(ls[3]));
        const float l1 = asf(__builtin_amdgcn_readlane(asu(lsum), 32));
        if (lane == 0) st_sc1(tag4(f32x4{m0, lsum, m1, l1}, tag), rws, 0, WS_ML + hk * ML_H + idx * ML_I);
      } else if (late && !is_wo) {
        u32x4 gg, gu;
        SPIN(SPIN_MAX) {
          gg = ld_sc1(rws, l16, WS_GP + dn_un * 2048) ^ tag_nibbles(tag);
          gu = ld_sc1(rws, l16, WS_GP + dn_un * 2048 + 1024) ^ tag_nibbles(tag);
          if (wave_none(((or4(gg) | or4(gu)) & 15u) != 0u)) break;
          MEM_FENCE();
        }
        const unsigned gt = pack_bf16(fold32(asf(gg[0]), asf(gg[2])), fold32(asf(gg[1]), asf(gg[3])));
        const unsigned up = pack_bf16(fold32(asf(gu[0]), asf(gu[2])), fold32(asf(gu[1]), asf(gu[3])));
        const float g0 = bf_lo(gt), g1 = bf_hi(gt);
        const unsigned hv = pack_bf16(g0 * (g0 > 0.f ? 1.f : 0.f) * bf_lo(up), g1 * (g1 > 0.f ? 1.f : 0.f) * bf_hi(up));
        lds_st<unsigned>(lds, L_A + ((lane >> 3) & 3) * 64 + (lane & 3) * 16 + (lane & 4) * 2 + (lane >> 5) * 4, hv);
      } else if (is_red) {
        xv = __builtin_amdgcn_raw_buffer_load_b16(make_rsrc(p_x, NB * DM * 2), oi * 2, 0, 0);
        PIN_V(xv);
      }
      const bool mlw = w4 == 3 && is_wo;
      const unsigned mso = mlw ? WS_ML + (wo_un >> 4) * ML_H : WS_END;
      SPIN(SPIN_MAX) { if (wave_none(*(volatile LAS unsigned*)(lds + L_PROG) < (unsigned)MLAT)) break; __builtin_amdgcn_s_sleep(4); }
      u32x4 mv = ld_sc1(rws, (lane & 31) * ML_I, mso);
      STAMP(6);
      WG_BARRIER();
      mv ^= tag_bytes(tag);
      PIN_V(mv);
      if (mlw) {
        SPIN(SPIN_TAIL) {
          if (wave_none((or4(mv) & 255u) != 0u)) break;
          MEM_FENCE();
          mv = ld_sc1(rws, (lane & 31) * ML_I, mso) ^ tag_bytes(tag);
        }
        const float m = asf((idx & 8) ? mv[2] : mv[0]);
        float mx = fmaxf(m, dpp<XOR7>(m));
        mx = fmaxf(mx, dpp<XOR2>(mx));
        mx = fmaxf(mx, dpp<XOR1>(mx));
        const float e = __builtin_amdgcn_exp2f((m - mx) * LOG2E);
        float lt = e * asf((idx & 8) ? mv[3] : mv[1]);
        lt += dpp<XOR7>(lt);
        lt += dpp<XOR2>(lt);
        lt += dpp<XOR1>(lt);
        *(volatile LAS unsigned*)(lds + L_F + (lane & 31) * 4) = asu(e * __builtin_amdgcn_rcpf(lt)) & 0xfffffffeu;
      }
    }
  }

  if (ABL & 16) { VM_WAIT(0); STAMP(8); STAMP_OUT(); return; }
  const u32x4 tb = tag_bytes(tag);
  if (late) {
    const int tile = loader ? w4 : 3 + w4;
    const bool gather = is_wo && !loader;
    float f;
    u32x4 ov;
    unsigned oso = WS_OP + wo_un * 4096 + w4 * 1024;
    bf16x8 wf[4];
    LAS char* aa[4];
    unsigned rvo = (((lane >> 3) & 1) * 4 + (lane >> 4)) * (184 * 128) + (lane & 7) * 16, rso = WS_RP + (tile * 8 * 184 + (is_wo ? wo_un : 128 + dn_un)) * 128;
    LAS char* ta[4];
    LAS char* ad = lds + L_A + w4 * 64 + (lane & 3) * 16 + ((lane & 4) + (lane >> 4)) * 2;
#pragma unroll
    for (int t = 0; t < 4; ++t) {
      aa[t] = lds + ((lane & 3) == t ? L_A + ((lane >> 2) & 3) * 64 + (lane >> 4) * 16 : L_ZERO);
      ta[t] = lds + L_TILE + tile * 4096 + (lane >> 2) * 128 + (((lane & 2) * 2 + (t ^ ((lane >> 3) & 3))) << 4) + 8 * (lane & 1);
      PIN_V(aa[t]); PIN_V(ta[t]);
    }
    PIN_V(rvo); PIN_S(rso); PIN_V(ad);
    if (loader) {
      VM_WAIT(FPW);
      lds_flag(lds, F_T + w4 * 4, 1u);
      VM_DRAIN();
      lds_flag(lds, F_T + w4 * 4, 2u);
    } else {
      LAS char* ft = lds + F_T + (w4 ? w4 - 1 : 0) * 4;
      if (is_wo) {
        SPIN(SPIN_MAX) { if (wave_none(*(volatile LAS unsigned*)ft == 0u)) break; }
        MEM_FENCE();
        if (OD) __builtin_amdgcn_s_sleep(OD);
        ov = ld_sc1(rws, l16, oso);
        SPIN(SPIN_MAX) {
          f = *(volatile LAS float*)(lds + L_F + w4 * 32 + (lane >> 3) * 4);
          if (wave_none(asu(f) == 0xffffffffu)) break;
        }
      }
      if (wave != 0) SPIN(SPIN_MAX) { if (wave_none(*(volatile LAS unsigned*)ft != 2u)) break; }
      MEM_FENCE();
    }
    if (wave != 0) {
#pragma unroll
      for (int t = 0; t < 4; ++t) wf[t] = tr_frag<2048>(ta[t]);
      MEM_FENCE();
    }
    STAMP(8);
    if (is_wo) {
      if (gather) {
        SPIN(SPIN_TAIL) {
          ov ^= tb;
          if (wave_none((or4(ov) & 255u) != 0u)) break;
          MEM_FENCE();
          ov = ld_sc1(rws, l16, oso);
        }
        STAMP(9);
        const float o = fold_hi(asf4(ov) * f);
        lds_st<u16>(ad, 0, (u16)pack_bf16(o, o));
      }
      WG_BARRIER();
    }
    STAMP(10);
    if (wave != 0) {
      u32x4 act[4];
#pragma unroll
      for (int t = 0; t < 4; ++t) act[t] = lds_ld<u32x4>(aa[t], 0);
      SCHED_FENCE();
      f32x4 acc = {0.f, 0.f, 0.f, 0.f};
#pragma unroll
      for (int t = 0; t < 4; ++t) acc = mma(asb(act[t]), wf[t], acc);
      st_sc1(tag4(acc, tag), rws, rvo, HALF && ((((is_wo ? wo_un : dn_un) >> 3) ^ (tile >> 2)) & 1) ? WS_END : rso);
    }
    STAMP(11);
  } else if (is_red) {
    LAS char* fw = lds + L_FIN + ((lane >> 4) * 8 + (lane & 7)) * 64 + wave * 4 + (lane & 8) * 4;
    LAS char* fr = lds + L_FIN + (ORI ? (lane & 31) * 64 + (lane >> 5) * 32 : ((lane >> 4) * 8 + (lane & 7)) * 64 + (lane & 8) * 2);
    unsigned oo = (lane & (ORI ? 32 + PKO : 8)) ? 0x7fffffffu : oi * 2;
    float xf = bf_lo(xv);
    PIN_V(fw); PIN_V(fr); PIN_V(oo); PIN_V(xf);
    const rsrc_t ro = make_rsrc(p_out, NB * DM * 2);
    const unsigned so = WS_RP + red * 184 * 128, sd = wave < 7 && !(HALF && ((wave ^ (bid >> 5)) & 1)) ? so + 16384 + wave * 1024 : so + wave * 2048 + (HALF ? (bid >> 5) * 1024 : 0);
    const float dw = wave < 7 ? 1.f : 0.f;
    u32x4 a0, a1, a2;
    if (RD) __builtin_amdgcn_s_sleep(RD);
    STAMP(8);
    if (CAN) {
      const unsigned co = (lane & (CAN - 1)) * 640;
      SPIN(SPIN_TAIL) {
        const unsigned c = __builtin_amdgcn_raw_buffer_load_b32(rws, co, so + wave * 2048, AUX_SC1) ^ tb[0];
        if (wave_none((c & 255u) != 0u)) break;
        MEM_FENCE();
      }
      if (CAND) __builtin_amdgcn_s_sleep(CAND);
    }
    SPIN(SPIN_TAIL) {
      a2 = ld_sc1(rws, l16, sd) ^ tb;
      a0 = ld_sc1(rws, l16, so + wave * 2048 + (HALF ? (bid >> 5) * 1024 : 0)) ^ tb;
      a1 = HALF ? a0 : ld_sc1(rws, l16, so + wave * 2048 + 1024) ^ tb;
      if (wave_none(((or4(a0) | or4(a1) | or4(a2)) & 255u) != 0u)) break;
      MEM_FENCE();
    }
    STAMP(9);
    {
      const f32x4 u = asf4(a0) + asf4(a1), d = asf4(a2);
      const float zs = fold16(fold32(u[0], u[2]), fold32(u[1], u[3])), zd = fold16(fold32(d[0], d[2]), fold32(d[1], d[3])) * dw;
      const bool hi = lane & 8;
      lds_st<float>(fw, 0, (hi ? zd : zs) + dpp<XOR8>(hi ? zs : zd));
    }
    WG_BARRIER();
    STAMP(10);
    if (wave == 2) {
      float dn, sv;
      if (ORI) {
        const f32x4 t = lds_ld<f32x4>(fr, 0) + lds_ld<f32x4>(fr, 16);
        const float u = round_bf16((t[0] + t[1]) + (t[2] + t[3]));
        auto sw = __builtin_amdgcn_permlane32_swap(asu(u), asu(u), false, false);
        sv = asf(sw[0]);
        dn = asf(sw[1]);
      } else {
        const f32x4 fd = lds_ld<f32x4>(fr, 32), fp = lds_ld<f32x4>(fr, 0);
        dn = (fd[0] + fd[1]) + (fd[2] + fd[3]);
        dn = round_bf16(dn + dpp<XOR8>(dn));
        sv = (fp[0] + fp[1]) + (fp[2] + fp[3]);
        sv = round_bf16(sv + dpp<XOR8>(sv));
      }
      const float g = round_bf16(dn * __builtin_amdgcn_rcpf(1.0f + __builtin_amdgcn_exp2f(-dn * LOG2E)));
      const float res = xf + round_bf16(sv * g);
      if (PKO) __builtin_amdgcn_raw_buffer_store_b32(pack_bf16(res, dpp<XOR1>(res)), ro, oo, 0, OUT_AUX);
      else __builtin_amdgcn_raw_buffer_store_b16((u16)pack_bf16(res, res), ro, oo, 0, OUT_AUX);
      if (!EPO && red == 0 && lane == 0) __builtin_amdgcn_raw_buffer_store_b32(tag, rws, 0, WS_EPOCH, AUX_SC1);
      STAMP(11);
    }
  } else if (EPO && bid == 56 && wave == 3) {
    SPIN(SPIN_TAIL) {
      unsigned bad = 0;
#pragma unroll
      for (int u = 0; u < 4; ++u) bad |= or4(ld_sc1(rws, l16 + u * 1024, WS_ML) ^ tb);
      if (wave_none((bad & 255u) != 0u)) break;
      MEM_FENCE();
    }
    if (lane == 0) __builtin_amdgcn_raw_buffer_store_b32(tag, rws, 0, WS_EPOCH, AUX_SC1);
  }
  STAMP_OUT();
}

}

extern "C" void kernel_launch(void* const* d_in, const int* in_sizes, int n_in, void* d_out, int out_size, void* d_ws,
                              size_t ws_size, hipStream_t stream) {
  fused_decode<<<256, 512, 0, stream>>>((const u16*)d_in[0], (const u16*)d_in[1], (const u16*)d_in[2], (const u16*)d_in[3],
                                        (const u16*)d_in[4], (const u16*)d_in[6], (const u16*)d_in[7], (const u16*)d_in[8],
                                        (u16*)d_out, (char*)d_ws + WS_OFF);
}
```

```cpp
#include <hip/hip_runtime.h>
#include <hip/hip_bf16.h>
#include <stdint.h>

#define DIAG 0
#define CP (10800 + 400 * SYNC)
#define MLAT 7
#define RD 0
#define FPW 4
#define P0 68
#define DT 56
#define PL 60
#define PLD 80
#define B0W 10
#define STG 10
#define PAIR 0
#define SYNC 1
#define SYAT 4
#define SYB 0
#define SYUP 160
#define STE STG
#define WS_OFF 0
#define KV_AUX 2
#define W_AUX 2

namespace {

typedef unsigned short u16;
typedef unsigned long long u64;
typedef unsigned u32x2 __attribute__((ext_vector_type(2)));
typedef unsigned u32x4 __attribute__((ext_vector_type(4)));
typedef unsigned u32x16 __attribute__((ext_vector_type(16)));
typedef float f32x2 __attribute__((ext_vector_type(2)));
typedef float f32x4 __attribute__((ext_vector_type(4)));
typedef short s16x4 __attribute__((ext_vector_type(4)));
typedef __bf16 bf16x2 __attribute__((ext_vector_type(2)));
typedef __bf16 bf16x8 __attribute__((ext_vector_type(8)));
#define LAS __attribute__((address_space(3)))
typedef __amdgpu_buffer_rsrc_t rsrc_t;

constexpr int NB = 4, DM = 448, SK = 8192, WIN = 2048, FF = 1792, QW = 4096;
constexpr int AUX_SC1 = 16;
constexpr unsigned SPIN_MAX = 1u << 18;
constexpr float LOG2E = 1.4426950408889634f;

constexpr unsigned WS_EPOCH = 0;
constexpr unsigned WS_TS = 2048;
constexpr unsigned WS_QP = 4096;
constexpr unsigned WS_GP = WS_QP + 8 * 4 * 4096;
constexpr unsigned WS_OP = WS_GP + 28 * 4096;
constexpr unsigned WS_ML = WS_OP + 64 * 8192;
constexpr unsigned WS_RP = WS_ML + 4096;
constexpr unsigned RP_BOX = 92 * 128;
constexpr unsigned WS_END = WS_RP + 56 * RP_BOX;
constexpr unsigned WS_DIAG = 8u << 20;

constexpr int L_V = 0;
constexpr int L_TILE = L_V;
constexpr int L_XN = L_V + 32768;
constexpr int L_FIN = L_V;
constexpr int L_Q = 131072;
constexpr int L_P = L_Q + 1024;
constexpr int L_A = L_P + 1024;
constexpr int L_SYNC = L_A + 512;
constexpr int L_ZERO = L_SYNC;
constexpr int F_XN = L_SYNC + 16, F_EP = L_SYNC + 32, F_Q = L_SYNC + 48, L_PROG = L_SYNC + 64, F_PV = L_SYNC + 80, F_T = L_SYNC + 96;
constexpr int L_F = L_SYNC + 128;
constexpr int L_MX = L_SYNC + 384;
constexpr int L_LS = L_MX + 32;
constexpr int L_END = L_SYNC + 512;

#define SCHED_FENCE() __builtin_amdgcn_sched_barrier(0)
#define MEM_FENCE() asm volatile("" ::: "memory")
#define WG_BARRIER() do { SCHED_FENCE(); asm volatile("s_waitcnt lgkmcnt(0)\n\ts_barrier" ::: "memory"); SCHED_FENCE(); } while (0)
#define WG_BARRIER_BARE() do { SCHED_FENCE(); asm volatile("s_barrier" ::: "memory"); SCHED_FENCE(); } while (0)
#define VM_WAIT(n) do { SCHED_FENCE(); asm volatile("s_waitcnt vmcnt(%0)" :: "i"(n) : "memory"); SCHED_FENCE(); } while (0)
#define VM_DRAIN() do { SCHED_FENCE(); __builtin_amdgcn_s_waitcnt(0x0F70); SCHED_FENCE(); MEM_FENCE(); } while (0)
#define NAP() __builtin_amdgcn_s_sleep(1)
#define PACE(t) do { SCHED_FENCE(); pace_until(t_go + (t)); SCHED_FENCE(); } while (0)
#define PIN_V(x) asm volatile("" : "+v"(x))
#define PIN_S(x) asm volatile("" : "+s"(x))
#define SPIN(limit) _Pragma("nounroll") for (unsigned s_ = 0; s_ < (limit); ++s_)

__device__ __forceinline__ void pace_until(unsigned when) {
  asm volatile("1:\n\ts_memrealtime vcc\n\ts_waitcnt lgkmcnt(0)\n\ts_sub_i32 vcc_lo, vcc_lo, %0\n\ts_cmp_lt_i32 vcc_lo, 0\n\ts_cbranch_scc1 1b"
               :: "s"(when) : "vcc", "scc", "memory");
}
__device__ __forceinline__ float asf(unsigned u) { return __builtin_bit_cast(float, u); }
__device__ __forceinline__ unsigned asu(float f) { return __builtin_bit_cast(unsigned, f); }
__device__ __forceinline__ unsigned pack_bf16(float lo, float hi) {
  f32x2 f = {lo, hi};
  return __builtin_bit_cast(unsigned, __builtin_convertvector(f, bf16x2));
}
__device__ __forceinline__ float round_bf16(float v) { return asf(pack_bf16(0.f, v)); }
__device__ __forceinline__ float bf_lo(unsigned u) { return asf(u << 16); }
__device__ __forceinline__ float bf_hi(unsigned u) { return asf(u & 0xffff0000u); }
template <int CTRL> __device__ __forceinline__ float dpp(float x) {
  return asf((unsigned)__builtin_amdgcn_mov_dpp((int)asu(x), CTRL, 0xf, 0xf, true));
}
constexpr int XOR1 = 0xB1, XOR2 = 0x4E, XOR7 = 0x141, XOR8 = 0x128;
__device__ __forceinline__ float fold16(float a, float b) {
  auto s = __builtin_amdgcn_permlane16_swap(asu(a), asu(b), false, false);
  return asf(s[0]) + asf(s[1]);
}
__device__ __forceinline__ float fold32(float a, float b) {
  auto s = __builtin_amdgcn_permlane32_swap(asu(a), asu(b), false, false);
  return asf(s[0]) + asf(s[1]);
}
__device__ __forceinline__ float max16(float a, float b) {
  auto s = __builtin_amdgcn_permlane16_swap(asu(a), asu(b), false, false);
  return fmaxf(asf(s[0]), asf(s[1]));
}
__device__ __forceinline__ float max32(float a, float b) {
  auto s = __builtin_amdgcn_permlane32_swap(asu(a), asu(b), false, false);
  return fmaxf(asf(s[0]), asf(s[1]));
}
__device__ __forceinline__ float row_sum(float v) {
  v += dpp<XOR8>(v); v += dpp<XOR7>(v); v += dpp<XOR2>(v); v += dpp<XOR1>(v);
  return v;
}
__device__ __forceinline__ float row_max(float v) {
  v = fmaxf(v, dpp<XOR8>(v)); v = fmaxf(v, dpp<XOR7>(v)); v = fmaxf(v, dpp<XOR2>(v)); v = fmaxf(v, dpp<XOR1>(v));
  return v;
}
__device__ __forceinline__ float fold_pair(f32x4 a, f32x4 b, int lane) {
  const float za = fold16(fold32(a[0], a[2]), fold32(a[1], a[3])), zb = fold16(fold32(b[0], b[2]), fold32(b[1], b[3]));
  const bool hi = lane & 8;
  return (hi ? zb : za) + dpp<XOR8>(hi ? za : zb);
}
__device__ __forceinline__ rsrc_t make_rsrc(u64 p, unsigned bytes) {
  return __builtin_amdgcn_make_buffer_rsrc((void*)p, 0, bytes, 0x00020000);
}
__device__ __forceinline__ u32x4 tag4(f32x4 v, unsigned tag) {
  u32x4 r;
  r[0] = __builtin_amdgcn_perm(asu(v[0]), tag, 0x07060500u);
  r[1] = __builtin_amdgcn_perm(asu(v[1]), tag, 0x07060501u);
  r[2] = __builtin_amdgcn_perm(asu(v[2]), tag, 0x07060502u);
  r[3] = __builtin_amdgcn_perm(asu(v[3]), tag, 0x07060503u);
  return r;
}
__device__ __forceinline__ unsigned next_tag(unsigned epoch) {
  const unsigned e = __builtin_amdgcn_readfirstlane(epoch);
  return ((e & 0x7f7f7f7fu) + 0x01010101u) ^ (e & 0x80808080u);
}
__device__ __forceinline__ u32x4 tag_bytes(unsigned tag) { return u32x4{tag & 255u, (tag >> 8) & 255u, (tag >> 16) & 255u, tag >> 24}; }
__device__ __forceinline__ u32x4 tag_nibbles(unsigned tag) { return u32x4{tag & 15u, (tag >> 8) & 15u, (tag >> 16) & 15u, (tag >> 24) & 15u}; }
__device__ __forceinline__ u32x4 tag4_fine(f32x4 v, unsigned tag) {
  return ((__builtin_bit_cast(u32x4, v) + 8u) & 0xfffffff0u) | tag_nibbles(tag);
}
__device__ __forceinline__ unsigned or4(u32x4 v) { return v[0] | v[1] | v[2] | v[3]; }
__device__ __forceinline__ f32x4 asf4(u32x4 v) { return __builtin_bit_cast(f32x4, v); }
__device__ __forceinline__ u32x4 ld_sc1(rsrc_t r, unsigned voff, unsigned soff) {
  return __builtin_amdgcn_raw_buffer_load_b128(r, voff, soff, AUX_SC1);
}
__device__ __forceinline__ void st_sc1(u32x4 v, rsrc_t r, unsigned voff, unsigned soff) {
  __builtin_amdgcn_raw_buffer_store_b128(v, r, voff, soff, AUX_SC1);
}
__device__ __forceinline__ bool wave_none(bool bad) { return __builtin_amdgcn_ballot_w64(bad) == 0ull; }

template <typename T> __device__ __forceinline__ T lds_ld(LAS char* l, int off) { return *(LAS T*)(l + off); }
template <typename T> __device__ __forceinline__ void lds_st(LAS char* l, int off, T v) { *(LAS T*)(l + off) = v; }
__device__ __forceinline__ void lds_flag(LAS char* l, int off, unsigned v) {
  MEM_FENCE();
  *(volatile LAS unsigned*)(l + off) = v;
  MEM_FENCE();
}
__device__ __forceinline__ u32x4 lds_peek(LAS char* l, int off) { return *(volatile LAS u32x4*)(l + off); }
__device__ __forceinline__ void lds_wait_all(LAS char* l, int off) {
  SPIN(SPIN_MAX) { const u32x4 f = lds_peek(l, off); if (wave_none((f[0] & f[1] & f[2] & f[3]) == 0u)) break; NAP(); }
  MEM_FENCE();
}
__device__ __forceinline__ bool unset4(u32x4 v) { return max(max(v[0], v[1]), max(v[2], v[3])) == 0xffffffffu; }

__device__ __forceinline__ void dma16(rsrc_t r, LAS char* dst, unsigned voff, unsigned soff) {
  __builtin_amdgcn_raw_ptr_buffer_load_lds(r, (LAS void*)dst, 16, voff, soff, 0, W_AUX);
}
__device__ __forceinline__ void tile_fetch(rsrc_t r, unsigned voff, unsigned soff, unsigned step, LAS char* img) {
#pragma unroll
  for (int i = 0; i < 4; ++i) dma16(r, img + i * 1024, voff, soff + i * step);
}
template <int GAP> __device__ __forceinline__ bf16x8 tr_frag(LAS char* a) {
  const s16x4 lo = __builtin_amdgcn_ds_read_tr16_b64_v4i16((LAS s16x4*)a);
  const s16x4 hi = __builtin_amdgcn_ds_read_tr16_b64_v4i16((LAS s16x4*)(a + GAP));
  return __builtin_bit_cast(bf16x8, __builtin_shufflevector(lo, hi, 0, 1, 2, 3, 4, 5, 6, 7));
}
__device__ __forceinline__ void tile_frags(LAS char* img, int lane, bf16x8 (&w)[4]) {
  const int row = lane >> 2, p = lane & 3;
#pragma unroll
  for (int t = 0; t < 4; ++t) w[t] = tr_frag<2048>(img + row * 128 + (((2 * t + (p >> 1)) ^ (row & 6)) << 4) + 8 * (p & 1));
}
__device__ __forceinline__ f32x4 mma(bf16x8 a, bf16x8 b, f32x4 c) { return __builtin_amdgcn_mfma_f32_16x16x32_bf16(a, b, c, 0, 0, 0); }
__device__ __forceinline__ bf16x8 asb(u32x4 v) { return __builtin_bit_cast(bf16x8, v); }

#define STAMP(k)
#define STAMP_OUT()

__global__ void __launch_bounds__(512, 2)
fused_decode(const u16* x, const u16* kc, const u16* vc, const u16* wq, const u16* wo, const u16* wg, const u16* wu,
             const u16* wd, u16* out, char* ws) {
  __shared__ __attribute__((aligned(16))) char lds_raw[L_END];
  LAS char* lds = (LAS char*)lds_raw;
  STAMP(0);
  const int bid = blockIdx.x, lane = threadIdx.x & 63, wave = __builtin_amdgcn_readfirstlane(threadIdx.x >> 6);
  const int hk = bid & 7, idx = bid >> 3, w4 = wave & 3;
  const bool loader = wave >= 4;
  const u64 p_x = (u64)x, p_k = (u64)kc, p_v = (u64)vc, p_wq = (u64)wq, p_wo = (u64)wo, p_wg = (u64)wg, p_wu = (u64)wu, p_wd = (u64)wd;
  const u64 p_out = (u64)out, p_ws = (u64)ws;
#define ARGS_IN "s"(p_x), "s"(p_k), "s"(p_v), "s"(p_wq), "s"(p_wo), "s"(p_wg), "s"(p_wu), "s"(p_wd), "s"(p_out), "s"(p_ws)
#define KV_OFF ((((idx >> 3) * 8 + hk) * SK + (SK - WIN) + (idx & 7) * 256) * 512 + w4 * 32768)
#define SEQ ((unsigned)(0x0564312075643120ull >> (((idx + 3 * hk) & 7) * 4)))
#define T_OFF (P0 + (3 - (idx >> 3)) * STG)
#define is_wo (idx >= 16)
#define late (idx >= 9)
#define is_red (idx < 7)
#define wo_rg (((hk + 1 + ((PAIR ? idx : idx >> 1) & 3)) & 7) * 8 + ((idx >> 1) & 7))
#define dn_un (bid - 72)
#define late_rg (is_wo ? wo_rg : dn_un >> 1)
#define late_ch ((is_wo ? idx : dn_un) & 1)
#define oi ((bid & 3) * DM + (bid >> 2) * 32 + (lane & 31))
#define rws make_rsrc(p_ws, WS_END)
  const unsigned l16 = lane * 16;
  unsigned tag, xv = 0, cpf = 0, t_k;

  if (!loader) {
    const unsigned iv = (lane & 16) ? 0xffffffffu : 0u;
    lds_st<u32x2>(lds, L_SYNC + lane * 8, u32x2{iv, iv});
    if (CP) cpf = __builtin_amdgcn_raw_buffer_load_b32(make_rsrc(__builtin_amdgcn_s_getpc(), CP), 0, (idx * 4 + w4) * 128, 0);
    asm volatile("" :: ARGS_IN);
    WG_BARRIER();
    t_k = (unsigned)__builtin_amdgcn_s_memrealtime();
    PIN_S(t_k);
    STAMP(1);
  }
  MEM_FENCE();
  if (loader) {
    __builtin_amdgcn_s_setprio(2);
    const bool is_q = idx < 16, is_gate = idx < 23, has_early = idx < 30;
    const int half = hk & 1;
    int et_q = idx + 16 * ((idx + (hk >> 1)) & 3), et_f = (idx - (is_gate ? 16 : 23)) * 4 + (hk >> 1);
    asm("" : "+s"(et_q), "+s"(et_f));
    const int etile = is_q ? et_q : et_f;
    const int r8 = lane >> 3;
    LAS char* img = lds + L_TILE + w4 * 4096;
    const unsigned stride = is_q ? QW * 2 : FF * 2;
    unsigned evo = r8 * stride + (((lane & 7) ^ (r8 & 6)) << 4), eso = (half * 224 + w4 * 32) * stride + etile * 128, xvo = l16;
    unsigned ebytes = has_early ? DM * stride : 0, eso2 = w4 < 3 ? eso + 128 * stride : 0x40000000u, xso = w4 * DM * 2, estep = 8 * stride;
    asm volatile("" : "+v"(evo), "+v"(xvo));
    asm volatile("" : "+s"(eso), "+s"(estep), "+s"(ebytes), "+s"(eso2), "+s"(xso));
    SCHED_FENCE();
    asm volatile("" :: ARGS_IN : "memory");
    const u32x4 xr = __builtin_amdgcn_raw_buffer_load_b128(make_rsrc(p_x, NB * DM * 2), xvo, xso, 0);
    const unsigned epoch = __builtin_amdgcn_raw_buffer_load_b32(rws, 0, WS_EPOCH, 0);
    SCHED_FENCE();
    {
      const rsrc_t rw = make_rsrc(is_q ? p_wq : (is_gate ? p_wg : p_wu), ebytes);
      tile_fetch(rw, evo, eso, estep, img);
      tile_fetch(rw, evo, eso2, estep, img + 4 * 4096);
    }
    if (B0W < 10) VM_WAIT(B0W);
    WG_BARRIER_BARE();
    const unsigned t_go = (unsigned)__builtin_amdgcn_s_memrealtime() + T_OFF;
    SCHED_FENCE();
    STAMP(1);
    {
      float v[8], s1 = 0.f, s2 = 0.f;
      const float x0 = bf_lo(__builtin_amdgcn_readfirstlane(xr[0]));
#pragma unroll
      for (int k = 0; k < 4; ++k) { v[2 * k] = bf_lo(xr[k]); v[2 * k + 1] = bf_hi(xr[k]); }
#pragma unroll
      for (int k = 0; k < 8; ++k) { const float d = v[k] - x0; s1 += d; s2 = fmaf(d, d, s2); }
      float z = fold32(lane < 56 ? s1 : 0.f, lane < 56 ? s2 : 0.f);
      z = row_sum(fold16(z, z)) * (1.0f / DM);
      const float md = asf(__builtin_amdgcn_readlane(asu(z), 0)), m2 = asf(__builtin_amdgcn_readlane(asu(z), 32));
      const float var = round_bf16(fmaxf(fmaf(-md, md, m2), 0.f)), mb = round_bf16(x0 + md);
      const float rc = __builtin_amdgcn_rcpf(round_bf16(__builtin_amdgcn_sqrtf(round_bf16(var + 1e-5f))));
      u32x4 xn;
#pragma unroll
      for (int k = 0; k < 4; ++k) {
        const unsigned d = pack_bf16(v[2 * k] - mb, v[2 * k + 1] - mb);
        xn[k] = pack_bf16(bf_lo(d) * rc, bf_hi(d) * rc);
      }
      lds_st<u32x4>(lds, L_XN + w4 * 1024 + lane * 16, xn);
      lds_flag(lds, F_XN + w4 * 4, 1u);
      lds_wait_all(lds, F_XN);
      tag = next_tag(epoch);
      PIN_S(tag);
      if (SYNC && idx == 24 && w4 == 0 && lane == 0) st_sc1(tag4(f32x4{asf(t_go << 8), 0.f, 0.f, 0.f}, tag), rws, 0, WS_TS + hk * 16);
      const int bt = lane & 3, g = lane >> 4, jj = (lane >> 2) & 3;
      const int xo = L_XN + bt * 1024 + (half * 224 + w4 * 32 + 4 * g) * 2;
      const u32x2 a0 = lds_ld<u32x2>(lds, xo), a1 = lds_ld<u32x2>(lds, xo + 32);
      const u32x2 a2 = lds_ld<u32x2>(lds, xo + 256), a3 = lds_ld<u32x2>(lds, xo + 288);
      const u32x4 act0 = {a0[0], a0[1], a1[0], a1[1]}, act1 = {a2[0], a2[1], a3[0], a3[1]}, zero = {0u, 0u, 0u, 0u};
      VM_DRAIN();
      bf16x8 wf0[4], wf1[4];
      tile_frags(img, lane, wf0);
      f32x4 acc = {0.f, 0.f, 0.f, 0.f};
      if (w4 < 3) {
        tile_frags(img + 4 * 4096, lane, wf1);
#pragma unroll
        for (int t = 0; t < 4; ++t) acc = mma(wf1[t], asb(jj == t ? act1 : zero), acc);
      }
#pragma unroll
      for (int t = 0; t < 4; ++t) acc = mma(wf0[t], asb(jj == t ? act0 : zero), acc);
      lds_st<f32x4>(img, bt * 256 + ((lane & 12) + g) * 16, acc);
      lds_flag(lds, F_EP + w4 * 4, 1u);
      if (w4 == 3 && has_early) {
        const unsigned voff = is_q ? WS_QP + (etile >> 3) * 16384 + (lane >> 4) * 4096 + (etile & 6) * 512 + half * 512 + (etile & 1) * 256 + (lane & 15) * 16
                                   : WS_GP + etile * 4096 + (is_gate ? 0 : 2048) + half * 1024 + l16;
        lds_wait_all(lds, F_EP);
        f32x4 sum = lds_ld<f32x4>(lds, L_TILE + lane * 16);
#pragma unroll
        for (int p = 1; p < 4; ++p) sum += lds_ld<f32x4>(lds, L_TILE + p * 4096 + lane * 16);
        st_sc1(tag4_fine(sum, tag), rws, voff, 0);
      }
    }
    STAMP(2);
    __builtin_amdgcn_s_setprio(0);
    const rsrc_t rv = make_rsrc(p_v + KV_OFF, 32768);
    const unsigned hf = lane >> 5;
    unsigned vo0 = (lane ^ hf) * 16, vo1 = (lane ^ (2 + hf)) * 16, vo2 = (lane ^ (4 + hf)) * 16, vo3 = (lane ^ (6 + hf)) * 16;
    const int lcol = late_ch * 4 + w4;
    const bool has_tile = late && lcol < 7;
    const rsrc_t rl = make_rsrc(is_wo ? p_wo : p_wd, (is_wo ? QW : FF) * DM * 2);
    unsigned lvo = r8 * (DM * 2) + (((lane & 7) ^ (r8 >> 1)) << 4), lso = late_rg * (64 * DM * 2) + lcol * 128;
    unsigned rvo = (((lane >> 3) & 1) * 4 + (lane >> 4)) * RP_BOX + (lane & 7) * 16;
    unsigned rso = WS_RP + lcol * 8 * RP_BOX + ((is_wo ? 0 : 64) + late_rg) * 128;
    LAS char *aa[4], *ta[4];
#pragma unroll
    for (int t = 0; t < 4; ++t) {
      aa[t] = lds + ((lane & 3) == t ? L_A + ((lane >> 2) & 3) * 64 + (lane >> 4) * 16 : L_ZERO);
      ta[t] = img + (lane >> 2) * 128 + (((lane & 2) * 2 + (t ^ ((lane >> 3) & 3))) << 4) + 8 * (lane & 1);
      PIN_V(aa[t]); PIN_V(ta[t]);
    }
    PIN_V(vo0); PIN_V(vo1); PIN_V(vo2); PIN_V(vo3); PIN_V(lvo); PIN_V(rvo); PIN_S(lso); PIN_S(rso);
    int dt8 = DT * 8 + (3 - (idx >> 3)) * (STE - STG);
    if (SYNC) {
      PACE(SYAT * DT);
      const u32x4 ts = ld_sc1(rws, (lane & 7) * 16, WS_TS) ^ tag_bytes(tag);
      const unsigned org = t_go - (3 - (idx >> 3)) * STG;
      float d = (or4(ts) & 255u) ? 0.f : fminf(fmaxf((float)((int)(ts[0] - (org << 8)) >> 8), -160.f), 160.f);
      d += dpp<XOR7>(d);
      d += dpp<XOR2>(d);
      d += dpp<XOR1>(d);
      dt8 += min(__builtin_amdgcn_readfirstlane((int)(d * 0.125f)) + SYB, SYUP);
    }
    WG_BARRIER();
    STAMP(3);
    int t_v = 7 * DT * 8;
    _Pragma("nounroll") for (int g = 0; g < 8; ++g) {
      const unsigned so = ((SEQ >> (4 * g)) & 7) << 12;
      t_v += dt8;
      PACE(t_v >> 3);
      LAS void* dst = (LAS void*)(lds + L_V + (g * 4 + w4) * 4096);
      __builtin_amdgcn_raw_ptr_buffer_load_lds(rv, dst, 16, vo0, so, 0, KV_AUX);
      __builtin_amdgcn_raw_ptr_buffer_load_lds(rv, dst, 16, vo1, so, 1024, KV_AUX);
      __builtin_amdgcn_raw_ptr_buffer_load_lds(rv, dst, 16, vo2, so, 2048, KV_AUX);
      __builtin_amdgcn_raw_ptr_buffer_load_lds(rv, dst, 16, vo3, so, 3072, KV_AUX);
      VM_WAIT(8);
      lds_flag(lds, L_PROG + w4 * 4, g + 1);
    }
    STAMP(4);
    VM_WAIT(4);
    lds_flag(lds, L_PROG + w4 * 4, 9);
    const bool before = has_tile && is_wo;
    if (!before) { VM_WAIT(0); STAMP(5); WG_BARRIER_BARE(); }
    if (has_tile) {
      SPIN(SPIN_MAX) { const u32x4 f = lds_peek(lds, F_PV); if (wave_none((f[0] & f[1]) == 0u)) break; }
      PACE((t_v >> 3) + (is_wo ? PL : PLD));
      tile_fetch(rl, lvo, lso, 8 * DM * 2, img);
      tile_fetch(rl, lvo, lso + 32 * DM * 2, 8 * DM * 2, img + 4 * 4096);
      if (before) {
        VM_WAIT(8);
        STAMP(5);
        WG_BARRIER_BARE();
        VM_WAIT(FPW);
        lds_flag(lds, F_T + w4 * 4, 1u);
      }
      VM_DRAIN();
      bf16x8 wf[8];
#pragma unroll
      for (int t = 0; t < 4; ++t) { wf[t] = tr_frag<2048>(ta[t]); wf[4 + t] = tr_frag<2048>(ta[t] + 4 * 4096); }
      STAMP(8);
      if (is_wo) WG_BARRIER();
      STAMP(10);
      u32x4 act[8];
#pragma unroll
      for (int t = 0; t < 4; ++t) { act[2 * t] = lds_ld<u32x4>(aa[t], 0); act[2 * t + 1] = lds_ld<u32x4>(aa[t], 256); }
      SCHED_FENCE();
      f32x4 acc = {0.f, 0.f, 0.f, 0.f};
#pragma unroll
      for (int t = 0; t < 4; ++t) { acc = mma(asb(act[2 * t]), wf[t], acc); acc = mma(asb(act[2 * t + 1]), wf[4 + t], acc); }
      st_sc1(tag4(acc, tag), rws, rvo, rso);
      STAMP(11);
    } else if (is_wo) {
      WG_BARRIER_BARE();
    }
  }
  int role = wave;
  PIN_S(role);
  if (role < 4) {
    u32x4 kr[32], qv;
#define qso (WS_QP + hk * 16384 + (idx >> 3) * 4096 + w4 * 1024)
    const unsigned kvo = (lane & 7) * 512 + ((lane >> 3) & 1) * 64 + (lane >> 4) * 16, seq = SEQ;
    const rsrc_t rk = make_rsrc(p_k + KV_OFF, 32768);
    const unsigned t_go = t_k + T_OFF;
#pragma unroll
    for (int g = 0; g < 8; ++g) {
      const unsigned so = ((seq >> (4 * g)) & 7) << 12;
      PACE(g * DT);
#pragma unroll
      for (int i = 0; i < 4; ++i) kr[g * 4 + i] = __builtin_amdgcn_raw_buffer_load_b128(rk, kvo + i * 128, so, KV_AUX);
    }
    SCHED_FENCE();
    const unsigned kepoch = __builtin_amdgcn_raw_buffer_load_b32(rws, 0, WS_EPOCH, 0);
    qv = ld_sc1(rws, l16, qso);
    STAMP(2);
    WG_BARRIER_BARE();
    STAMP(3);
    tag = next_tag(kepoch);
    asm volatile("" : "+s"(tag) : "v"(cpf));
    SPIN(SPIN_MAX) {
      qv ^= tag_nibbles(tag);
      if (wave_none((or4(qv) & 15u) != 0u)) break;
      MEM_FENCE();
      qv = ld_sc1(rws, l16, qso);
    }
    lds_st<unsigned>(lds, L_Q + w4 * 256 + (lane & 31) * 8 + (lane >> 5) * 4,
                     pack_bf16(fold32(asf(qv[0]), asf(qv[2])), fold32(asf(qv[1]), asf(qv[3]))));
    lds_flag(lds, F_Q + w4 * 4, 1u);
    lds_wait_all(lds, F_Q);
    STAMP(4);
    float m0, m1;
    {
      const int qa = L_Q + (lane & 1) * 512 + ((lane >> 1) & 1) * 64 + (lane >> 4) * 16;
      f32x4 c0, c1;
      {
        u32x4 qf[4];
#pragma unroll
        for (int s = 0; s < 4; ++s) qf[s] = lds_ld<u32x4>(lds, qa + s * 128);
        f32x4 c[8];
#pragma unroll
        for (int t = 0; t < 8; ++t) c[t] = mma(asb(qf[0]), asb(kr[t * 4]), f32x4{0.f, 0.f, 0.f, 0.f});
#pragma unroll
        for (int s = 1; s < 4; ++s)
#pragma unroll
          for (int t = 0; t < 8; ++t) c[t] = mma(asb(qf[s]), asb(kr[t * 4 + s]), c[t]);
        const bool g1 = lane & 16, g2 = lane & 32;
        c0 = g2 ? (g1 ? c[3] : c[2]) : (g1 ? c[1] : c[0]);
        c1 = g2 ? (g1 ? c[7] : c[6]) : (g1 ? c[5] : c[4]);
      }
      const bool cb = lane & 8;
      const float r0 = (cb ? c1[2] : c0[0]) + dpp<XOR8>(cb ? c0[2] : c1[0]);
      const float r1 = (cb ? c1[3] : c0[1]) + dpp<XOR8>(cb ? c0[3] : c1[1]);
      const unsigned sp = pack_bf16(r0, r1);
      const float s0 = bf_lo(sp) * 0.0625f, s1 = bf_hi(sp) * 0.0625f;
      float mw = max32(s0, s1);
      mw = row_max(max16(mw, mw));
      *(volatile LAS float*)(lds + L_MX + (lane >> 5) * 16 + w4 * 4) = mw;
      u32x4 x0, x1;
      SPIN(SPIN_MAX) { x0 = lds_peek(lds, L_MX); x1 = lds_peek(lds, L_MX + 16); if (wave_none(unset4(x0) | unset4(x1))) break; }
      m0 = fmaxf(fmaxf(asf(x0[0]), asf(x0[1])), fmaxf(asf(x0[2]), asf(x0[3])));
      m1 = fmaxf(fmaxf(asf(x1[0]), asf(x1[1])), fmaxf(asf(x1[2]), asf(x1[3])));
      const float p0 = __builtin_amdgcn_exp2f((s0 - m0) * LOG2E), p1 = __builtin_amdgcn_exp2f((s1 - m1) * LOG2E);
      const unsigned pk = pack_bf16(p0, p1);
      const int po = L_P + (((lane >> 3) & 1) * 4 + (lane >> 4)) * 64 + ((w4 & 1) * 2 + ((lane >> 2) & 1)) * 16 + ((w4 >> 1) * 4 + (lane & 3)) * 2;
      lds_st<u16>(lds, po, (u16)pk);
      lds_st<u16>(lds, po + 512, (u16)(pk >> 16));
      float l = fold32(p0, p1);
      l = row_sum(fold16(l, l));
      MEM_FENCE();
      *(volatile LAS float*)(lds + L_LS + (lane >> 5) * 16 + w4 * 4) = l;
    }
    u32x4 ls;
    SPIN(SPIN_MAX) { ls = lds_peek(lds, L_LS + (lane >> 5) * 16); if (wave_none(unset4(ls))) break; }
    MEM_FENCE();
    STAMP(5);
    u32x4 tb = tag_bytes(tag);
    unsigned oso = WS_OP + wo_rg * 8192 + w4 * 2048;
    LAS char* ad = lds + L_A + (lane & 4) * 64 + w4 * 64 + ((lane & 1) * 2 + ((lane >> 3) & 1)) * 16 + ((lane & 2) * 2 + (lane >> 4)) * 2;
    LAS char* fa = lds + L_F + w4 * 32 + (lane >> 3) * 4;
    PIN_S(tb); PIN_S(oso); PIN_V(ad); PIN_V(fa);
    if (w4 < 2) {
      const int g = lane >> 4, row = lane >> 2, p = lane & 3, tl = lane & 7;
      LAS char* va[8];
#pragma unroll
      for (int t = 0; t < 8; ++t) va[t] = lds + L_V + row * 512 + ((w4 * 16 + (p >> 1) * 8 + (t ^ (row & 7))) << 4) + 8 * (p & 1);
      LAS char* pa = lds + L_P + ((lane >> 3) & 1) * 512 + g * 16;
      unsigned ovo = (((lane >> 3) & 1) * 4 + (g >> 1)) * 8192 + (g & 1) * 1024 + tl * 16;
      unsigned pso = WS_OP + (hk * 8 + w4 * 2) * 8192 + (idx >> 3) * 2048 + (idx & 7) * 128;
      PIN_V(ovo); PIN_S(pso);
      f32x4 o = {0.f, 0.f, 0.f, 0.f};
      u32x4 pm[8];
      {
        const u32x4 pl = lds_ld<u32x4>(pa, 0);
#pragma unroll
        for (int t = 0; t < 8; ++t) pm[t] = tl == t ? pl : u32x4{0u, 0u, 0u, 0u};
      }
      _Pragma("nounroll") for (int ks = 0; ks < 7; ++ks) {
        SPIN(SPIN_MAX) {
          const u32x4 pr = lds_peek(lds, L_PROG);
          if (wave_none(min(min(pr[0], pr[1]), min(pr[2], pr[3])) <= (unsigned)(ks + 2))) break;
        }
        lds_flag(lds, F_PV + w4 * 4, ks >= 2 ? 1u : 0u);
        pa += 64;
        const u32x4 pl = lds_ld<u32x4>(pa, 0);
        bf16x8 vf[8];
#pragma unroll
        for (int t = 0; t < 8; ++t) vf[t] = tr_frag<8192>(va[t]);
        SCHED_FENCE();
#pragma unroll
        for (int t = 0; t < 8; ++t) {
          o = mma(vf[t], asb(pm[t]), o);
          SCHED_FENCE();
          pm[t] = tl == t ? pl : u32x4{0u, 0u, 0u, 0u};
          va[t] += 16384;
          PIN_V(pm[t]); PIN_V(va[t]);
          SCHED_FENCE();
        }
      }
      STAMP(6);
      WG_BARRIER_BARE();
      {
        bf16x8 vf[8];
#pragma unroll
        for (int t = 0; t < 8; ++t) vf[t] = tr_frag<8192>(va[t]);
        SCHED_FENCE();
#pragma unroll
        for (int t = 0; t < 8; ++t) o = mma(vf[t], asb(pm[t]), o);
      }
      st_sc1(tag4(o, tag), rws, ovo, pso);
      STAMP(7);
    } else if (w4 == 2) {
      if (late && !is_wo) {
        u32x4 gv[4];
        SPIN(SPIN_MAX) {
          unsigned bad = 0;
#pragma unroll
          for (int u = 0; u < 4; ++u) gv[u] = ld_sc1(rws, l16 + u * 1024, WS_GP + (dn_un >> 1) * 4096);
#pragma unroll
          for (int u = 0; u < 4; ++u) { gv[u] ^= tag_nibbles(tag); bad |= or4(gv[u]); }
          if (wave_none((bad & 15u) != 0u)) break;
          MEM_FENCE();
        }
        const f32x4 gs = asf4(gv[0]) + asf4(gv[1]), us = asf4(gv[2]) + asf4(gv[3]);
        u32x2 hv;
#pragma unroll
        for (int e = 0; e < 2; ++e) {
          const unsigned gt = pack_bf16(gs[2 * e], gs[2 * e + 1]), up = pack_bf16(us[2 * e], us[2 * e + 1]);
          const float g0 = bf_lo(gt), g1 = bf_hi(gt);
          hv[e] = pack_bf16(g0 * (g0 > 0.f ? 1.f : 0.f) * bf_lo(up), g1 * (g1 > 0.f ? 1.f : 0.f) * bf_hi(up));
        }
        lds_st<u32x2>(lds, L_A + (lane & 8) * 32 + (lane >> 4) * 64 + (lane & 3) * 16 + (lane & 4) * 2, hv);
      } else if (is_red) {
        xv = __builtin_amdgcn_raw_buffer_load_b16(make_rsrc(p_x, NB * DM * 2), oi * 2, 0, 0);
        PIN_V(xv);
      }
      WG_BARRIER();
    } else {
      const float lsum = (asf(ls[0]) + asf(ls[1])) + (asf(ls[2]) + asf(ls[3]));
      const float l1 = asf(__builtin_amdgcn_readlane(asu(lsum), 32));
      if (lane == 0) st_sc1(tag4(f32x4{m0, lsum, m1, l1}, tag), rws, 0, WS_ML + (hk * 32 + idx) * 16);
      const unsigned mso = is_wo ? WS_ML + (wo_rg >> 3) * 512 : WS_END;
      SPIN(SPIN_MAX) { if (wave_none(*(volatile LAS unsigned*)(lds + L_PROG) < (unsigned)MLAT)) break; __builtin_amdgcn_s_sleep(4); }
      u32x4 mv = ld_sc1(rws, (lane & 31) * 16, mso);
      WG_BARRIER_BARE();
      mv ^= tb;
      PIN_V(mv);
      if (is_wo) {
        SPIN(SPIN_MAX) {
          if (wave_none((or4(mv) & 255u) != 0u)) break;
          MEM_FENCE();
          mv = ld_sc1(rws, (lane & 31) * 16, mso) ^ tb;
        }
        const float m = asf((idx & 8) ? mv[2] : mv[0]);
        float mx = fmaxf(m, dpp<XOR7>(m));
        mx = fmaxf(mx, dpp<XOR2>(mx));
        mx = fmaxf(mx, dpp<XOR1>(mx));
        const float e = __builtin_amdgcn_exp2f((m - mx) * LOG2E);
        float lt = e * asf((idx & 8) ? mv[3] : mv[1]);
        lt += dpp<XOR7>(lt);
        lt += dpp<XOR2>(lt);
        lt += dpp<XOR1>(lt);
        *(volatile LAS unsigned*)(lds + L_F + (lane & 31) * 4) = asu(e * __builtin_amdgcn_rcpf(lt)) & 0xfffffffeu;
      } else if (bid == 56) {
        SPIN(SPIN_MAX) {
          unsigned bad = 0;
#pragma unroll
          for (int u = 0; u < 4; ++u) bad |= or4(ld_sc1(rws, l16 + u * 1024, WS_ML) ^ tb);
          if (wave_none((bad & 255u) != 0u)) break;
          MEM_FENCE();
        }
        if (lane == 0) __builtin_amdgcn_raw_buffer_store_b32(tag, rws, 0, WS_EPOCH, AUX_SC1);
      }
    }
    if (is_wo) {
      float f;
      SPIN(SPIN_MAX) {
        f = *(volatile LAS float*)fa;
        if (wave_none((asu(f) == 0xffffffffu) | (*(volatile LAS unsigned*)(lds + F_T) == 0u))) break;
      }
      STAMP(8);
      u32x4 o0, o1;
      SPIN(SPIN_MAX) {
        MEM_FENCE();
        o0 = ld_sc1(rws, l16, oso) ^ tb;
        o1 = ld_sc1(rws, l16 + 1024, oso) ^ tb;
        if (wave_none(((or4(o0) | or4(o1)) & 255u) != 0u)) break;
      }
      STAMP(9);
      const float o = fold_pair(asf4(o0) * f, asf4(o1) * f, lane);
      lds_st<u16>(ad, 0, (u16)pack_bf16(o, o));
      WG_BARRIER();
    }
  }

  if (is_red) {
    const u32x4 tb = tag_bytes(tag);
    LAS char* fw = lds + L_FIN + ((lane >> 4) * 8 + (lane & 7)) * 64 + wave * 4 + (lane & 8) * 4;
    LAS char* fr = lds + L_FIN + (lane & 31) * 64 + (lane >> 5) * 32;
    unsigned oo = (lane & 32) ? 0x7fffffffu : oi * 2;
    const bool dn_lane = wave * 64 + lane < 28 * 8;
    unsigned dvo = dn_lane ? l16 + 8192 : l16;
    float xf = bf_lo(xv), dw = dn_lane ? 1.f : 0.f;
    PIN_V(fw); PIN_V(fr); PIN_V(oo); PIN_V(xf); PIN_V(dvo); PIN_V(dw);
    const rsrc_t ro = make_rsrc(p_out, NB * DM * 2);
    const unsigned so = WS_RP + bid * RP_BOX + wave * 1024;
    u32x4 a0, a1;
    if (RD) __builtin_amdgcn_s_sleep(RD);
    STAMP(8);
    SPIN(SPIN_MAX) {
      MEM_FENCE();
      a1 = ld_sc1(rws, dvo, so) ^ tb;
      a0 = ld_sc1(rws, l16, so) ^ tb;
      if (wave_none(((or4(a0) | or4(a1)) & 255u) != 0u)) break;
    }
    STAMP(9);
    lds_st<float>(fw, 0, fold_pair(asf4(a0), asf4(a1) * dw, lane));
    WG_BARRIER();
    STAMP(10);
    if (wave == 2) {
      const f32x4 t = lds_ld<f32x4>(fr, 0) + lds_ld<f32x4>(fr, 16);
      const float u = round_bf16((t[0] + t[1]) + (t[2] + t[3]));
      auto sw = __builtin_amdgcn_permlane32_swap(asu(u), asu(u), false, false);
      const float sv = asf(sw[0]), dn = asf(sw[1]);
      const float g = round_bf16(dn * __builtin_amdgcn_rcpf(1.0f + __builtin_amdgcn_exp2f(-dn * LOG2E)));
      const float res = xf + round_bf16(sv * g);
      __builtin_amdgcn_raw_buffer_store_b16((u16)pack_bf16(res, res), ro, oo, 0, 0);
      STAMP(11);
    }
  }
  STAMP_OUT();
}

}

extern "C" void kernel_launch(void* const* d_in, const int* in_sizes, int n_in, void* d_out, int out_size, void* d_ws,
                              size_t ws_size, hipStream_t stream) {
  fused_decode<<<256, 512, 0, stream>>>((const u16*)d_in[0], (const u16*)d_in[1], (const u16*)d_in[2], (const u16*)d_in[3],
                                        (const u16*)d_in[4], (const u16*)d_in[6], (const u16*)d_in[7], (const u16*)d_in[8],
                                        (u16*)d_out, (char*)d_ws + WS_OFF);
}
```

```cpp
#define PROBE 1
#define PROBE_X 2
#include <hip/hip_runtime.h>
#include <hip/hip_bf16.h>
#include <stdint.h>

#define DIAG 0
#define CP 10500
#define MLAT 7
#define RD 0
#define FPW 4
#define P0 72
#define DT 56
#define PL 60
#define PLD 80
#define B0W 10
#define STG 10
#define PAIR 0
#define EMAP 0
#define EAUX 16
#define RSL 0
#define OSL 0
#define WS_OFF 0
#define KV_AUX 2
#define W_AUX 2

namespace {

typedef unsigned short u16;
typedef unsigned long long u64;
typedef unsigned u32x2 __attribute__((ext_vector_type(2)));
typedef unsigned u32x4 __attribute__((ext_vector_type(4)));
typedef unsigned u32x16 __attribute__((ext_vector_type(16)));
typedef float f32x2 __attribute__((ext_vector_type(2)));
typedef float f32x4 __attribute__((ext_vector_type(4)));
typedef short s16x4 __attribute__((ext_vector_type(4)));
typedef __bf16 bf16x2 __attribute__((ext_vector_type(2)));
typedef __bf16 bf16x8 __attribute__((ext_vector_type(8)));
#define LAS __attribute__((address_space(3)))
typedef __amdgpu_buffer_rsrc_t rsrc_t;

constexpr int NB = 4, DM = 448, SK = 8192, WIN = 2048, FF = 1792, QW = 4096;
constexpr int AUX_SC1 = 16;
constexpr unsigned SPIN_MAX = 1u << 22;
constexpr float LOG2E = 1.4426950408889634f;

constexpr unsigned WS_EPOCH = 0;
constexpr unsigned WS_QP = 4096;
constexpr unsigned WS_GP = WS_QP + 8 * 4 * 4096;
constexpr unsigned WS_OP = WS_GP + 28 * 4096;
constexpr unsigned WS_ML = WS_OP + 64 * 8192;
constexpr unsigned WS_RP = WS_ML + 4096;
constexpr unsigned RP_BOX = 92 * 128;
constexpr unsigned WS_END = WS_RP + 56 * RP_BOX;
constexpr unsigned WS_DIAG = 8u << 20;

constexpr int L_V = 0;
constexpr int L_TILE = L_V;
constexpr int L_XN = L_V + 32768;
constexpr int L_FIN = L_V;
constexpr int L_Q = 131072;
constexpr int L_P = L_Q + 1024;
constexpr int L_A = L_P + 1024;
constexpr int L_SYNC = L_A + 512;
constexpr int L_ZERO = L_SYNC;
constexpr int F_XN = L_SYNC + 16, F_EP = L_SYNC + 32, F_Q = L_SYNC + 48, L_PROG = L_SYNC + 64, F_PV = L_SYNC + 80, F_T = L_SYNC + 96;
constexpr int L_F = L_SYNC + 128;
constexpr int L_MX = L_SYNC + 384;
constexpr int L_LS = L_MX + 32;
constexpr int L_END = L_SYNC + 512;

#define SCHED_FENCE() __builtin_amdgcn_sched_barrier(0)
#define MEM_FENCE() asm volatile("" ::: "memory")
#define WG_BARRIER() do { SCHED_FENCE(); asm volatile("s_waitcnt lgkmcnt(0)\n\ts_barrier" ::: "memory"); SCHED_FENCE(); } while (0)
#define WG_BARRIER_BARE() do { SCHED_FENCE(); asm volatile("s_barrier" ::: "memory"); SCHED_FENCE(); } while (0)
#define VM_WAIT(n) do { SCHED_FENCE(); asm volatile("s_waitcnt vmcnt(%0)" :: "i"(n) : "memory"); SCHED_FENCE(); } while (0)
#define VM_DRAIN() do { SCHED_FENCE(); __builtin_amdgcn_s_waitcnt(0x0F70); SCHED_FENCE(); MEM_FENCE(); } while (0)
#define NAP() __builtin_amdgcn_s_sleep(1)
#define PACE(t) do { SCHED_FENCE(); pace_until(t_go + (t)); SCHED_FENCE(); } while (0)
#define PIN_V(x) asm volatile("" : "+v"(x))
#define PIN_S(x) asm volatile("" : "+s"(x))
#define LSPIN() _Pragma("nounroll") for (;;)
#define SPIN(limit) _Pragma("nounroll") for (unsigned s_ = 0; s_ < (limit); ++s_)

__device__ __forceinline__ void pace_until(unsigned when) {
  asm volatile("1:\n\ts_memrealtime vcc\n\ts_waitcnt lgkmcnt(0)\n\ts_sub_i32 vcc_lo, vcc_lo, %0\n\ts_cmp_lt_i32 vcc_lo, 0\n\ts_cbranch_scc1 1b"
               :: "s"(when) : "vcc", "scc", "memory");
}
__device__ __forceinline__ float asf(unsigned u) { return __builtin_bit_cast(float, u); }
__device__ __forceinline__ unsigned asu(float f) { return __builtin_bit_cast(unsigned, f); }
__device__ __forceinline__ unsigned pack_bf16(float lo, float hi) {
  f32x2 f = {lo, hi};
  return __builtin_bit_cast(unsigned, __builtin_convertvector(f, bf16x2));
}
__device__ __forceinline__ float round_bf16(float v) { return asf(pack_bf16(0.f, v)); }
__device__ __forceinline__ float bf_lo(unsigned u) { return asf(u << 16); }
__device__ __forceinline__ float bf_hi(unsigned u) { return asf(u & 0xffff0000u); }
template <int CTRL> __device__ __forceinline__ float dpp(float x) {
  return asf((unsigned)__builtin_amdgcn_mov_dpp((int)asu(x), CTRL, 0xf, 0xf, true));
}
constexpr int XOR1 = 0xB1, XOR2 = 0x4E, XOR7 = 0x141, XOR8 = 0x128;
__device__ __forceinline__ float fold16(float a, float b) {
  auto s = __builtin_amdgcn_permlane16_swap(asu(a), asu(b), false, false);
  return asf(s[0]) + asf(s[1]);
}
__device__ __forceinline__ float fold32(float a, float b) {
  auto s = __builtin_amdgcn_permlane32_swap(asu(a), asu(b), false, false);
  return asf(s[0]) + asf(s[1]);
}
__device__ __forceinline__ float max16(float a, float b) {
  auto s = __builtin_amdgcn_permlane16_swap(asu(a), asu(b), false, false);
  return fmaxf(asf(s[0]), asf(s[1]));
}
__device__ __forceinline__ float max32(float a, float b) {
  auto s = __builtin_amdgcn_permlane32_swap(asu(a), asu(b), false, false);
  return fmaxf(asf(s[0]), asf(s[1]));
}
__device__ __forceinline__ float row_sum(float v) {
  v += dpp<XOR8>(v); v += dpp<XOR7>(v); v += dpp<XOR2>(v); v += dpp<XOR1>(v);
  return v;
}
__device__ __forceinline__ float row_max(float v) {
  v = fmaxf(v, dpp<XOR8>(v)); v = fmaxf(v, dpp<XOR7>(v)); v = fmaxf(v, dpp<XOR2>(v)); v = fmaxf(v, dpp<XOR1>(v));
  return v;
}
__device__ __forceinline__ float fold_pair(f32x4 a, f32x4 b) {
  const float a0 = fold32(a[0], a[2]), a1 = fold32(a[1], a[3]), b0 = fold32(b[0], b[2]), b1 = fold32(b[1], b[3]);
  SCHED_FENCE();
  const float za = fold16(a0, a1), zb = fold16(b0, b1);
  const bool hi = __builtin_amdgcn_inverse_ballot_w64(0xff00ff00ff00ff00ull);
  return (hi ? zb : za) + dpp<XOR8>(hi ? za : zb);
}
__device__ __forceinline__ rsrc_t make_rsrc(u64 p, unsigned bytes) {
  return __builtin_amdgcn_make_buffer_rsrc((void*)p, 0, bytes, 0x00020000);
}
__device__ __forceinline__ u32x4 tag4(f32x4 v, unsigned tag) {
  u32x4 r;
  r[0] = __builtin_amdgcn_perm(asu(v[0]), tag, 0x07060500u);
  r[1] = __builtin_amdgcn_perm(asu(v[1]), tag, 0x07060501u);
  r[2] = __builtin_amdgcn_perm(asu(v[2]), tag, 0x07060502u);
  r[3] = __builtin_amdgcn_perm(asu(v[3]), tag, 0x07060503u);
  return r;
}
__device__ __forceinline__ unsigned next_tag(unsigned epoch) {
  const unsigned e = __builtin_amdgcn_readfirstlane(epoch);
  return ((e & 0x7f7f7f7fu) + 0x07050301u) ^ (e & 0x80808080u);
}
__device__ __forceinline__ u32x4 tag_bytes(unsigned tag) { return u32x4{tag & 255u, (tag >> 8) & 255u, (tag >> 16) & 255u, tag >> 24}; }
__device__ __forceinline__ u32x4 tag_nibbles(unsigned tag) { return u32x4{tag & 15u, (tag >> 8) & 15u, (tag >> 16) & 15u, (tag >> 24) & 15u}; }
__device__ __forceinline__ u32x4 tag4_fine(f32x4 v, unsigned tag) {
  return ((__builtin_bit_cast(u32x4, v) + 8u) & 0xfffffff0u) | tag_nibbles(tag);
}
__device__ __forceinline__ unsigned or4(u32x4 v) { return v[0] | v[1] | v[2] | v[3]; }
__device__ __forceinline__ f32x4 asf4(u32x4 v) { return __builtin_bit_cast(f32x4, v); }
__device__ __forceinline__ u32x4 ld_sc1(rsrc_t r, unsigned voff, unsigned soff) {
  return __builtin_amdgcn_raw_buffer_load_b128(r, voff, soff, AUX_SC1);
}
__device__ __forceinline__ void st_sc1(u32x4 v, rsrc_t r, unsigned voff, unsigned soff) {
  __builtin_amdgcn_raw_buffer_store_b128(v, r, voff, soff, AUX_SC1);
}
__device__ __forceinline__ bool wave_none(bool bad) { return __builtin_amdgcn_ballot_w64(bad) == 0ull; }

template <typename T> __device__ __forceinline__ T lds_ld(LAS char* l, int off) { return *(LAS T*)(l + off); }
template <typename T> __device__ __forceinline__ void lds_st(LAS char* l, int off, T v) { *(LAS T*)(l + off) = v; }
__device__ __forceinline__ void lds_flag(LAS char* l, int off, unsigned v) {
  MEM_FENCE();
  *(volatile LAS unsigned*)(l + off) = v;
  MEM_FENCE();
}
__device__ __forceinline__ u32x4 lds_peek(LAS char* l, int off) { return *(volatile LAS u32x4*)(l + off); }
__device__ __forceinline__ void lds_wait_all(LAS char* l, int off) {
  LSPIN() { const u32x4 f = lds_peek(l, off); if (wave_none((f[0] & f[1] & f[2] & f[3]) == 0u)) break; NAP(); }
  MEM_FENCE();
}
__device__ __forceinline__ bool unset4(u32x4 v) { return max(max(v[0], v[1]), max(v[2], v[3])) == 0xffffffffu; }

__device__ __forceinline__ void dma16(rsrc_t r, LAS char* dst, unsigned voff, unsigned soff) {
  __builtin_amdgcn_raw_ptr_buffer_load_lds(r, (LAS void*)dst, 16, voff, soff, 0, W_AUX);
}
__device__ __forceinline__ void tile_fetch(rsrc_t r, unsigned voff, unsigned soff, unsigned step, LAS char* img) {
#pragma unroll
  for (int i = 0; i < 4; ++i) dma16(r, img + i * 1024, voff, soff + i * step);
}
template <int GAP> __device__ __forceinline__ bf16x8 tr_frag(LAS char* a) {
  const s16x4 lo = __builtin_amdgcn_ds_read_tr16_b64_v4i16((LAS s16x4*)a);
  const s16x4 hi = __builtin_amdgcn_ds_read_tr16_b64_v4i16((LAS s16x4*)(a + GAP));
  return __builtin_bit_cast(bf16x8, __builtin_shufflevector(lo, hi, 0, 1, 2, 3, 4, 5, 6, 7));
}
__device__ __forceinline__ void tile_frags(LAS char* img, int lane, bf16x8 (&w)[4]) {
  const int row = lane >> 2, p = lane & 3;
#pragma unroll
  for (int t = 0; t < 4; ++t) w[t] = tr_frag<2048>(img + row * 128 + (((2 * t + (p >> 1)) ^ (row & 6)) << 4) + 8 * (p & 1));
}
__device__ __forceinline__ f32x4 mma(bf16x8 a, bf16x8 b, f32x4 c) { return __builtin_amdgcn_mfma_f32_16x16x32_bf16(a, b, c, 0, 0, 0); }
__device__ __forceinline__ bf16x8 asb(u32x4 v) { return __builtin_bit_cast(bf16x8, v); }

#define STAMP(k) do { if ((k) == PROBE) { SCHED_FENCE(); pst = __builtin_amdgcn_s_memrealtime(); SCHED_FENCE(); } } while (0)
#define STAMP_OUT() do { if (wave == 4 && ((PROBE_X >> hk) & 1)) while (__builtin_amdgcn_s_memrealtime() < pst + 3000) __builtin_amdgcn_s_sleep(2); } while (0)

__global__ void __launch_bounds__(512, 2)
fused_decode(const u16* x, const u16* kc, const u16* vc, const u16* wq, const u16* wo, const u16* wg, const u16* wu,
             const u16* wd, u16* out, char* ws) {
  __shared__ __attribute__((aligned(16))) char lds_raw[L_END];
  LAS char* lds = (LAS char*)lds_raw;
  u64 pst = 0;
  STAMP(0);
  const int bid = blockIdx.x, lane = threadIdx.x & 63, wave = __builtin_amdgcn_readfirstlane(threadIdx.x >> 6);
  const int hk = bid & 7, idx = bid >> 3, w4 = wave & 3;
  const bool loader = wave >= 4;
  const u64 p_x = (u64)x, p_k = (u64)kc, p_v = (u64)vc, p_wq = (u64)wq, p_wo = (u64)wo, p_wg = (u64)wg, p_wu = (u64)wu, p_wd = (u64)wd;
  const u64 p_out = (u64)out, p_ws = (u64)ws;
#define ARGS_IN "s"(p_x), "s"(p_k), "s"(p_v), "s"(p_wq), "s"(p_wo), "s"(p_wg), "s"(p_wu), "s"(p_wd), "s"(p_out), "s"(p_ws)
#define KV_OFF ((((idx >> 3) * 8 + hk) * SK + (SK - WIN) + (idx & 7) * 256) * 512 + w4 * 32768)
#define SEQ ((unsigned)(0x0564312075643120ull >> (((idx + 3 * hk) & 7) * 4)))
#define T_OFF (P0 + (3 - (idx >> 3)) * STG)
#define is_wo (idx >= 16)
#define late (idx >= 9)
#define is_red (idx < 7)
#define wo_rg (((hk + 1 + ((PAIR ? idx : idx >> 1) & 3)) & 7) * 8 + ((idx >> 1) & 7))
#define dn_un (bid - 72)
#define late_rg (is_wo ? wo_rg : dn_un >> 1)
#define late_ch ((is_wo ? idx : dn_un) & 1)
#define oi ((bid & 3) * DM + (bid >> 2) * 32 + (lane & 31))
#define rws make_rsrc(p_ws, WS_END)
  const unsigned l16 = lane * 16;
  unsigned tag, xv = 0, cpf = 0, t_k;

  if (!loader) {
    const unsigned iv = (lane & 16) ? 0xffffffffu : 0u;
    lds_st<u32x2>(lds, L_SYNC + lane * 8, u32x2{iv, iv});
    if (CP) cpf = __builtin_amdgcn_raw_buffer_load_b32(make_rsrc(__builtin_amdgcn_s_getpc(), CP), 0, (idx * 4 + w4) * 128, 0);
    asm volatile("" :: ARGS_IN);
    WG_BARRIER();
    t_k = (unsigned)__builtin_amdgcn_s_memrealtime();
    PIN_S(t_k);
    STAMP(1);
  }
  MEM_FENCE();
  if (loader) {
    __builtin_amdgcn_s_setprio(2);
    const bool is_q = idx < 16, is_gate = idx < 23, has_early = idx < 30;
    const int half = hk & 1;
    int et_q = idx + 16 * (((EMAP == 0 ? idx : EMAP == 1 ? idx >> 1 : 0) + (hk >> 1)) & 3), et_f = (idx - (is_gate ? 16 : 23)) * 4 + (hk >> 1);
    asm("" : "+s"(et_q), "+s"(et_f));
    const int etile = is_q ? et_q : et_f;
    const int r8 = lane >> 3;
    LAS char* img = lds + L_TILE + w4 * 4096;
    const unsigned stride = is_q ? QW * 2 : FF * 2;
    unsigned evo = r8 * stride + (((lane & 7) ^ (r8 & 6)) << 4), eso = (half * 224 + w4 * 32) * stride + etile * 128, xvo = l16;
    unsigned ebytes = has_early ? DM * stride : 0, eso2 = w4 < 3 ? eso + 128 * stride : 0x40000000u, xso = w4 * DM * 2, estep = 8 * stride;
    asm volatile("" : "+v"(evo), "+v"(xvo));
    asm volatile("" : "+s"(eso), "+s"(estep), "+s"(ebytes), "+s"(eso2), "+s"(xso));
    SCHED_FENCE();
    asm volatile("" :: ARGS_IN : "memory");
    const u32x4 xr = __builtin_amdgcn_raw_buffer_load_b128(make_rsrc(p_x, NB * DM * 2), xvo, xso, 0);
    const unsigned epoch = __builtin_amdgcn_raw_buffer_load_b32(rws, 0, WS_EPOCH, EAUX);
    SCHED_FENCE();
    {
      const rsrc_t rw = make_rsrc(is_q ? p_wq : (is_gate ? p_wg : p_wu), ebytes);
      tile_fetch(rw, evo, eso, estep, img);
      tile_fetch(rw, evo, eso2, estep, img + 4 * 4096);
    }
    if (B0W < 10) VM_WAIT(B0W);
    WG_BARRIER_BARE();
    const unsigned t_go = (unsigned)__builtin_amdgcn_s_memrealtime() + T_OFF;
    SCHED_FENCE();
    STAMP(1);
    {
      float v[8], s1 = 0.f, s2 = 0.f;
      const float x0 = bf_lo(__builtin_amdgcn_readfirstlane(xr[0]));
#pragma unroll
      for (int k = 0; k < 4; ++k) { v[2 * k] = bf_lo(xr[k]); v[2 * k + 1] = bf_hi(xr[k]); }
#pragma unroll
      for (int k = 0; k < 8; ++k) { const float d = v[k] - x0; s1 += d; s2 = fmaf(d, d, s2); }
      float z = fold32(lane < 56 ? s1 : 0.f, lane < 56 ? s2 : 0.f);
      z = row_sum(fold16(z, z)) * (1.0f / DM);
      const float md = asf(__builtin_amdgcn_readlane(asu(z), 0)), m2 = asf(__builtin_amdgcn_readlane(asu(z), 32));
      const float var = round_bf16(fmaxf(fmaf(-md, md, m2), 0.f)), mb = round_bf16(x0 + md);
      const float rc = __builtin_amdgcn_rcpf(round_bf16(__builtin_amdgcn_sqrtf(round_bf16(var + 1e-5f))));
      u32x4 xn;
#pragma unroll
      for (int k = 0; k < 4; ++k) {
        const unsigned d = pack_bf16(v[2 * k] - mb, v[2 * k + 1] - mb);
        xn[k] = pack_bf16(bf_lo(d) * rc, bf_hi(d) * rc);
      }
      lds_st<u32x4>(lds, L_XN + w4 * 1024 + lane * 16, xn);
      lds_flag(lds, F_XN + w4 * 4, 1u);
      lds_wait_all(lds, F_XN);
      tag = next_tag(epoch);
      PIN_S(tag);
      const int bt = lane & 3, g = lane >> 4, jj = (lane >> 2) & 3;
      const int xo = L_XN + bt * 1024 + (half * 224 + w4 * 32 + 4 * g) * 2;
      const u32x2 a0 = lds_ld<u32x2>(lds, xo), a1 = lds_ld<u32x2>(lds, xo + 32);
      const u32x2 a2 = lds_ld<u32x2>(lds, xo + 256), a3 = lds_ld<u32x2>(lds, xo + 288);
      const u32x4 act0 = {a0[0], a0[1], a1[0], a1[1]}, act1 = {a2[0], a2[1], a3[0], a3[1]}, zero = {0u, 0u, 0u, 0u};
      VM_DRAIN();
      bf16x8 wf0[4], wf1[4];
      tile_frags(img, lane, wf0);
      f32x4 acc = {0.f, 0.f, 0.f, 0.f};
      if (w4 < 3) {
        tile_frags(img + 4 * 4096, lane, wf1);
#pragma unroll
        for (int t = 0; t < 4; ++t) acc = mma(wf1[t], asb(jj == t ? act1 : zero), acc);
      }
#pragma unroll
      for (int t = 0; t < 4; ++t) acc = mma(wf0[t], asb(jj == t ? act0 : zero), acc);
      lds_st<f32x4>(img, bt * 256 + ((lane & 12) + g) * 16, acc);
      lds_flag(lds, F_EP + w4 * 4, 1u);
      if (w4 == 3 && has_early) {
        const unsigned voff = is_q ? WS_QP + (etile >> 3) * 16384 + (lane >> 4) * 4096 + (etile & 6) * 512 + half * 512 + (etile & 1) * 256 + (lane & 15) * 16
                                   : WS_GP + etile * 4096 + (is_gate ? 0 : 2048) + half * 1024 + l16;
        lds_wait_all(lds, F_EP);
        f32x4 sum = lds_ld<f32x4>(lds, L_TILE + lane * 16);
#pragma unroll
        for (int p = 1; p < 4; ++p) sum += lds_ld<f32x4>(lds, L_TILE + p * 4096 + lane * 16);
        st_sc1(tag4_fine(sum, tag), rws, voff, 0);
      }
    }
    STAMP(2);
    __builtin_amdgcn_s_setprio(0);
    const rsrc_t rv = make_rsrc(p_v + KV_OFF, 32768);
    const unsigned hf = lane >> 5;
    unsigned vo0 = (lane ^ hf) * 16, vo1 = (lane ^ (2 + hf)) * 16, vo2 = (lane ^ (4 + hf)) * 16, vo3 = (lane ^ (6 + hf)) * 16;
    const int lcol = late_ch * 4 + w4;
    const bool has_tile = late && lcol < 7;
    const rsrc_t rl = make_rsrc(is_wo ? p_wo : p_wd, (is_wo ? QW : FF) * DM * 2);
    unsigned lvo = r8 * (DM * 2) + (((lane & 7) ^ (r8 >> 1)) << 4), lso = late_rg * (64 * DM * 2) + lcol * 128;
    unsigned rvo = (((lane >> 3) & 1) * 4 + (lane >> 4)) * RP_BOX + (lane & 7) * 16;
    unsigned rso = WS_RP + lcol * 8 * RP_BOX + ((is_wo ? 0 : 64) + late_rg) * 128;
    LAS char *aa[4], *ta[4];
#pragma unroll
    for (int t = 0; t < 4; ++t) {
      aa[t] = lds + ((lane & 3) == t ? L_A + ((lane >> 2) & 3) * 64 + (lane >> 4) * 16 : L_ZERO);
      ta[t] = img + (lane >> 2) * 128 + (((lane & 2) * 2 + (t ^ ((lane >> 3) & 3))) << 4) + 8 * (lane & 1);
      PIN_V(aa[t]); PIN_V(ta[t]);
    }
    PIN_V(vo0); PIN_V(vo1); PIN_V(vo2); PIN_V(vo3); PIN_V(lvo); PIN_V(rvo); PIN_S(lso); PIN_S(rso);
    WG_BARRIER();
    STAMP(3);
    _Pragma("nounroll") for (int g = 0; g < 8; ++g) {
      const unsigned so = ((SEQ >> (4 * g)) & 7) << 12;
      PACE((8 + g) * DT);
      LAS void* dst = (LAS void*)(lds + L_V + (g * 4 + w4) * 4096);
      __builtin_amdgcn_raw_ptr_buffer_load_lds(rv, dst, 16, vo0, so, 0, KV_AUX);
      __builtin_amdgcn_raw_ptr_buffer_load_lds(rv, dst, 16, vo1, so, 1024, KV_AUX);
      __builtin_amdgcn_raw_ptr_buffer_load_lds(rv, dst, 16, vo2, so, 2048, KV_AUX);
      __builtin_amdgcn_raw_ptr_buffer_load_lds(rv, dst, 16, vo3, so, 3072, KV_AUX);
      VM_WAIT(8);
      lds_flag(lds, L_PROG + w4 * 4, g + 1);
    }
    STAMP(4);
    VM_WAIT(4);
    lds_flag(lds, L_PROG + w4 * 4, 9);
    const bool before = has_tile && is_wo;
    if (!before) { VM_WAIT(0); STAMP(5); WG_BARRIER_BARE(); }
    if (has_tile) {
      LSPIN() { const u32x4 f = lds_peek(lds, F_PV); if (wave_none((f[0] & f[1]) == 0u)) break; }
      PACE(15 * DT + (is_wo ? PL : PLD));
      tile_fetch(rl, lvo, lso, 8 * DM * 2, img);
      tile_fetch(rl, lvo, lso + 32 * DM * 2, 8 * DM * 2, img + 4 * 4096);
      if (before) {
        VM_WAIT(8);
        STAMP(5);
        WG_BARRIER_BARE();
        VM_WAIT(FPW);
        lds_flag(lds, F_T + w4 * 4, 1u);
      }
      VM_DRAIN();
      bf16x8 wf[8];
#pragma unroll
      for (int t = 0; t < 4; ++t) { wf[t] = tr_frag<2048>(ta[t]); wf[4 + t] = tr_frag<2048>(ta[t] + 4 * 4096); }
      STAMP(8);
      if (is_wo) WG_BARRIER();
      STAMP(10);
      u32x4 act[8];
#pragma unroll
      for (int t = 0; t < 4; ++t) { act[2 * t] = lds_ld<u32x4>(aa[t], 0); act[2 * t + 1] = lds_ld<u32x4>(aa[t], 256); }
      SCHED_FENCE();
      f32x4 acc = {0.f, 0.f, 0.f, 0.f};
#pragma unroll
      for (int t = 0; t < 4; ++t) { acc = mma(asb(act[2 * t]), wf[t], acc); acc = mma(asb(act[2 * t + 1]), wf[4 + t], acc); }
      st_sc1(tag4(acc, tag), rws, rvo, rso);
      STAMP(11);
    } else if (is_wo) {
      WG_BARRIER_BARE();
    }
  }
  int role = wave;
  PIN_S(role);
  if (role < 4) {
    u32x4 kr[32], qv;
#define qso (WS_QP + hk * 16384 + (idx >> 3) * 4096 + w4 * 1024)
    const unsigned kvo = (lane & 7) * 512 + ((lane >> 3) & 1) * 64 + (lane >> 4) * 16, seq = SEQ;
    const rsrc_t rk = make_rsrc(p_k + KV_OFF, 32768);
    const unsigned t_go = t_k + T_OFF;
#pragma unroll
    for (int g = 0; g < 8; ++g) {
      const unsigned so = ((seq >> (4 * g)) & 7) << 12;
      PACE(g * DT);
#pragma unroll
      for (int i = 0; i < 4; ++i) kr[g * 4 + i] = __builtin_amdgcn_raw_buffer_load_b128(rk, kvo + i * 128, so, KV_AUX);
    }
    SCHED_FENCE();
    const unsigned kepoch = __builtin_amdgcn_raw_buffer_load_b32(rws, 0, WS_EPOCH, EAUX);
    qv = ld_sc1(rws, l16, qso);
    STAMP(2);
    WG_BARRIER_BARE();
    STAMP(3);
    tag = next_tag(kepoch);
    asm volatile("" : "+s"(tag) : "v"(cpf));
    SPIN(SPIN_MAX) {
      qv ^= tag_nibbles(tag);
      if (wave_none((or4(qv) & 15u) != 0u)) break;
      MEM_FENCE();
      qv = ld_sc1(rws, l16, qso);
    }
    lds_st<unsigned>(lds, L_Q + w4 * 256 + (lane & 31) * 8 + (lane >> 5) * 4,
                     pack_bf16(fold32(asf(qv[0]), asf(qv[2])), fold32(asf(qv[1]), asf(qv[3]))));
    lds_flag(lds, F_Q + w4 * 4, 1u);
    lds_wait_all(lds, F_Q);
    STAMP(4);
    float m0, m1;
    {
      const int qa = L_Q + (lane & 1) * 512 + ((lane >> 1) & 1) * 64 + (lane >> 4) * 16;
      f32x4 c0, c1;
      {
        u32x4 qf[4];
#pragma unroll
        for (int s = 0; s < 4; ++s) qf[s] = lds_ld<u32x4>(lds, qa + s * 128);
        f32x4 c[8];
#pragma unroll
        for (int t = 0; t < 8; ++t) c[t] = mma(asb(qf[0]), asb(kr[t * 4]), f32x4{0.f, 0.f, 0.f, 0.f});
#pragma unroll
        for (int s = 1; s < 4; ++s)
#pragma unroll
          for (int t = 0; t < 8; ++t) c[t] = mma(asb(qf[s]), asb(kr[t * 4 + s]), c[t]);
        const bool g1 = lane & 16, g2 = lane & 32;
        c0 = g2 ? (g1 ? c[3] : c[2]) : (g1 ? c[1] : c[0]);
        c1 = g2 ? (g1 ? c[7] : c[6]) : (g1 ? c[5] : c[4]);
      }
      const bool cb = lane & 8;
      const float r0 = (cb ? c1[2] : c0[0]) + dpp<XOR8>(cb ? c0[2] : c1[0]);
      const float r1 = (cb ? c1[3] : c0[1]) + dpp<XOR8>(cb ? c0[3] : c1[1]);
      const unsigned sp = pack_bf16(r0, r1);
      const float s0 = bf_lo(sp) * 0.0625f, s1 = bf_hi(sp) * 0.0625f;
      float mw = max32(s0, s1);
      mw = row_max(max16(mw, mw));
      *(volatile LAS unsigned*)(lds + L_MX + (lane >> 5) * 16 + w4 * 4) = asu(mw) & 0xfffffffeu;
      u32x4 x0, x1;
      LSPIN() { x0 = lds_peek(lds, L_MX); x1 = lds_peek(lds, L_MX + 16); if (wave_none(unset4(x0) | unset4(x1))) break; }
      m0 = fmaxf(fmaxf(asf(x0[0]), asf(x0[1])), fmaxf(asf(x0[2]), asf(x0[3])));
      m1 = fmaxf(fmaxf(asf(x1[0]), asf(x1[1])), fmaxf(asf(x1[2]), asf(x1[3])));
      const float p0 = __builtin_amdgcn_exp2f((s0 - m0) * LOG2E), p1 = __builtin_amdgcn_exp2f((s1 - m1) * LOG2E);
      const unsigned pk = pack_bf16(p0, p1);
      const int po = L_P + (((lane >> 3) & 1) * 4 + (lane >> 4)) * 64 + ((w4 & 1) * 2 + ((lane >> 2) & 1)) * 16 + ((w4 >> 1) * 4 + (lane & 3)) * 2;
      lds_st<u16>(lds, po, (u16)pk);
      lds_st<u16>(lds, po + 512, (u16)(pk >> 16));
      float l = fold32(p0, p1);
      l = row_sum(fold16(l, l));
      MEM_FENCE();
      *(volatile LAS unsigned*)(lds + L_LS + (lane >> 5) * 16 + w4 * 4) = asu(l) & 0xfffffffeu;
    }
    u32x4 ls;
    LSPIN() { ls = lds_peek(lds, L_LS + (lane >> 5) * 16); if (wave_none(unset4(ls))) break; }
    MEM_FENCE();
    STAMP(5);
    u32x4 tb = tag_bytes(tag);
    unsigned oso = WS_OP + wo_rg * 8192 + w4 * 2048;
    LAS char* ad = lds + L_A + (lane & 4) * 64 + w4 * 64 + ((lane & 1) * 2 + ((lane >> 3) & 1)) * 16 + ((lane & 2) * 2 + (lane >> 4)) * 2;
    LAS char* fa = lds + L_F + w4 * 32 + (lane >> 3) * 4;
    PIN_S(tb); PIN_S(oso); PIN_V(ad); PIN_V(fa);
    if (w4 < 2) {
      const int g = lane >> 4, row = lane >> 2, p = lane & 3, tl = lane & 7;
      LAS char* va[8];
#pragma unroll
      for (int t = 0; t < 8; ++t) va[t] = lds + L_V + row * 512 + ((w4 * 16 + (p >> 1) * 8 + (t ^ (row & 7))) << 4) + 8 * (p & 1);
      LAS char* pa = lds + L_P + ((lane >> 3) & 1) * 512 + g * 16;
      unsigned ovo = (((lane >> 3) & 1) * 4 + (g >> 1)) * 8192 + (g & 1) * 1024 + tl * 16;
      unsigned pso = WS_OP + (hk * 8 + w4 * 2) * 8192 + (idx >> 3) * 2048 + (idx & 7) * 128;
      PIN_V(ovo); PIN_S(pso);
      f32x4 o = {0.f, 0.f, 0.f, 0.f};
      u32x4 pm[8];
      {
        const u32x4 pl = lds_ld<u32x4>(pa, 0);
#pragma unroll
        for (int t = 0; t < 8; ++t) pm[t] = tl == t ? pl : u32x4{0u, 0u, 0u, 0u};
      }
      _Pragma("nounroll") for (int ks = 0; ks < 7; ++ks) {
        LSPIN() {
          const u32x4 pr = lds_peek(lds, L_PROG);
          if (wave_none(min(min(pr[0], pr[1]), min(pr[2], pr[3])) <= (unsigned)(ks + 2))) break;
        }
        lds_flag(lds, F_PV + w4 * 4, ks >= 2 ? 1u : 0u);
        pa += 64;
        const u32x4 pl = lds_ld<u32x4>(pa, 0);
        bf16x8 vf[8];
#pragma unroll
        for (int t = 0; t < 8; ++t) vf[t] = tr_frag<8192>(va[t]);
        SCHED_FENCE();
#pragma unroll
        for (int t = 0; t < 8; ++t) {
          o = mma(vf[t], asb(pm[t]), o);
          SCHED_FENCE();
          pm[t] = tl == t ? pl : u32x4{0u, 0u, 0u, 0u};
          va[t] += 16384;
          PIN_V(pm[t]); PIN_V(va[t]);
          SCHED_FENCE();
        }
      }
      STAMP(6);
      WG_BARRIER_BARE();
      {
        bf16x8 vf[8];
#pragma unroll
        for (int t = 0; t < 8; ++t) vf[t] = tr_frag<8192>(va[t]);
        SCHED_FENCE();
#pragma unroll
        for (int t = 0; t < 8; ++t) o = mma(vf[t], asb(pm[t]), o);
      }
      st_sc1(tag4(o, tag), rws, ovo, pso);
      STAMP(7);
    } else if (w4 == 2) {
      if (late && !is_wo) {
        u32x4 gv[4];
        SPIN(SPIN_MAX) {
          unsigned bad = 0;
#pragma unroll
          for (int u = 0; u < 4; ++u) gv[u] = ld_sc1(rws, l16 + u * 1024, WS_GP + (dn_un >> 1) * 4096);
#pragma unroll
          for (int u = 0; u < 4; ++u) { gv[u] ^= tag_nibbles(tag); bad |= or4(gv[u]); }
          if (wave_none((bad & 15u) != 0u)) break;
          MEM_FENCE();
        }
        const f32x4 gs = asf4(gv[0]) + asf4(gv[1]), us = asf4(gv[2]) + asf4(gv[3]);
        u32x2 hv;
#pragma unroll
        for (int e = 0; e < 2; ++e) {
          const unsigned gt = pack_bf16(gs[2 * e], gs[2 * e + 1]), up = pack_bf16(us[2 * e], us[2 * e + 1]);
          const float g0 = bf_lo(gt), g1 = bf_hi(gt);
          hv[e] = pack_bf16(g0 * (g0 > 0.f ? 1.f : 0.f) * bf_lo(up), g1 * (g1 > 0.f ? 1.f : 0.f) * bf_hi(up));
        }
        lds_st<u32x2>(lds, L_A + (lane & 8) * 32 + (lane >> 4) * 64 + (lane & 3) * 16 + (lane & 4) * 2, hv);
      } else if (is_red) {
        xv = __builtin_amdgcn_raw_buffer_load_b16(make_rsrc(p_x, NB * DM * 2), oi * 2, 0, 0);
        PIN_V(xv);
      }
      WG_BARRIER();
    } else {
      const float lsum = (asf(ls[0]) + asf(ls[1])) + (asf(ls[2]) + asf(ls[3]));
      const float l1 = asf(__builtin_amdgcn_readlane(asu(lsum), 32));
      if (lane == 0) st_sc1(tag4(f32x4{m0, lsum, m1, l1}, tag), rws, 0, WS_ML + (hk * 32 + idx) * 16);
      const unsigned mso = is_wo ? WS_ML + (wo_rg >> 3) * 512 : WS_END;
      LSPIN() { if (wave_none(*(volatile LAS unsigned*)(lds + L_PROG) < (unsigned)MLAT)) break; __builtin_amdgcn_s_sleep(4); }
      u32x4 mv = ld_sc1(rws, (lane & 31) * 16, mso);
      WG_BARRIER_BARE();
      mv ^= tb;
      PIN_V(mv);
      if (is_wo) {
        SPIN(SPIN_MAX) {
          if (wave_none((or4(mv) & 255u) != 0u)) break;
          MEM_FENCE();
          mv = ld_sc1(rws, (lane & 31) * 16, mso) ^ tb;
        }
        const float m = asf((idx & 8) ? mv[2] : mv[0]);
        float mx = fmaxf(m, dpp<XOR7>(m));
        mx = fmaxf(mx, dpp<XOR2>(mx));
        mx = fmaxf(mx, dpp<XOR1>(mx));
        const float e = __builtin_amdgcn_exp2f((m - mx) * LOG2E);
        float lt = e * asf((idx & 8) ? mv[3] : mv[1]);
        lt += dpp<XOR7>(lt);
        lt += dpp<XOR2>(lt);
        lt += dpp<XOR1>(lt);
        *(volatile LAS unsigned*)(lds + L_F + (lane & 31) * 4) = asu(e * __builtin_amdgcn_rcpf(lt)) & 0xfffffffeu;
      } else if (bid == 56) {
        SPIN(SPIN_MAX) {
          unsigned bad = 0;
#pragma unroll
          for (int u = 0; u < 4; ++u) bad |= or4(ld_sc1(rws, l16 + u * 1024, WS_ML) ^ tb);
          if (wave_none((bad & 255u) != 0u)) break;
          MEM_FENCE();
        }
        if (lane == 0) __builtin_amdgcn_raw_buffer_store_b32(tag, rws, 0, WS_EPOCH, AUX_SC1);
      }
    }
    if (is_wo) {
      float f;
      LSPIN() {
        f = *(volatile LAS float*)fa;
        if (wave_none((asu(f) == 0xffffffffu) | (*(volatile LAS unsigned*)(lds + F_T) == 0u))) break;
      }
      STAMP(8);
      u32x4 o0, o1;
      MEM_FENCE();
      SPIN(SPIN_MAX) {
        o0 = ld_sc1(rws, l16, oso) ^ tb;
        o1 = ld_sc1(rws, l16 + 1024, oso) ^ tb;
        if (wave_none(((or4(o0) | or4(o1)) & 255u) != 0u)) break;
        if (OSL) __builtin_amdgcn_s_sleep(OSL);
        MEM_FENCE();
      }
      STAMP(9);
      const float o = fold_pair(asf4(o0) * f, asf4(o1) * f);
      lds_st<u16>(ad, 0, (u16)pack_bf16(o, o));
      WG_BARRIER();
    }
  }

  if (is_red) {
    const u32x4 tb = tag_bytes(tag);
    LAS char* fw = lds + L_FIN + ((lane >> 4) * 8 + (lane & 7)) * 64 + wave * 4 + (lane & 8) * 4;
    LAS char* fr = lds + L_FIN + (lane & 31) * 64 + (lane >> 5) * 32;
    unsigned oo = (lane & 32) ? 0x7fffffffu : oi * 2;
    const bool dn_lane = wave * 64 + lane < 28 * 8;
    unsigned dvo = dn_lane ? l16 + 8192 : 0x7fffffffu;
    u32x4 td = dn_lane ? tb : u32x4{0u, 0u, 0u, 0u};
    float xf = bf_lo(xv);
    PIN_V(fw); PIN_V(fr); PIN_V(oo); PIN_V(xf); PIN_V(dvo); PIN_V(td);
    const rsrc_t ro = make_rsrc(p_out, NB * DM * 2);
    const unsigned so = WS_RP + bid * RP_BOX + wave * 1024;
    u32x4 a0, a1;
    if (RD) __builtin_amdgcn_s_sleep(RD);
    STAMP(8);
    SPIN(SPIN_MAX) {
      a1 = ld_sc1(rws, dvo, so) ^ td;
      a0 = ld_sc1(rws, l16, so) ^ tb;
      if (wave_none(((or4(a0) | or4(a1)) & 255u) != 0u)) break;
      if (RSL) __builtin_amdgcn_s_sleep(RSL);
      MEM_FENCE();
    }
    STAMP(9);
    lds_st<float>(fw, 0, fold_pair(asf4(a0), asf4(a1)));
    WG_BARRIER();
    STAMP(10);
    if (wave == 2) {
      const f32x4 t = lds_ld<f32x4>(fr, 0) + lds_ld<f32x4>(fr, 16);
      const f32x2 t2 = f32x2{t[0], t[1]} + f32x2{t[2], t[3]};
      const float u = round_bf16(t2[0] + t2[1]);
      auto sw = __builtin_amdgcn_permlane32_swap(asu(u), asu(u), false, false);
      const float sv = asf(sw[0]), dn = asf(sw[1]);
      const float g = round_bf16(dn * __builtin_amdgcn_rcpf(1.0f + __builtin_amdgcn_exp2f(-dn * LOG2E)));
      const float res = xf + round_bf16(sv * g);
      __builtin_amdgcn_raw_buffer_store_b16((u16)pack_bf16(res, res), ro, oo, 0, 0);
      STAMP(11);
    }
  }
  STAMP_OUT();
}

}

extern "C" void kernel_launch(void* const* d_in, const int* in_sizes, int n_in, void* d_out, int out_size, void* d_ws,
                              size_t ws_size, hipStream_t stream) {
  fused_decode<<<256, 512, 0, stream>>>((const u16*)d_in[0], (const u16*)d_in[1], (const u16*)d_in[2], (const u16*)d_in[3],
                                        (const u16*)d_in[4], (const u16*)d_in[6], (const u16*)d_in[7], (const u16*)d_in[8],
                                        (u16*)d_out, (char*)d_ws + WS_OFF);
}
```

```cpp
#include <hip/hip_runtime.h>
#include <hip/hip_bf16.h>
#include <stdint.h>

namespace {

typedef unsigned short u16;
typedef unsigned long long u64;
typedef unsigned u32x2 __attribute__((ext_vector_type(2)));
typedef unsigned u32x4 __attribute__((ext_vector_type(4)));
typedef float f32x2 __attribute__((ext_vector_type(2)));
typedef float f32x4 __attribute__((ext_vector_type(4)));
typedef short s16x4 __attribute__((ext_vector_type(4)));
typedef __bf16 bf16x2 __attribute__((ext_vector_type(2)));
typedef __bf16 bf16x8 __attribute__((ext_vector_type(8)));
#define LAS __attribute__((address_space(3)))
typedef __amdgpu_buffer_rsrc_t rsrc_t;

constexpr int NB = 4, DM = 448, SK = 8192, WIN = 2048, FF = 1792, QW = 4096;
constexpr int AUX_SC1 = 16, AUX_NT = 2;
constexpr unsigned SPIN_MAX = 1u << 22;
constexpr unsigned CODE_BYTES = 10780;
constexpr int T_FIRST = 64;
constexpr int T_STAGGER = 10;
constexpr int T_STEP = 56;
constexpr int T_WO = 60, T_DOWN = 80;
constexpr float LOG2E = 1.4426950408889634f;

constexpr unsigned WS_EPOCH = 0;
constexpr unsigned WS_QP = 4096;
constexpr unsigned WS_GP = WS_QP + 8 * 4 * 4096;
constexpr unsigned WS_OP = WS_GP + 28 * 4096;
constexpr unsigned WS_ML = WS_OP + 64 * 8192;
constexpr unsigned WS_RP = WS_ML + 4096;
constexpr unsigned RP_BOX = 92 * 128;
constexpr unsigned WS_END = WS_RP + 56 * RP_BOX;

constexpr int L_V = 0;
constexpr int L_TILE = L_V;
constexpr int L_XN = L_V + 32768;
constexpr int L_FIN = L_V;
constexpr int L_Q = 131072;
constexpr int L_P = L_Q + 1024;
constexpr int L_A = L_P + 1024;
constexpr int L_SYNC = L_A + 512;
constexpr int L_ZERO = L_SYNC;
constexpr int F_XN = L_SYNC + 16, F_EP = L_SYNC + 32, F_Q = L_SYNC + 48, L_PROG = L_SYNC + 64, F_PV = L_SYNC + 80, F_T = L_SYNC + 96;
constexpr int L_F = L_SYNC + 128;
constexpr int L_MX = L_SYNC + 384;
constexpr int L_LS = L_MX + 32;
constexpr int L_END = L_SYNC + 512;

#define SCHED_FENCE() __builtin_amdgcn_sched_barrier(0)
#define MEM_FENCE() asm volatile("" ::: "memory")
#define WG_BARRIER() do { SCHED_FENCE(); asm volatile("s_waitcnt lgkmcnt(0)\n\ts_barrier" ::: "memory"); SCHED_FENCE(); } while (0)
#define WG_BARRIER_BARE() do { SCHED_FENCE(); asm volatile("s_barrier" ::: "memory"); SCHED_FENCE(); } while (0)
#define VM_WAIT(n) do { SCHED_FENCE(); asm volatile("s_waitcnt vmcnt(%0)" :: "i"(n) : "memory"); SCHED_FENCE(); } while (0)
#define VM_DRAIN() do { SCHED_FENCE(); __builtin_amdgcn_s_waitcnt(0x0F70); SCHED_FENCE(); MEM_FENCE(); } while (0)
#define NAP() __builtin_amdgcn_s_sleep(1)
#define PACE(t) do { SCHED_FENCE(); pace_until(t_go + (t)); SCHED_FENCE(); } while (0)
#define PIN_V(x) asm volatile("" : "+v"(x))
#define PIN_S(x) asm volatile("" : "+s"(x))
#define LSPIN() _Pragma("nounroll") for (;;)
#define SPIN(limit) _Pragma("nounroll") for (unsigned s_ = 0; s_ < (limit); ++s_)

__device__ __forceinline__ void pace_until(unsigned when) {
  asm volatile("1:\n\ts_memrealtime vcc\n\ts_waitcnt lgkmcnt(0)\n\ts_sub_i32 vcc_lo, vcc_lo, %0\n\ts_cmp_lt_i32 vcc_lo, 0\n\ts_cbranch_scc1 1b"
               :: "s"(when) : "vcc", "scc", "memory");
}
__device__ __forceinline__ float asf(unsigned u) { return __builtin_bit_cast(float, u); }
__device__ __forceinline__ unsigned asu(float f) { return __builtin_bit_cast(unsigned, f); }
__device__ __forceinline__ unsigned pack_bf16(float lo, float hi) {
  f32x2 f = {lo, hi};
  return __builtin_bit_cast(unsigned, __builtin_convertvector(f, bf16x2));
}
__device__ __forceinline__ float round_bf16(float v) { return asf(pack_bf16(0.f, v)); }
__device__ __forceinline__ float bf_lo(unsigned u) { return asf(u << 16); }
__device__ __forceinline__ float bf_hi(unsigned u) { return asf(u & 0xffff0000u); }
template <int CTRL> __device__ __forceinline__ float dpp(float x) {
  return asf((unsigned)__builtin_amdgcn_mov_dpp((int)asu(x), CTRL, 0xf, 0xf, true));
}
constexpr int XOR1 = 0xB1, XOR2 = 0x4E, XOR7 = 0x141, XOR8 = 0x128;
__device__ __forceinline__ float fold16(float a, float b) {
  auto s = __builtin_amdgcn_permlane16_swap(asu(a), asu(b), false, false);
  return asf(s[0]) + asf(s[1]);
}
__device__ __forceinline__ float fold32(float a, float b) {
  auto s = __builtin_amdgcn_permlane32_swap(asu(a), asu(b), false, false);
  return asf(s[0]) + asf(s[1]);
}
__device__ __forceinline__ float max16(float a, float b) {
  auto s = __builtin_amdgcn_permlane16_swap(asu(a), asu(b), false, false);
  return fmaxf(asf(s[0]), asf(s[1]));
}
__device__ __forceinline__ float max32(float a, float b) {
  auto s = __builtin_amdgcn_permlane32_swap(asu(a), asu(b), false, false);
  return fmaxf(asf(s[0]), asf(s[1]));
}
__device__ __forceinline__ float row_sum(float v) {
  v += dpp<XOR8>(v); v += dpp<XOR7>(v); v += dpp<XOR2>(v); v += dpp<XOR1>(v);
  return v;
}
__device__ __forceinline__ float wave_sum(float v) { v = fold32(v, v); return row_sum(fold16(v, v)); }
__device__ __forceinline__ float row_max(float v) {
  v = fmaxf(v, dpp<XOR8>(v)); v = fmaxf(v, dpp<XOR7>(v)); v = fmaxf(v, dpp<XOR2>(v)); v = fmaxf(v, dpp<XOR1>(v));
  return v;
}
__device__ __forceinline__ float fold_pair(f32x4 a, f32x4 b) {
  const float a0 = fold32(a[0], a[2]), a1 = fold32(a[1], a[3]), b0 = fold32(b[0], b[2]), b1 = fold32(b[1], b[3]);
  SCHED_FENCE();
  const float za = fold16(a0, a1), zb = fold16(b0, b1);
  const bool hi = __builtin_amdgcn_inverse_ballot_w64(0xff00ff00ff00ff00ull);
  return (hi ? zb : za) + dpp<XOR8>(hi ? za : zb);
}
__device__ __forceinline__ rsrc_t make_rsrc(u64 p, unsigned bytes) {
  return __builtin_amdgcn_make_buffer_rsrc((void*)p, 0, bytes, 0x00020000);
}
__device__ __forceinline__ u32x4 tag4(f32x4 v, unsigned tag) {
  u32x4 r;
  r[0] = __builtin_amdgcn_perm(asu(v[0]), tag, 0x07060500u);
  r[1] = __builtin_amdgcn_perm(asu(v[1]), tag, 0x07060501u);
  r[2] = __builtin_amdgcn_perm(asu(v[2]), tag, 0x07060502u);
  r[3] = __builtin_amdgcn_perm(asu(v[3]), tag, 0x07060503u);
  return r;
}
__device__ __forceinline__ unsigned next_tag(unsigned epoch) {
  const unsigned e = __builtin_amdgcn_readfirstlane(epoch);
  return ((e & 0x7f7f7f7fu) + 0x07050301u) ^ (e & 0x80808080u);
}
__device__ __forceinline__ u32x4 tag_bytes(unsigned tag) { return u32x4{tag & 255u, (tag >> 8) & 255u, (tag >> 16) & 255u, tag >> 24}; }
__device__ __forceinline__ u32x4 tag_nibbles(unsigned tag) { return u32x4{tag & 15u, (tag >> 8) & 15u, (tag >> 16) & 15u, (tag >> 24) & 15u}; }
__device__ __forceinline__ u32x4 tag4_fine(f32x4 v, unsigned tag) {
  return ((__builtin_bit_cast(u32x4, v) + 8u) & 0xfffffff0u) | tag_nibbles(tag);
}
__device__ __forceinline__ unsigned or4(u32x4 v) { return v[0] | v[1] | v[2] | v[3]; }
__device__ __forceinline__ f32x4 asf4(u32x4 v) { return __builtin_bit_cast(f32x4, v); }
__device__ __forceinline__ u32x4 ld_sc1(rsrc_t r, unsigned voff, unsigned soff) {
  return __builtin_amdgcn_raw_buffer_load_b128(r, voff, soff, AUX_SC1);
}
__device__ __forceinline__ void st_sc1(u32x4 v, rsrc_t r, unsigned voff, unsigned soff) {
  __builtin_amdgcn_raw_buffer_store_b128(v, r, voff, soff, AUX_SC1);
}
__device__ __forceinline__ bool wave_none(bool bad) { return __builtin_amdgcn_ballot_w64(bad) == 0ull; }

template <typename T> __device__ __forceinline__ T lds_ld(LAS char* l, int off) { return *(LAS T*)(l + off); }
template <typename T> __device__ __forceinline__ void lds_st(LAS char* l, int off, T v) { *(LAS T*)(l + off) = v; }
__device__ __forceinline__ void lds_flag(LAS char* l, int off, unsigned v) {
  MEM_FENCE();
  *(volatile LAS unsigned*)(l + off) = v;
  MEM_FENCE();
}
__device__ __forceinline__ u32x4 lds_peek(LAS char* l, int off) { return *(volatile LAS u32x4*)(l + off); }
__device__ __forceinline__ void lds_wait_all(LAS char* l, int off) {
  LSPIN() { const u32x4 f = lds_peek(l, off); if (wave_none((f[0] & f[1] & f[2] & f[3]) == 0u)) break; NAP(); }
  MEM_FENCE();
}
__device__ __forceinline__ bool unset4(u32x4 v) { return max(max(v[0], v[1]), max(v[2], v[3])) == 0xffffffffu; }

__device__ __forceinline__ void dma16(rsrc_t r, LAS char* dst, unsigned voff, unsigned soff) {
  __builtin_amdgcn_raw_ptr_buffer_load_lds(r, (LAS void*)dst, 16, voff, soff, 0, AUX_NT);
}
__device__ __forceinline__ void tile_fetch(rsrc_t r, unsigned voff, unsigned soff, unsigned step, LAS char* img) {
#pragma unroll
  for (int i = 0; i < 4; ++i) dma16(r, img + i * 1024, voff, soff + i * step);
}
template <int GAP> __device__ __forceinline__ bf16x8 tr_frag(LAS char* a) {
  const s16x4 lo = __builtin_amdgcn_ds_read_tr16_b64_v4i16((LAS s16x4*)a);
  const s16x4 hi = __builtin_amdgcn_ds_read_tr16_b64_v4i16((LAS s16x4*)(a + GAP));
  return __builtin_bit_cast(bf16x8, __builtin_shufflevector(lo, hi, 0, 1, 2, 3, 4, 5, 6, 7));
}
__device__ __forceinline__ void tile_frags(LAS char* img, int lane, bf16x8 (&w)[4]) {
  const int row = lane >> 2, p = lane & 3;
#pragma unroll
  for (int t = 0; t < 4; ++t) w[t] = tr_frag<2048>(img + row * 128 + (((2 * t + (p >> 1)) ^ (row & 6)) << 4) + 8 * (p & 1));
}
__device__ __forceinline__ f32x4 mma(bf16x8 a, bf16x8 b, f32x4 c) { return __builtin_amdgcn_mfma_f32_16x16x32_bf16(a, b, c, 0, 0, 0); }
__device__ __forceinline__ bf16x8 asb(u32x4 v) { return __builtin_bit_cast(bf16x8, v); }

__global__ void __launch_bounds__(512, 2)
fused_decode(const u16* x, const u16* kc, const u16* vc, const u16* wq, const u16* wo, const u16* wg, const u16* wu,
             const u16* wd, u16* out, char* ws) {
  __shared__ __attribute__((aligned(16))) char lds_raw[L_END];
  LAS char* lds = (LAS char*)lds_raw;
  const int bid = blockIdx.x, lane = threadIdx.x & 63, wave = __builtin_amdgcn_readfirstlane(threadIdx.x >> 6);
  const int hk = bid & 7, idx = bid >> 3, w4 = wave & 3;
  const bool loader = wave >= 4;
  const u64 p_x = (u64)x, p_k = (u64)kc, p_v = (u64)vc, p_wq = (u64)wq, p_wo = (u64)wo, p_wg = (u64)wg, p_wu = (u64)wu, p_wd = (u64)wd;
  const u64 p_out = (u64)out, p_ws = (u64)ws;
#define ARGS_IN "s"(p_x), "s"(p_k), "s"(p_v), "s"(p_wq), "s"(p_wo), "s"(p_wg), "s"(p_wu), "s"(p_wd), "s"(p_out), "s"(p_ws)
#define KV_OFF ((((idx >> 3) * 8 + hk) * SK + (SK - WIN) + (idx & 7) * 256) * 512 + w4 * 32768)
#define SEQ ((unsigned)(0x0564312075643120ull >> (((idx + 3 * hk) & 7) * 4)))
#define T_OFF (T_FIRST + (3 - (idx >> 3)) * T_STAGGER)
#define is_wo (idx >= 16)
#define late (idx >= 9)
#define is_red (idx < 7)
#define wo_rg (((hk + 1 + ((idx >> 1) & 3)) & 7) * 8 + ((idx >> 1) & 7))
#define dn_un (bid - 72)
#define late_rg (is_wo ? wo_rg : dn_un >> 1)
#define late_ch ((is_wo ? idx : dn_un) & 1)
#define oi ((bid & 3) * DM + (bid >> 2) * 32 + (lane & 31))
#define rws make_rsrc(p_ws, WS_END)
  const unsigned l16 = lane * 16;
  unsigned tag = 0, xv = 0, cpf = 0, t_k = 0;

  if (!loader) {
    const unsigned iv = (lane & 16) ? 0xffffffffu : 0u;
    lds_st<u32x2>(lds, L_SYNC + lane * 8, u32x2{iv, iv});
    cpf = __builtin_amdgcn_raw_buffer_load_b32(make_rsrc(__builtin_amdgcn_s_getpc() & ~127ull, CODE_BYTES), 0, (idx * 4 + w4) * 128, 0);
    asm volatile("" :: ARGS_IN);
    WG_BARRIER();
    t_k = (unsigned)__builtin_amdgcn_s_memrealtime();
    PIN_S(t_k);
  }
  MEM_FENCE();
  if (loader) {
    __builtin_amdgcn_s_setprio(2);
    const bool is_q = idx < 16, is_gate = idx < 23, has_early = idx < 30;
    const int half = hk & 1;
    int et_q = idx + 16 * ((idx + (hk >> 1)) & 3), et_f = (idx - (is_gate ? 16 : 23)) * 4 + (hk >> 1);
    asm("" : "+s"(et_q), "+s"(et_f));
    const int etile = is_q ? et_q : et_f;
    const int r8 = lane >> 3;
    LAS char* img = lds + L_TILE + w4 * 4096;
    const unsigned stride = is_q ? QW * 2 : FF * 2;
    unsigned evo = r8 * stride + (((lane & 7) ^ (r8 & 6)) << 4), eso = (half * 224 + w4 * 32) * stride + etile * 128, xvo = l16;
    unsigned ebytes = has_early ? DM * stride : 0, eso2 = w4 < 3 ? eso + 128 * stride : 0x40000000u, xso = w4 * DM * 2, estep = 8 * stride;
    asm volatile("" : "+v"(evo), "+v"(xvo));
    asm volatile("" : "+s"(eso), "+s"(estep), "+s"(ebytes), "+s"(eso2), "+s"(xso));
    SCHED_FENCE();
    asm volatile("" :: ARGS_IN : "memory");
    const u32x4 xr = __builtin_amdgcn_raw_buffer_load_b128(make_rsrc(p_x, NB * DM * 2), xvo, xso, 0);
    const unsigned epoch = __builtin_amdgcn_raw_buffer_load_b32(rws, 0, WS_EPOCH, AUX_SC1);
    SCHED_FENCE();
    {
      const rsrc_t rw = make_rsrc(is_q ? p_wq : (is_gate ? p_wg : p_wu), ebytes);
      tile_fetch(rw, evo, eso, estep, img);
      tile_fetch(rw, evo, eso2, estep, img + 4 * 4096);
    }
    WG_BARRIER_BARE();
    const unsigned t_go = (unsigned)__builtin_amdgcn_s_memrealtime() + T_OFF;
    SCHED_FENCE();
    {
      float v[8], t = 0.f;
#pragma unroll
      for (int k = 0; k < 4; ++k) { v[2 * k] = bf_lo(xr[k]); v[2 * k + 1] = bf_hi(xr[k]); t += v[2 * k] + v[2 * k + 1]; }
      const float mean = wave_sum(lane < 56 ? t : 0.f) * (1.0f / DM);
      t = 0.f;
#pragma unroll
      for (int k = 0; k < 8; ++k) { const float d = v[k] - mean; t = fmaf(d, d, t); }
      const float var = round_bf16(wave_sum(lane < 56 ? t : 0.f) * (1.0f / DM)), mb = round_bf16(mean);
      const float rc = __builtin_amdgcn_rcpf(round_bf16(__builtin_amdgcn_sqrtf(round_bf16(var + 1e-5f))));
      u32x4 xn;
#pragma unroll
      for (int k = 0; k < 4; ++k) {
        const unsigned d = pack_bf16(v[2 * k] - mb, v[2 * k + 1] - mb);
        xn[k] = pack_bf16(bf_lo(d) * rc, bf_hi(d) * rc);
      }
      lds_st<u32x4>(lds, L_XN + w4 * 1024 + lane * 16, xn);
      lds_flag(lds, F_XN + w4 * 4, 1u);
      lds_wait_all(lds, F_XN);
      tag = next_tag(epoch);
      PIN_S(tag);
      const int bt = lane & 3, g = lane >> 4, jj = (lane >> 2) & 3;
      const int xo = L_XN + bt * 1024 + (half * 224 + w4 * 32 + 4 * g) * 2;
      const u32x2 a0 = lds_ld<u32x2>(lds, xo), a1 = lds_ld<u32x2>(lds, xo + 32);
      const u32x2 a2 = lds_ld<u32x2>(lds, xo + 256), a3 = lds_ld<u32x2>(lds, xo + 288);
      const u32x4 act0 = {a0[0], a0[1], a1[0], a1[1]}, act1 = {a2[0], a2[1], a3[0], a3[1]}, zero = {0u, 0u, 0u, 0u};
      VM_DRAIN();
      bf16x8 wf0[4], wf1[4];
      tile_frags(img, lane, wf0);
      f32x4 acc = {0.f, 0.f, 0.f, 0.f};
      if (w4 < 3) {
        tile_frags(img + 4 * 4096, lane, wf1);
#pragma unroll
        for (int t = 0; t < 4; ++t) acc = mma(wf1[t], asb(jj == t ? act1 : zero), acc);
      }
#pragma unroll
      for (int t = 0; t < 4; ++t) acc = mma(wf0[t], asb(jj == t ? act0 : zero), acc);
      lds_st<f32x4>(img, bt * 256 + ((lane & 12) + g) * 16, acc);
      lds_flag(lds, F_EP + w4 * 4, 1u);
      if (w4 == 3 && has_early) {
        const unsigned voff = is_q ? WS_QP + (etile >> 3) * 16384 + (lane >> 4) * 4096 + (etile & 6) * 512 + half * 512 + (etile & 1) * 256 + (lane & 15) * 16
                                   : WS_GP + etile * 4096 + (is_gate ? 0 : 2048) + half * 1024 + l16;
        lds_wait_all(lds, F_EP);
        f32x4 sum = lds_ld<f32x4>(lds, L_TILE + lane * 16);
#pragma unroll
        for (int p = 1; p < 4; ++p) sum += lds_ld<f32x4>(lds, L_TILE + p * 4096 + lane * 16);
        st_sc1(tag4_fine(sum, tag), rws, voff, 0);
      }
    }
    __builtin_amdgcn_s_setprio(0);
    const rsrc_t rv = make_rsrc(p_v + KV_OFF, 32768);
    const unsigned hf = lane >> 5;
    unsigned vo0 = (lane ^ hf) * 16, vo1 = (lane ^ (2 + hf)) * 16, vo2 = (lane ^ (4 + hf)) * 16, vo3 = (lane ^ (6 + hf)) * 16;
    const int lcol = late_ch * 4 + w4;
    const bool has_tile = late && lcol < 7;
    const rsrc_t rl = make_rsrc(is_wo ? p_wo : p_wd, (is_wo ? QW : FF) * DM * 2);
    unsigned lvo = r8 * (DM * 2) + (((lane & 7) ^ (r8 >> 1)) << 4), lso = late_rg * (64 * DM * 2) + lcol * 128;
    unsigned rvo = (((lane >> 3) & 1) * 4 + (lane >> 4)) * RP_BOX + (lane & 7) * 16;
    unsigned rso = WS_RP + lcol * 8 * RP_BOX + ((is_wo ? 0 : 64) + late_rg) * 128;
    LAS char *aa[4], *ta[4];
#pragma unroll
    for (int t = 0; t < 4; ++t) {
      aa[t] = lds + ((lane & 3) == t ? L_A + ((lane >> 2) & 3) * 64 + (lane >> 4) * 16 : L_ZERO);
      ta[t] = img + (lane >> 2) * 128 + (((lane & 2) * 2 + (t ^ ((lane >> 3) & 3))) << 4) + 8 * (lane & 1);
      PIN_V(aa[t]); PIN_V(ta[t]);
    }
    PIN_V(vo0); PIN_V(vo1); PIN_V(vo2); PIN_V(vo3); PIN_V(lvo); PIN_V(rvo); PIN_S(lso); PIN_S(rso);
    WG_BARRIER();
    _Pragma("nounroll") for (int g = 0; g < 8; ++g) {
      const unsigned so = ((SEQ >> (4 * g)) & 7) << 12;
      PACE((8 + g) * T_STEP);
      LAS void* dst = (LAS void*)(lds + L_V + (g * 4 + w4) * 4096);
      __builtin_amdgcn_raw_ptr_buffer_load_lds(rv, dst, 16, vo0, so, 0, AUX_NT);
      __builtin_amdgcn_raw_ptr_buffer_load_lds(rv, dst, 16, vo1, so, 1024, AUX_NT);
      __builtin_amdgcn_raw_ptr_buffer_load_lds(rv, dst, 16, vo2, so, 2048, AUX_NT);
      __builtin_amdgcn_raw_ptr_buffer_load_lds(rv, dst, 16, vo3, so, 3072, AUX_NT);
      VM_WAIT(8);
      lds_flag(lds, L_PROG + w4 * 4, g + 1);
    }
    VM_WAIT(4);
    lds_flag(lds, L_PROG + w4 * 4, 9);
    const bool before = has_tile && is_wo;
    if (!before) { VM_WAIT(0); WG_BARRIER_BARE(); }
    if (has_tile) {
      LSPIN() { const u32x4 f = lds_peek(lds, F_PV); if (wave_none((f[0] & f[1]) == 0u)) break; }
      PACE(15 * T_STEP + (is_wo ? T_WO : T_DOWN));
      tile_fetch(rl, lvo, lso, 8 * DM * 2, img);
      tile_fetch(rl, lvo, lso + 32 * DM * 2, 8 * DM * 2, img + 4 * 4096);
      if (before) {
        VM_WAIT(8);
        WG_BARRIER_BARE();
        VM_WAIT(4);
        lds_flag(lds, F_T + w4 * 4, 1u);
      }
      VM_DRAIN();
      bf16x8 wf[8];
#pragma unroll
      for (int t = 0; t < 4; ++t) { wf[t] = tr_frag<2048>(ta[t]); wf[4 + t] = tr_frag<2048>(ta[t] + 4 * 4096); }
      if (is_wo) WG_BARRIER();
      u32x4 act[8];
#pragma unroll
      for (int t = 0; t < 4; ++t) { act[2 * t] = lds_ld<u32x4>(aa[t], 0); act[2 * t + 1] = lds_ld<u32x4>(aa[t], 256); }
      SCHED_FENCE();
      f32x4 acc = {0.f, 0.f, 0.f, 0.f};
#pragma unroll
      for (int t = 0; t < 4; ++t) { acc = mma(asb(act[2 * t]), wf[t], acc); acc = mma(asb(act[2 * t + 1]), wf[4 + t], acc); }
      st_sc1(tag4(acc, tag), rws, rvo, rso);
    } else if (is_wo) {
      WG_BARRIER_BARE();
    }
  }
  int role = wave;
  PIN_S(role);
  if (role < 4) {
    u32x4 kr[32], qv;
#define qso (WS_QP + hk * 16384 + (idx >> 3) * 4096 + w4 * 1024)
    const unsigned kvo = (lane & 7) * 512 + ((lane >> 3) & 1) * 64 + (lane >> 4) * 16, seq = SEQ;
    const rsrc_t rk = make_rsrc(p_k + KV_OFF, 32768);
    const unsigned t_go = t_k + T_OFF;
#pragma unroll
    for (int g = 0; g < 8; ++g) {
      const unsigned so = ((seq >> (4 * g)) & 7) << 12;
      PACE(g * T_STEP);
#pragma unroll
      for (int i = 0; i < 4; ++i) kr[g * 4 + i] = __builtin_amdgcn_raw_buffer_load_b128(rk, kvo + i * 128, so, AUX_NT);
    }
    SCHED_FENCE();
    const unsigned kepoch = __builtin_amdgcn_raw_buffer_load_b32(rws, 0, WS_EPOCH, AUX_SC1);
    qv = ld_sc1(rws, l16, qso);
    WG_BARRIER_BARE();
    tag = next_tag(kepoch);
    asm volatile("" : "+s"(tag) : "v"(cpf));
    SPIN(SPIN_MAX) {
      qv ^= tag_nibbles(tag);
      if (wave_none((or4(qv) & 15u) != 0u)) break;
      MEM_FENCE();
      qv = ld_sc1(rws, l16, qso);
    }
    lds_st<unsigned>(lds, L_Q + w4 * 256 + (lane & 31) * 8 + (lane >> 5) * 4,
                     pack_bf16(fold32(asf(qv[0]), asf(qv[2])), fold32(asf(qv[1]), asf(qv[3]))));
    lds_flag(lds, F_Q + w4 * 4, 1u);
    lds_wait_all(lds, F_Q);
    float m0, m1;
    {
      const int qa = L_Q + (lane & 1) * 512 + ((lane >> 1) & 1) * 64 + (lane >> 4) * 16;
      f32x4 c0, c1;
      {
        u32x4 qf[4];
#pragma unroll
        for (int s = 0; s < 4; ++s) qf[s] = lds_ld<u32x4>(lds, qa + s * 128);
        f32x4 c[8];
#pragma unroll
        for (int t = 0; t < 8; ++t) c[t] = mma(asb(qf[0]), asb(kr[t * 4]), f32x4{0.f, 0.f, 0.f, 0.f});
#pragma unroll
        for (int s = 1; s < 4; ++s)
#pragma unroll
          for (int t = 0; t < 8; ++t) c[t] = mma(asb(qf[s]), asb(kr[t * 4 + s]), c[t]);
        const bool g1 = lane & 16, g2 = lane & 32;
        c0 = g2 ? (g1 ? c[3] : c[2]) : (g1 ? c[1] : c[0]);
        c1 = g2 ? (g1 ? c[7] : c[6]) : (g1 ? c[5] : c[4]);
      }
      const bool cb = lane & 8;
      const float r0 = (cb ? c1[2] : c0[0]) + dpp<XOR8>(cb ? c0[2] : c1[0]);
      const float r1 = (cb ? c1[3] : c0[1]) + dpp<XOR8>(cb ? c0[3] : c1[1]);
      const unsigned sp = pack_bf16(r0, r1);
      const float s0 = bf_lo(sp) * 0.0625f, s1 = bf_hi(sp) * 0.0625f;
      float mw = max32(s0, s1);
      mw = row_max(max16(mw, mw));
      *(volatile LAS unsigned*)(lds + L_MX + (lane >> 5) * 16 + w4 * 4) = asu(mw) & 0xfffffffeu;
      u32x4 x0, x1;
      LSPIN() { x0 = lds_peek(lds, L_MX); x1 = lds_peek(lds, L_MX + 16); if (wave_none(unset4(x0) | unset4(x1))) break; }
      m0 = fmaxf(fmaxf(asf(x0[0]), asf(x0[1])), fmaxf(asf(x0[2]), asf(x0[3])));
      m1 = fmaxf(fmaxf(asf(x1[0]), asf(x1[1])), fmaxf(asf(x1[2]), asf(x1[3])));
      const float p0 = __builtin_amdgcn_exp2f((s0 - m0) * LOG2E), p1 = __builtin_amdgcn_exp2f((s1 - m1) * LOG2E);
      const unsigned pk = pack_bf16(p0, p1);
      const int po = L_P + (((lane >> 3) & 1) * 4 + (lane >> 4)) * 64 + ((w4 & 1) * 2 + ((lane >> 2) & 1)) * 16 + ((w4 >> 1) * 4 + (lane & 3)) * 2;
      lds_st<u16>(lds, po, (u16)pk);
      lds_st<u16>(lds, po + 512, (u16)(pk >> 16));
      float l = fold32(p0, p1);
      l = row_sum(fold16(l, l));
      MEM_FENCE();
      *(volatile LAS unsigned*)(lds + L_LS + (lane >> 5) * 16 + w4 * 4) = asu(l) & 0xfffffffeu;
    }
    u32x4 ls;
    LSPIN() { ls = lds_peek(lds, L_LS + (lane >> 5) * 16); if (wave_none(unset4(ls))) break; }
    MEM_FENCE();
    u32x4 tb = tag_bytes(tag);
    unsigned oso = WS_OP + wo_rg * 8192 + w4 * 2048;
    LAS char* ad = lds + L_A + (lane & 4) * 64 + w4 * 64 + ((lane & 1) * 2 + ((lane >> 3) & 1)) * 16 + ((lane & 2) * 2 + (lane >> 4)) * 2;
    LAS char* fa = lds + L_F + w4 * 32 + (lane >> 3) * 4;
    PIN_S(tb); PIN_S(oso); PIN_V(ad); PIN_V(fa);
    if (w4 < 2) {
      const int g = lane >> 4, row = lane >> 2, p = lane & 3, tl = lane & 7;
      LAS char* va[8];
#pragma unroll
      for (int t = 0; t < 8; ++t) va[t] = lds + L_V + row * 512 + ((w4 * 16 + (p >> 1) * 8 + (t ^ (row & 7))) << 4) + 8 * (p & 1);
      LAS char* pa = lds + L_P + ((lane >> 3) & 1) * 512 + g * 16;
      unsigned ovo = (((lane >> 3) & 1) * 4 + (g >> 1)) * 8192 + (g & 1) * 1024 + tl * 16;
      unsigned pso = WS_OP + (hk * 8 + w4 * 2) * 8192 + (idx >> 3) * 2048 + (idx & 7) * 128;
      PIN_V(ovo); PIN_S(pso);
      f32x4 o = {0.f, 0.f, 0.f, 0.f};
      u32x4 pm[8];
      {
        const u32x4 pl = lds_ld<u32x4>(pa, 0);
#pragma unroll
        for (int t = 0; t < 8; ++t) pm[t] = tl == t ? pl : u32x4{0u, 0u, 0u, 0u};
      }
      _Pragma("nounroll") for (int ks = 0; ks < 7; ++ks) {
        LSPIN() {
          const u32x4 pr = lds_peek(lds, L_PROG);
          if (wave_none(min(min(pr[0], pr[1]), min(pr[2], pr[3])) <= (unsigned)(ks + 2))) break;
        }
        lds_flag(lds, F_PV + w4 * 4, ks >= 2 ? 1u : 0u);
        pa += 64;
        const u32x4 pl = lds_ld<u32x4>(pa, 0);
        bf16x8 vf[8];
#pragma unroll
        for (int t = 0; t < 8; ++t) vf[t] = tr_frag<8192>(va[t]);
        SCHED_FENCE();
#pragma unroll
        for (int t = 0; t < 8; ++t) {
          o = mma(vf[t], asb(pm[t]), o);
          SCHED_FENCE();
          pm[t] = tl == t ? pl : u32x4{0u, 0u, 0u, 0u};
          va[t] += 16384;
          PIN_V(pm[t]); PIN_V(va[t]);
          SCHED_FENCE();
        }
      }
      WG_BARRIER_BARE();
      {
        bf16x8 vf[8];
#pragma unroll
        for (int t = 0; t < 8; ++t) vf[t] = tr_frag<8192>(va[t]);
        SCHED_FENCE();
#pragma unroll
        for (int t = 0; t < 8; ++t) o = mma(vf[t], asb(pm[t]), o);
      }
      st_sc1(tag4(o, tag), rws, ovo, pso);
    } else if (w4 == 2) {
      if (late && !is_wo) {
        u32x4 gv[4];
        SPIN(SPIN_MAX) {
          unsigned bad = 0;
#pragma unroll
          for (int u = 0; u < 4; ++u) gv[u] = ld_sc1(rws, l16 + u * 1024, WS_GP + (dn_un >> 1) * 4096);
#pragma unroll
          for (int u = 0; u < 4; ++u) { gv[u] ^= tag_nibbles(tag); bad |= or4(gv[u]); }
          if (wave_none((bad & 15u) != 0u)) break;
          MEM_FENCE();
        }
        const f32x4 gs = asf4(gv[0]) + asf4(gv[1]), us = asf4(gv[2]) + asf4(gv[3]);
        u32x2 hv;
#pragma unroll
        for (int e = 0; e < 2; ++e) {
          const unsigned gt = pack_bf16(gs[2 * e], gs[2 * e + 1]), up = pack_bf16(us[2 * e], us[2 * e + 1]);
          const float g0 = bf_lo(gt), g1 = bf_hi(gt);
          hv[e] = pack_bf16(g0 * (g0 > 0.f ? 1.f : 0.f) * bf_lo(up), g1 * (g1 > 0.f ? 1.f : 0.f) * bf_hi(up));
        }
        lds_st<u32x2>(lds, L_A + (lane & 8) * 32 + (lane >> 4) * 64 + (lane & 3) * 16 + (lane & 4) * 2, hv);
      } else if (is_red) {
        xv = __builtin_amdgcn_raw_buffer_load_b16(make_rsrc(p_x, NB * DM * 2), oi * 2, 0, 0);
        PIN_V(xv);
      }
      WG_BARRIER();
    } else {
      const float lsum = (asf(ls[0]) + asf(ls[1])) + (asf(ls[2]) + asf(ls[3]));
      const float l1 = asf(__builtin_amdgcn_readlane(asu(lsum), 32));
      if (lane == 0) st_sc1(tag4(f32x4{m0, lsum, m1, l1}, tag), rws, 0, WS_ML + (hk * 32 + idx) * 16);
      const unsigned mso = is_wo ? WS_ML + (wo_rg >> 3) * 512 : WS_END;
      LSPIN() { if (wave_none(*(volatile LAS unsigned*)(lds + L_PROG) < 7u)) break; __builtin_amdgcn_s_sleep(4); }
      u32x4 mv = ld_sc1(rws, (lane & 31) * 16, mso);
      WG_BARRIER_BARE();
      mv ^= tb;
      PIN_V(mv);
      if (is_wo) {
        SPIN(SPIN_MAX) {
          if (wave_none((or4(mv) & 255u) != 0u)) break;
          MEM_FENCE();
          mv = ld_sc1(rws, (lane & 31) * 16, mso) ^ tb;
        }
        const float m = asf((idx & 8) ? mv[2] : mv[0]);
        float mx = fmaxf(m, dpp<XOR7>(m));
        mx = fmaxf(mx, dpp<XOR2>(mx));
        mx = fmaxf(mx, dpp<XOR1>(mx));
        const float e = __builtin_amdgcn_exp2f((m - mx) * LOG2E);
        float lt = e * asf((idx & 8) ? mv[3] : mv[1]);
        lt += dpp<XOR7>(lt);
        lt += dpp<XOR2>(lt);
        lt += dpp<XOR1>(lt);
        *(volatile LAS unsigned*)(lds + L_F + (lane & 31) * 4) = asu(e * __builtin_amdgcn_rcpf(lt)) & 0xfffffffeu;
      } else if (bid == 56) {
        SPIN(SPIN_MAX) {
          unsigned bad = 0;
#pragma unroll
          for (int u = 0; u < 4; ++u) bad |= or4(ld_sc1(rws, l16 + u * 1024, WS_ML) ^ tb);
          if (wave_none((bad & 255u) != 0u)) break;
          MEM_FENCE();
        }
        if (lane == 0) __builtin_amdgcn_raw_buffer_store_b32(tag, rws, 0, WS_EPOCH, AUX_SC1);
      }
    }
    if (is_wo) {
      float f;
      LSPIN() {
        f = *(volatile LAS float*)fa;
        if (wave_none((asu(f) == 0xffffffffu) | (*(volatile LAS unsigned*)(lds + F_T) == 0u))) break;
      }
      u32x4 o0, o1;
      MEM_FENCE();
      SPIN(SPIN_MAX) {
        o0 = ld_sc1(rws, l16, oso) ^ tb;
        o1 = ld_sc1(rws, l16 + 1024, oso) ^ tb;
        if (wave_none(((or4(o0) | or4(o1)) & 255u) != 0u)) break;
        MEM_FENCE();
      }
      const float o = fold_pair(asf4(o0) * f, asf4(o1) * f);
      lds_st<u16>(ad, 0, (u16)pack_bf16(o, o));
      WG_BARRIER();
    }
  }

  if (is_red) {
    const u32x4 tb = tag_bytes(tag);
    LAS char* fw = lds + L_FIN + ((lane >> 4) * 8 + (lane & 7)) * 64 + wave * 4 + (lane & 8) * 4;
    LAS char* fr = lds + L_FIN + (lane & 31) * 64 + (lane >> 5) * 32;
    unsigned oo = (lane & 32) ? 0x7fffffffu : oi * 2;
    const bool dn_lane = wave * 64 + lane < 28 * 8;
    unsigned dvo = dn_lane ? l16 + 8192 : 0x7fffffffu;
    u32x4 td = dn_lane ? tb : u32x4{0u, 0u, 0u, 0u};
    float xf = bf_lo(xv);
    PIN_V(fw); PIN_V(fr); PIN_V(oo); PIN_V(xf); PIN_V(dvo); PIN_V(td);
    const rsrc_t ro = make_rsrc(p_out, NB * DM * 2);
    const unsigned so = WS_RP + bid * RP_BOX + wave * 1024;
    u32x4 a0, a1;
    SPIN(SPIN_MAX) {
      a1 = ld_sc1(rws, dvo, so) ^ td;
      a0 = ld_sc1(rws, l16, so) ^ tb;
      if (wave_none(((or4(a0) | or4(a1)) & 255u) != 0u)) break;
      MEM_FENCE();
    }
    lds_st<float>(fw, 0, fold_pair(asf4(a0), asf4(a1)));
    WG_BARRIER();
    if (wave == 2) {
      const f32x4 t = lds_ld<f32x4>(fr, 0) + lds_ld<f32x4>(fr, 16);
      const f32x2 t2 = f32x2{t[0], t[1]} + f32x2{t[2], t[3]};
      const float u = round_bf16(t2[0] + t2[1]);
      auto sw = __builtin_amdgcn_permlane32_swap(asu(u), asu(u), false, false);
      const float sv = asf(sw[0]), dn = asf(sw[1]);
      const float g = round_bf16(dn * __builtin_amdgcn_rcpf(1.0f + __builtin_amdgcn_exp2f(-dn * LOG2E)));
      const float res = xf + round_bf16(sv * g);
      __builtin_amdgcn_raw_buffer_store_b16((u16)pack_bf16(res, res), ro, oo, 0, 0);
    }
  }
}

}

extern "C" void kernel_launch(void* const* d_in, const int* in_sizes, int n_in, void* d_out, int out_size, void* d_ws,
                              size_t ws_size, hipStream_t stream) {
  fused_decode<<<256, 512, 0, stream>>>((const u16*)d_in[0], (const u16*)d_in[1], (const u16*)d_in[2], (const u16*)d_in[3],
                                        (const u16*)d_in[4], (const u16*)d_in[6], (const u16*)d_in[7], (const u16*)d_in[8],
                                        (u16*)d_out, (char*)d_ws);
}
```

```cpp
#include <hip/hip_runtime.h>
#include <hip/hip_bf16.h>
#include <stdint.h>

namespace {

typedef unsigned short u16;
typedef unsigned long long u64;
typedef unsigned u32x2 __attribute__((ext_vector_type(2)));
typedef unsigned u32x4 __attribute__((ext_vector_type(4)));
typedef float f32x2 __attribute__((ext_vector_type(2)));
typedef float f32x4 __attribute__((ext_vector_type(4)));
typedef short s16x4 __attribute__((ext_vector_type(4)));
typedef __bf16 bf16x2 __attribute__((ext_vector_type(2)));
typedef __bf16 bf16x8 __attribute__((ext_vector_type(8)));
#define LAS __attribute__((address_space(3)))
typedef __amdgpu_buffer_rsrc_t rsrc_t;

constexpr int NB = 4, DM = 448, SK = 8192, WIN = 2048, FF = 1792, QW = 4096;
constexpr int AUX_SC1 = 16, AUX_NT = 2;
constexpr unsigned SPIN_MAX = 1u << 22;
constexpr unsigned CODE_BYTES = 10640;
constexpr int T_FIRST = 58;
constexpr int T_STAGGER = 14;
constexpr int T_STEP = 56;
constexpr int T_WO = 60, T_DOWN = 70;
constexpr float LOG2E = 1.4426950408889634f;

constexpr unsigned WS_EPOCH = 0;
constexpr unsigned WS_QP = 4096;
constexpr unsigned WS_GP = WS_QP + 8 * 4 * 4096;
constexpr unsigned WS_OP = WS_GP + 28 * 4096;
constexpr unsigned WS_ML = WS_OP + 64 * 8192;
constexpr unsigned WS_RP = WS_ML + 4096;
constexpr unsigned RP_BOX = 92 * 128;
constexpr unsigned WS_END = WS_RP + 56 * RP_BOX;

constexpr int L_V = 0;
constexpr int L_TILE = L_V;
constexpr int L_XN = L_V + 32768;
constexpr int L_FIN = L_V;
constexpr int L_Q = 131072;
constexpr int L_P = L_Q + 1024;
constexpr int L_A = L_P + 1024;
constexpr int L_SYNC = L_A + 512;
constexpr int L_ZERO = L_SYNC;
constexpr int F_XN = L_SYNC + 16, F_EP = L_SYNC + 32, F_Q = L_SYNC + 48, L_PROG = L_SYNC + 64, F_PV = L_SYNC + 80, F_T = L_SYNC + 96;
constexpr int L_F = L_SYNC + 128;
constexpr int L_MX = L_SYNC + 384;
constexpr int L_LS = L_MX + 32;
constexpr int L_END = L_SYNC + 512;

#define SCHED_FENCE() __builtin_amdgcn_sched_barrier(0)
#define MEM_FENCE() asm volatile("" ::: "memory")
#define WG_BARRIER() do { SCHED_FENCE(); asm volatile("s_waitcnt lgkmcnt(0)\n\ts_barrier" ::: "memory"); SCHED_FENCE(); } while (0)
#define WG_BARRIER_BARE() do { SCHED_FENCE(); asm volatile("s_barrier" ::: "memory"); SCHED_FENCE(); } while (0)
#define VM_WAIT(n) do { SCHED_FENCE(); asm volatile("s_waitcnt vmcnt(%0)" :: "i"(n) : "memory"); SCHED_FENCE(); } while (0)
#define VM_DRAIN() do { SCHED_FENCE(); __builtin_amdgcn_s_waitcnt(0x0F70); SCHED_FENCE(); MEM_FENCE(); } while (0)
#define NAP() __builtin_amdgcn_s_sleep(1)
#define PACE(t) do { SCHED_FENCE(); pace_until(t_go + (t)); SCHED_FENCE(); } while (0)
#define PIN_V(x) asm volatile("" : "+v"(x))
#define PIN_S(x) asm volatile("" : "+s"(x))
#define LSPIN() _Pragma("nounroll") for (;;)
#define SPIN(limit) _Pragma("nounroll") for (unsigned s_ = 0; s_ < (limit); ++s_)

__device__ __forceinline__ void pace_until(unsigned when) {
  asm volatile("1:\n\ts_memrealtime vcc\n\ts_waitcnt lgkmcnt(0)\n\ts_sub_i32 vcc_lo, vcc_lo, %0\n\ts_cmp_lt_i32 vcc_lo, 0\n\ts_cbranch_scc1 1b"
               :: "s"(when) : "vcc", "scc", "memory");
}
__device__ __forceinline__ float asf(unsigned u) { return __builtin_bit_cast(float, u); }
__device__ __forceinline__ unsigned asu(float f) { return __builtin_bit_cast(unsigned, f); }
__device__ __forceinline__ unsigned pack_bf16(float lo, float hi) {
  f32x2 f = {lo, hi};
  return __builtin_bit_cast(unsigned, __builtin_convertvector(f, bf16x2));
}
__device__ __forceinline__ float round_bf16(float v) { return asf(pack_bf16(0.f, v)); }
__device__ __forceinline__ float bf_lo(unsigned u) { return asf(u << 16); }
__device__ __forceinline__ float bf_hi(unsigned u) { return asf(u & 0xffff0000u); }
template <int CTRL> __device__ __forceinline__ float dpp(float x) {
  return asf((unsigned)__builtin_amdgcn_mov_dpp((int)asu(x), CTRL, 0xf, 0xf, true));
}
constexpr int XOR1 = 0xB1, XOR2 = 0x4E, XOR7 = 0x141, XOR8 = 0x128;
__device__ __forceinline__ float fold16(float a, float b) {
  auto s = __builtin_amdgcn_permlane16_swap(asu(a), asu(b), false, false);
  return asf(s[0]) + asf(s[1]);
}
__device__ __forceinline__ float fold32(float a, float b) {
  auto s = __builtin_amdgcn_permlane32_swap(asu(a), asu(b), false, false);
  return asf(s[0]) + asf(s[1]);
}
__device__ __forceinline__ float max16(float a, float b) {
  auto s = __builtin_amdgcn_permlane16_swap(asu(a), asu(b), false, false);
  return fmaxf(asf(s[0]), asf(s[1]));
}
__device__ __forceinline__ float max32(float a, float b) {
  auto s = __builtin_amdgcn_permlane32_swap(asu(a), asu(b), false, false);
  return fmaxf(asf(s[0]), asf(s[1]));
}
__device__ __forceinline__ float row_sum(float v) {
  v += dpp<XOR8>(v); v += dpp<XOR7>(v); v += dpp<XOR2>(v); v += dpp<XOR1>(v);
  return v;
}
__device__ __forceinline__ float wave_sum(float v) { v = fold32(v, v); return row_sum(fold16(v, v)); }
__device__ __forceinline__ float row_max(float v) {
  v = fmaxf(v, dpp<XOR8>(v)); v = fmaxf(v, dpp<XOR7>(v)); v = fmaxf(v, dpp<XOR2>(v)); v = fmaxf(v, dpp<XOR1>(v));
  return v;
}
__device__ __forceinline__ float fold_pair(f32x4 a, f32x4 b) {
  const float a0 = fold32(a[0], a[2]), a1 = fold32(a[1], a[3]), b0 = fold32(b[0], b[2]), b1 = fold32(b[1], b[3]);
  SCHED_FENCE();
  const float za = fold16(a0, a1), zb = fold16(b0, b1);
  const bool hi = __builtin_amdgcn_inverse_ballot_w64(0xff00ff00ff00ff00ull);
  return (hi ? zb : za) + dpp<XOR8>(hi ? za : zb);
}
__device__ __forceinline__ rsrc_t make_rsrc(u64 p, unsigned bytes) {
  return __builtin_amdgcn_make_buffer_rsrc((void*)p, 0, bytes, 0x00020000);
}
__device__ __forceinline__ u32x4 tag4(f32x4 v, unsigned tag) {
  u32x4 r;
  r[0] = __builtin_amdgcn_perm(asu(v[0]), tag, 0x07060500u);
  r[1] = __builtin_amdgcn_perm(asu(v[1]), tag, 0x07060501u);
  r[2] = __builtin_amdgcn_perm(asu(v[2]), tag, 0x07060502u);
  r[3] = __builtin_amdgcn_perm(asu(v[3]), tag, 0x07060503u);
  return r;
}
__device__ __forceinline__ unsigned next_tag(unsigned epoch) {
  const unsigned e = __builtin_amdgcn_readfirstlane(epoch);
  return ((e & 0x7f7f7f7fu) + 0x07050301u) ^ (e & 0x80808080u);
}
__device__ __forceinline__ u32x4 tag_bytes(unsigned tag) { return u32x4{tag & 255u, (tag >> 8) & 255u, (tag >> 16) & 255u, tag >> 24}; }
__device__ __forceinline__ u32x4 tag_nibbles(unsigned tag) { return u32x4{tag & 15u, (tag >> 8) & 15u, (tag >> 16) & 15u, (tag >> 24) & 15u}; }
__device__ __forceinline__ u32x4 tag4_fine(f32x4 v, unsigned tag) {
  return ((__builtin_bit_cast(u32x4, v) + 8u) & 0xfffffff0u) | tag_nibbles(tag);
}
__device__ __forceinline__ unsigned or4(u32x4 v) { return v[0] | v[1] | v[2] | v[3]; }
__device__ __forceinline__ f32x4 asf4(u32x4 v) { return __builtin_bit_cast(f32x4, v); }
__device__ __forceinline__ u32x4 ld_sc1(rsrc_t r, unsigned voff, unsigned soff) {
  return __builtin_amdgcn_raw_buffer_load_b128(r, voff, soff, AUX_SC1);
}
__device__ __forceinline__ void st_sc1(u32x4 v, rsrc_t r, unsigned voff, unsigned soff) {
  __builtin_amdgcn_raw_buffer_store_b128(v, r, voff, soff, AUX_SC1);
}
__device__ __forceinline__ bool wave_none(bool bad) { return __builtin_amdgcn_ballot_w64(bad) == 0ull; }

template <typename T> __device__ __forceinline__ T lds_ld(LAS char* l, int off) { return *(LAS T*)(l + off); }
template <typename T> __device__ __forceinline__ void lds_st(LAS char* l, int off, T v) { *(LAS T*)(l + off) = v; }
__device__ __forceinline__ void lds_flag(LAS char* l, int off, unsigned v) {
  MEM_FENCE();
  *(volatile LAS unsigned*)(l + off) = v;
  MEM_FENCE();
}
__device__ __forceinline__ u32x4 lds_peek(LAS char* l, int off) { return *(volatile LAS u32x4*)(l + off); }
__device__ __forceinline__ void lds_wait_all(LAS char* l, int off) {
  LSPIN() { const u32x4 f = lds_peek(l, off); if (wave_none((f[0] & f[1] & f[2] & f[3]) == 0u)) break; NAP(); }
  MEM_FENCE();
}
__device__ __forceinline__ bool unset4(u32x4 v) { return max(max(v[0], v[1]), max(v[2], v[3])) == 0xffffffffu; }

__device__ __forceinline__ void tile_fetch(rsrc_t r, unsigned voff, unsigned soff, unsigned step, LAS char* img) {
  __builtin_amdgcn_raw_ptr_buffer_load_lds(r, (LAS void*)img, 16, voff, soff, 0, AUX_NT);
  __builtin_amdgcn_raw_ptr_buffer_load_lds(r, (LAS void*)img, 16, voff, soff + step, 1024, AUX_NT);
  __builtin_amdgcn_raw_ptr_buffer_load_lds(r, (LAS void*)img, 16, voff, soff + 2 * step, 2048, AUX_NT);
  __builtin_amdgcn_raw_ptr_buffer_load_lds(r, (LAS void*)img, 16, voff, soff + 3 * step, 3072, AUX_NT);
}
template <int GAP> __device__ __forceinline__ bf16x8 tr_frag(LAS char* a) {
  const s16x4 lo = __builtin_amdgcn_ds_read_tr16_b64_v4i16((LAS s16x4*)a);
  const s16x4 hi = __builtin_amdgcn_ds_read_tr16_b64_v4i16((LAS s16x4*)(a + GAP));
  return __builtin_bit_cast(bf16x8, __builtin_shufflevector(lo, hi, 0, 1, 2, 3, 4, 5, 6, 7));
}
__device__ __forceinline__ void tile_frags(LAS char* img, int lane, bf16x8 (&w)[4]) {
  const int row = lane >> 2, p = lane & 3;
#pragma unroll
  for (int t = 0; t < 4; ++t) w[t] = tr_frag<2048>(img + row * 128 + (((2 * t + (p >> 1)) ^ (row & 6)) << 4) + 8 * (p & 1));
}
__device__ __forceinline__ f32x4 mma(bf16x8 a, bf16x8 b, f32x4 c) { return __builtin_amdgcn_mfma_f32_16x16x32_bf16(a, b, c, 0, 0, 0); }
__device__ __forceinline__ bf16x8 asb(u32x4 v) { return __builtin_bit_cast(bf16x8, v); }

__global__ void __launch_bounds__(512, 2)
fused_decode(const u16* x, const u16* kc, const u16* vc, const u16* wq, const u16* wo, const u16* wg, const u16* wu,
             const u16* wd, u16* out, char* ws) {
  __shared__ __attribute__((aligned(16))) char lds_raw[L_END];
  LAS char* lds = (LAS char*)lds_raw;
  const int bid = blockIdx.x, lane = threadIdx.x & 63, wave = __builtin_amdgcn_readfirstlane(threadIdx.x >> 6);
  const int hk = bid & 7, idx = bid >> 3, w4 = wave & 3;
  const bool loader = wave >= 4;
  const u64 p_x = (u64)x, p_k = (u64)kc, p_v = (u64)vc, p_wq = (u64)wq, p_wo = (u64)wo, p_wg = (u64)wg, p_wu = (u64)wu, p_wd = (u64)wd;
  const u64 p_out = (u64)out, p_ws = (u64)ws;
#define ARGS_IN "s"(p_x), "s"(p_k), "s"(p_v), "s"(p_wq), "s"(p_wo), "s"(p_wg), "s"(p_wu), "s"(p_wd), "s"(p_out), "s"(p_ws)
#define KV_OFF ((((idx >> 3) * 8 + hk) * SK + (SK - WIN) + (idx & 7) * 256) * 512 + w4 * 32768)
#define SEQ ((unsigned)(0x12075643120ull >> ((idx & 3) * 4)))
#define T_OFF (T_FIRST + (3 - (idx >> 3)) * T_STAGGER)
#define is_wo (idx >= 16)
#define late (idx >= 9)
#define is_red (idx < 7)
#define wo_rg (((hk + 1 + ((idx >> 1) & 3)) & 7) * 8 + ((idx >> 1) & 7))
#define dn_un (bid - 72)
#define late_rg (is_wo ? wo_rg : dn_un >> 1)
#define late_ch ((is_wo ? idx : dn_un) & 1)
#define oi ((bid & 3) * DM + (bid >> 2) * 32 + (lane & 31))
#define rws make_rsrc(p_ws, WS_END)
  const unsigned l16 = lane * 16;
  unsigned tag = 0, xv = 0, cpf = 0, t_k = 0;

  if (!loader) {
    const unsigned iv = (lane & 16) ? 0xffffffffu : 0u;
    lds_st<u32x2>(lds, L_SYNC + lane * 8, u32x2{iv, iv});
    cpf = __builtin_amdgcn_raw_buffer_load_b32(make_rsrc(__builtin_amdgcn_s_getpc() & ~127ull, CODE_BYTES), 0, (idx * 4 + w4) * 128, 0);
    asm volatile("" :: ARGS_IN);
    WG_BARRIER();
    t_k = (unsigned)__builtin_amdgcn_s_memrealtime();
    PIN_S(t_k);
  }
  MEM_FENCE();
  if (loader) {
    const bool is_q = idx < 16, is_gate = idx < 23, has_early = idx < 30;
    const int half = hk & 1;
    int et_q = idx + 16 * ((idx + (hk >> 1)) & 3), et_f = (idx - (is_gate ? 16 : 23)) * 4 + (hk >> 1);
    asm("" : "+s"(et_q), "+s"(et_f));
    const int etile = is_q ? et_q : et_f;
    const int r8 = lane >> 3;
    LAS char* img = lds + L_TILE + w4 * 4096;
    const unsigned stride = is_q ? QW * 2 : FF * 2;
    unsigned evo = r8 * stride + (((lane & 7) ^ (r8 & 6)) << 4), eso = (half * 224 + w4 * 32) * stride + etile * 128;
    unsigned ebytes = has_early ? DM * stride : 0, eso2 = eso + 128 * stride, estep = 8 * stride - 1024;
    PIN_V(evo);
    asm volatile("" : "+s"(eso), "+s"(estep), "+s"(ebytes), "+s"(eso2));
    SCHED_FENCE();
    asm volatile("" :: ARGS_IN : "memory");
    {
      const rsrc_t rw = make_rsrc(is_q ? p_wq : (is_gate ? p_wg : p_wu), ebytes);
      tile_fetch(rw, evo, eso, estep, img);
      if (w4 < 3) tile_fetch(rw, evo, eso2, estep, img + 4 * 4096);
    }
    WG_BARRIER_BARE();
    const unsigned t_go = (unsigned)__builtin_amdgcn_s_memrealtime() + T_OFF;
    SCHED_FENCE();
    const u32x4 xr = __builtin_amdgcn_raw_buffer_load_b128(make_rsrc(p_x, NB * DM * 2), l16, w4 * DM * 2, 0);
    const unsigned epoch = __builtin_amdgcn_raw_buffer_load_b32(rws, 0, WS_EPOCH, AUX_SC1);
    SCHED_FENCE();
    {
      float v[8], t = 0.f;
#pragma unroll
      for (int k = 0; k < 4; ++k) { v[2 * k] = bf_lo(xr[k]); v[2 * k + 1] = bf_hi(xr[k]); t += v[2 * k] + v[2 * k + 1]; }
      const float mean = wave_sum(lane < 56 ? t : 0.f) * (1.0f / DM);
      t = 0.f;
#pragma unroll
      for (int k = 0; k < 8; ++k) { const float d = v[k] - mean; t = fmaf(d, d, t); }
      const float var = round_bf16(wave_sum(lane < 56 ? t : 0.f) * (1.0f / DM)), mb = round_bf16(mean);
      const float rc = __builtin_amdgcn_rcpf(round_bf16(__builtin_amdgcn_sqrtf(round_bf16(var + 1e-5f))));
      u32x4 xn;
#pragma unroll
      for (int k = 0; k < 4; ++k) {
        const unsigned d = pack_bf16(v[2 * k] - mb, v[2 * k + 1] - mb);
        xn[k] = pack_bf16(bf_lo(d) * rc, bf_hi(d) * rc);
      }
      lds_st<u32x4>(lds, L_XN + w4 * 1024 + lane * 16, xn);
      lds_flag(lds, F_XN + w4 * 4, 1u);
      lds_wait_all(lds, F_XN);
      tag = next_tag(epoch);
      PIN_S(tag);
      const int bt = lane & 3, g = lane >> 4, jj = (lane >> 2) & 3;
      const int xo = L_XN + bt * 1024 + (half * 224 + w4 * 32 + 4 * g) * 2;
      const u32x2 a0 = lds_ld<u32x2>(lds, xo), a1 = lds_ld<u32x2>(lds, xo + 32);
      const u32x2 a2 = lds_ld<u32x2>(lds, xo + 256), a3 = lds_ld<u32x2>(lds, xo + 288);
      const u32x4 act0 = {a0[0], a0[1], a1[0], a1[1]}, act1 = {a2[0], a2[1], a3[0], a3[1]}, zero = {0u, 0u, 0u, 0u};
      VM_DRAIN();
      bf16x8 wf0[4], wf1[4];
      tile_frags(img, lane, wf0);
      f32x4 acc = {0.f, 0.f, 0.f, 0.f};
      if (w4 < 3) {
        tile_frags(img + 4 * 4096, lane, wf1);
#pragma unroll
        for (int t = 0; t < 4; ++t) acc = mma(wf1[t], asb(jj == t ? act1 : zero), acc);
      }
#pragma unroll
      for (int t = 0; t < 4; ++t) acc = mma(wf0[t], asb(jj == t ? act0 : zero), acc);
      lds_st<f32x4>(img, bt * 256 + ((lane & 12) + g) * 16, acc);
      lds_flag(lds, F_EP + w4 * 4, 1u);
      if (w4 == 3 && has_early) {
        const unsigned voff = is_q ? WS_QP + (etile >> 3) * 16384 + (lane >> 4) * 4096 + (etile & 6) * 512 + half * 512 + (etile & 1) * 256 + (lane & 15) * 16
                                   : WS_GP + etile * 4096 + (is_gate ? 0 : 2048) + half * 1024 + l16;
        lds_wait_all(lds, F_EP);
        f32x4 sum = lds_ld<f32x4>(lds, L_TILE + lane * 16);
#pragma unroll
        for (int p = 1; p < 4; ++p) sum += lds_ld<f32x4>(lds, L_TILE + p * 4096 + lane * 16);
        st_sc1(tag4_fine(sum, tag), rws, voff, 0);
      }
    }
    const rsrc_t rv = make_rsrc(p_v + KV_OFF, 32768);
    const unsigned hf = lane >> 5;
    unsigned vo0 = (lane ^ hf) * 16, vo1 = (lane ^ (2 + hf)) * 16, vo2 = (lane ^ (4 + hf)) * 16, vo3 = (lane ^ (6 + hf)) * 16;
    const int lcol = late_ch * 4 + w4;
    const bool has_tile = late && lcol < 7;
    const rsrc_t rl = make_rsrc(is_wo ? p_wo : p_wd, (is_wo ? QW : FF) * DM * 2);
    unsigned lvo = r8 * (DM * 2) + (((lane & 7) ^ (r8 >> 1)) << 4), lso = late_rg * (64 * DM * 2) + lcol * 128;
    unsigned rvo = (((lane >> 3) & 1) * 4 + (lane >> 4)) * RP_BOX + (lane & 7) * 16;
    unsigned rso = WS_RP + lcol * 8 * RP_BOX + ((is_wo ? 0 : 64) + late_rg) * 128;
    LAS char *aa[4], *ta[4];
#pragma unroll
    for (int t = 0; t < 4; ++t) {
      aa[t] = lds + ((lane & 3) == t ? L_A + ((lane >> 2) & 3) * 64 + (lane >> 4) * 16 : L_ZERO);
      ta[t] = img + (lane >> 2) * 128 + (((lane & 2) * 2 + (t ^ ((lane >> 3) & 3))) << 4) + 8 * (lane & 1);
      PIN_V(aa[t]); PIN_V(ta[t]);
    }
    PIN_V(vo0); PIN_V(vo1); PIN_V(vo2); PIN_V(vo3); PIN_V(lvo); PIN_V(rvo); PIN_S(lso); PIN_S(rso);
    WG_BARRIER();
    _Pragma("nounroll") for (int g = 0; g < 8; ++g) {
      const unsigned so = ((SEQ >> (4 * g)) & 7) << 12;
      PACE((8 + g) * T_STEP);
      LAS void* dst = (LAS void*)(lds + L_V + (g * 4 + w4) * 4096);
      __builtin_amdgcn_raw_ptr_buffer_load_lds(rv, dst, 16, vo0, so, 0, AUX_NT);
      __builtin_amdgcn_raw_ptr_buffer_load_lds(rv, dst, 16, vo1, so, 1024, AUX_NT);
      __builtin_amdgcn_raw_ptr_buffer_load_lds(rv, dst, 16, vo2, so, 2048, AUX_NT);
      __builtin_amdgcn_raw_ptr_buffer_load_lds(rv, dst, 16, vo3, so, 3072, AUX_NT);
      VM_WAIT(8);
      lds_flag(lds, L_PROG + w4 * 4, g + 1);
    }
    VM_WAIT(4);
    lds_flag(lds, L_PROG + w4 * 4, 9);
    const bool before = has_tile && is_wo;
    if (!before) { VM_WAIT(0); WG_BARRIER_BARE(); }
    if (has_tile) {
      LSPIN() { const u32x4 f = lds_peek(lds, F_PV); if (wave_none((f[0] & f[1]) == 0u)) break; }
      PACE(15 * T_STEP + (is_wo ? T_WO : T_DOWN));
      tile_fetch(rl, lvo, lso, 8 * DM * 2 - 1024, img);
      tile_fetch(rl, lvo, lso + 32 * DM * 2, 8 * DM * 2 - 1024, img + 4 * 4096);
      if (before) {
        VM_WAIT(8);
        WG_BARRIER_BARE();
        VM_WAIT(2);
        lds_flag(lds, F_T + w4 * 4, 1u);
      }
      VM_DRAIN();
      bf16x8 wf[8];
#pragma unroll
      for (int t = 0; t < 4; ++t) { wf[t] = tr_frag<2048>(ta[t]); wf[4 + t] = tr_frag<2048>(ta[t] + 4 * 4096); }
      if (is_wo) WG_BARRIER();
      u32x4 act[8];
#pragma unroll
      for (int t = 0; t < 4; ++t) { act[2 * t] = lds_ld<u32x4>(aa[t], 0); act[2 * t + 1] = lds_ld<u32x4>(aa[t], 256); }
      SCHED_FENCE();
      f32x4 acc = {0.f, 0.f, 0.f, 0.f};
#pragma unroll
      for (int t = 0; t < 4; ++t) { acc = mma(asb(act[2 * t]), wf[t], acc); acc = mma(asb(act[2 * t + 1]), wf[4 + t], acc); }
      st_sc1(tag4(acc, tag), rws, rvo, rso);
    } else if (is_wo) {
      WG_BARRIER_BARE();
    }
  }
  int role = wave;
  PIN_S(role);
  if (role < 4) {
    u32x4 kr[32], qv;
#define qso (WS_QP + hk * 16384 + (idx >> 3) * 4096 + w4 * 1024)
    const unsigned kvo = (lane & 7) * 512 + ((lane >> 3) & 1) * 64 + (lane >> 4) * 16, seq = SEQ;
    const rsrc_t rk = make_rsrc(p_k + KV_OFF, 32768);
    const unsigned t_go = t_k + T_OFF;
#pragma unroll
    for (int g = 0; g < 8; ++g) {
      const unsigned so = ((seq >> (4 * g)) & 7) << 12;
      PACE(g * T_STEP);
#pragma unroll
      for (int i = 0; i < 4; ++i) kr[g * 4 + i] = __builtin_amdgcn_raw_buffer_load_b128(rk, kvo + i * 128, so, AUX_NT);
    }
    SCHED_FENCE();
    const unsigned kepoch = __builtin_amdgcn_raw_buffer_load_b32(rws, 0, WS_EPOCH, AUX_SC1);
    qv = ld_sc1(rws, l16, qso);
    WG_BARRIER_BARE();
    tag = next_tag(kepoch);
    asm volatile("" : "+s"(tag) : "v"(cpf));
    SPIN(SPIN_MAX) {
      qv ^= tag_nibbles(tag);
      if (wave_none((or4(qv) & 15u) != 0u)) break;
      MEM_FENCE();
      qv = ld_sc1(rws, l16, qso);
    }
    lds_st<unsigned>(lds, L_Q + w4 * 256 + (lane & 31) * 8 + (lane >> 5) * 4,
                     pack_bf16(fold32(asf(qv[0]), asf(qv[2])), fold32(asf(qv[1]), asf(qv[3]))));
    lds_flag(lds, F_Q + w4 * 4, 1u);
    lds_wait_all(lds, F_Q);
    float m0, m1;
    {
      const int qa = L_Q + (lane & 1) * 512 + ((lane >> 1) & 1) * 64 + (lane >> 4) * 16;
      f32x4 c0, c1;
      {
        u32x4 qf[4];
#pragma unroll
        for (int s = 0; s < 4; ++s) qf[s] = lds_ld<u32x4>(lds, qa + s * 128);
        f32x4 c[8];
#pragma unroll
        for (int t = 0; t < 8; ++t) c[t] = mma(asb(qf[0]), asb(kr[t * 4]), f32x4{0.f, 0.f, 0.f, 0.f});
#pragma unroll
        for (int s = 1; s < 4; ++s)
#pragma unroll
          for (int t = 0; t < 8; ++t) c[t] = mma(asb(qf[s]), asb(kr[t * 4 + s]), c[t]);
        const bool g1 = lane & 16, g2 = lane & 32;
        c0 = g2 ? (g1 ? c[3] : c[2]) : (g1 ? c[1] : c[0]);
        c1 = g2 ? (g1 ? c[7] : c[6]) : (g1 ? c[5] : c[4]);
      }
      const bool cb = lane & 8;
      const float r0 = (cb ? c1[2] : c0[0]) + dpp<XOR8>(cb ? c0[2] : c1[0]);
      const float r1 = (cb ? c1[3] : c0[1]) + dpp<XOR8>(cb ? c0[3] : c1[1]);
      const unsigned sp = pack_bf16(r0, r1);
      const float s0 = bf_lo(sp) * 0.0625f, s1 = bf_hi(sp) * 0.0625f;
      float mw = max32(s0, s1);
      mw = row_max(max16(mw, mw));
      *(volatile LAS unsigned*)(lds + L_MX + (lane >> 5) * 16 + w4 * 4) = asu(mw) & 0xfffffffeu;
      u32x4 x0, x1;
      LSPIN() { x0 = lds_peek(lds, L_MX); x1 = lds_peek(lds, L_MX + 16); if (wave_none(unset4(x0) | unset4(x1))) break; }
      m0 = fmaxf(fmaxf(asf(x0[0]), asf(x0[1])), fmaxf(asf(x0[2]), asf(x0[3])));
      m1 = fmaxf(fmaxf(asf(x1[0]), asf(x1[1])), fmaxf(asf(x1[2]), asf(x1[3])));
      const float p0 = __builtin_amdgcn_exp2f((s0 - m0) * LOG2E), p1 = __builtin_amdgcn_exp2f((s1 - m1) * LOG2E);
      const unsigned pk = pack_bf16(p0, p1);
      const int po = L_P + (((lane >> 3) & 1) * 4 + (lane >> 4)) * 64 + ((w4 & 1) * 2 + ((lane >> 2) & 1)) * 16 + ((w4 >> 1) * 4 + (lane & 3)) * 2;
      lds_st<u16>(lds, po, (u16)pk);
      lds_st<u16>(lds, po + 512, (u16)(pk >> 16));
      float l = fold32(p0, p1);
      l = row_sum(fold16(l, l));
      MEM_FENCE();
      *(volatile LAS unsigned*)(lds + L_LS + (lane >> 5) * 16 + w4 * 4) = asu(l) & 0xfffffffeu;
    }
    u32x4 ls;
    LSPIN() { ls = lds_peek(lds, L_LS + (lane >> 5) * 16); if (wave_none(unset4(ls))) break; }
    MEM_FENCE();
    u32x4 tb = tag_bytes(tag);
    unsigned oso = WS_OP + wo_rg * 8192 + w4 * 2048;
    LAS char* ad = lds + L_A + (lane & 4) * 64 + w4 * 64 + ((lane & 1) * 2 + ((lane >> 3) & 1)) * 16 + ((lane & 2) * 2 + (lane >> 4)) * 2;
    LAS char* fa = lds + L_F + w4 * 32 + (lane >> 3) * 4;
    PIN_S(tb); PIN_S(oso); PIN_V(ad); PIN_V(fa);
    if (w4 < 2) {
      const int g = lane >> 4, row = lane >> 2, p = lane & 3, tl = lane & 7;
      LAS char* va[8];
#pragma unroll
      for (int t = 0; t < 8; ++t) va[t] = lds + L_V + row * 512 + ((w4 * 16 + (p >> 1) * 8 + (t ^ (row & 7))) << 4) + 8 * (p & 1);
      LAS char* pa = lds + L_P + ((lane >> 3) & 1) * 512 + g * 16;
      unsigned ovo = (((lane >> 3) & 1) * 4 + (g >> 1)) * 8192 + (g & 1) * 1024 + tl * 16;
      unsigned pso = WS_OP + (hk * 8 + w4 * 2) * 8192 + (idx >> 3) * 2048 + (idx & 7) * 128;
      PIN_V(ovo); PIN_S(pso);
      f32x4 o = {0.f, 0.f, 0.f, 0.f};
      u32x4 pm[8];
      {
        const u32x4 pl = lds_ld<u32x4>(pa, 0);
#pragma unroll
        for (int t = 0; t < 8; ++t) pm[t] = tl == t ? pl : u32x4{0u, 0u, 0u, 0u};
      }
      _Pragma("nounroll") for (int ks = 0; ks < 7; ++ks) {
        LSPIN() {
          const u32x4 pr = lds_peek(lds, L_PROG);
          if (wave_none(min(min(pr[0], pr[1]), min(pr[2], pr[3])) <= (unsigned)(ks + 2))) break;
        }
        lds_flag(lds, F_PV + w4 * 4, ks >= 2 ? 1u : 0u);
        pa += 64;
        const u32x4 pl = lds_ld<u32x4>(pa, 0);
        bf16x8 vf[8];
#pragma unroll
        for (int t = 0; t < 8; ++t) vf[t] = tr_frag<8192>(va[t]);
        SCHED_FENCE();
#pragma unroll
        for (int t = 0; t < 8; ++t) {
          o = mma(vf[t], asb(pm[t]), o);
          SCHED_FENCE();
          pm[t] = tl == t ? pl : u32x4{0u, 0u, 0u, 0u};
          va[t] += 16384;
          PIN_V(pm[t]); PIN_V(va[t]);
          SCHED_FENCE();
        }
      }
      WG_BARRIER_BARE();
      {
        bf16x8 vf[8];
#pragma unroll
        for (int t = 0; t < 8; ++t) vf[t] = tr_frag<8192>(va[t]);
        SCHED_FENCE();
#pragma unroll
        for (int t = 0; t < 8; ++t) o = mma(vf[t], asb(pm[t]), o);
      }
      st_sc1(tag4(o, tag), rws, ovo, pso);
    } else if (w4 == 2) {
      if (late && !is_wo) {
        u32x4 gv[4];
        SPIN(SPIN_MAX) {
          unsigned bad = 0;
#pragma unroll
          for (int u = 0; u < 4; ++u) gv[u] = ld_sc1(rws, l16 + u * 1024, WS_GP + (dn_un >> 1) * 4096);
#pragma unroll
          for (int u = 0; u < 4; ++u) { gv[u] ^= tag_nibbles(tag); bad |= or4(gv[u]); }
          if (wave_none((bad & 15u) != 0u)) break;
          MEM_FENCE();
        }
        const f32x4 gs = asf4(gv[0]) + asf4(gv[1]), us = asf4(gv[2]) + asf4(gv[3]);
        u32x2 hv;
#pragma unroll
        for (int e = 0; e < 2; ++e) {
          const unsigned gt = pack_bf16(gs[2 * e], gs[2 * e + 1]), up = pack_bf16(us[2 * e], us[2 * e + 1]);
          const float g0 = bf_lo(gt), g1 = bf_hi(gt);
          hv[e] = pack_bf16(g0 * (g0 > 0.f ? 1.f : 0.f) * bf_lo(up), g1 * (g1 > 0.f ? 1.f : 0.f) * bf_hi(up));
        }
        lds_st<u32x2>(lds, L_A + (lane & 8) * 32 + (lane >> 4) * 64 + (lane & 3) * 16 + (lane & 4) * 2, hv);
      } else if (is_red) {
        xv = __builtin_amdgcn_raw_buffer_load_b16(make_rsrc(p_x, NB * DM * 2), oi * 2, 0, 0);
        PIN_V(xv);
      }
      WG_BARRIER();
    } else {
      const float lsum = (asf(ls[0]) + asf(ls[1])) + (asf(ls[2]) + asf(ls[3]));
      const float l1 = asf(__builtin_amdgcn_readlane(asu(lsum), 32));
      if (lane == 0) st_sc1(tag4(f32x4{m0, lsum, m1, l1}, tag), rws, 0, WS_ML + (hk * 32 + idx) * 16);
      const unsigned mso = is_wo ? WS_ML + (wo_rg >> 3) * 512 : WS_END;
      LSPIN() { if (wave_none(*(volatile LAS unsigned*)(lds + L_PROG) < 7u)) break; __builtin_amdgcn_s_sleep(4); }
      u32x4 mv = ld_sc1(rws, (lane & 31) * 16, mso);
      WG_BARRIER_BARE();
      mv ^= tb;
      PIN_V(mv);
      if (is_wo) {
        SPIN(SPIN_MAX) {
          if (wave_none((or4(mv) & 255u) != 0u)) break;
          MEM_FENCE();
          mv = ld_sc1(rws, (lane & 31) * 16, mso) ^ tb;
        }
        const float m = asf((idx & 8) ? mv[2] : mv[0]);
        float mx = fmaxf(m, dpp<XOR7>(m));
        mx = fmaxf(mx, dpp<XOR2>(mx));
        mx = fmaxf(mx, dpp<XOR1>(mx));
        const float e = __builtin_amdgcn_exp2f((m - mx) * LOG2E);
        float lt = e * asf((idx & 8) ? mv[3] : mv[1]);
        lt += dpp<XOR7>(lt);
        lt += dpp<XOR2>(lt);
        lt += dpp<XOR1>(lt);
        *(volatile LAS unsigned*)(lds + L_F + (lane & 31) * 4) = asu(e * __builtin_amdgcn_rcpf(lt)) & 0xfffffffeu;
      } else if (bid == 56) {
        SPIN(SPIN_MAX) {
          unsigned bad = 0;
#pragma unroll
          for (int u = 0; u < 4; ++u) bad |= or4(ld_sc1(rws, l16 + u * 1024, WS_ML) ^ tb);
          if (wave_none((bad & 255u) != 0u)) break;
          MEM_FENCE();
        }
        if (lane == 0) __builtin_amdgcn_raw_buffer_store_b32(tag, rws, 0, WS_EPOCH, AUX_SC1);
      }
    }
    if (is_wo) {
      float f;
      LSPIN() {
        f = *(volatile LAS float*)fa;
        if (wave_none((asu(f) == 0xffffffffu) | (*(volatile LAS unsigned*)(lds + F_T) == 0u))) break;
      }
      u32x4 o0, o1;
      MEM_FENCE();
      SPIN(SPIN_MAX) {
        o0 = ld_sc1(rws, l16, oso) ^ tb;
        o1 = ld_sc1(rws, l16 + 1024, oso) ^ tb;
        if (wave_none(((or4(o0) | or4(o1)) & 255u) != 0u)) break;
        MEM_FENCE();
      }
      const float o = fold_pair(asf4(o0) * f, asf4(o1) * f);
      lds_st<u16>(ad, 0, (u16)pack_bf16(o, o));
      WG_BARRIER();
    }
  }

  if (is_red) {
    const u32x4 tb = tag_bytes(tag);
    LAS char* fw = lds + L_FIN + ((lane >> 4) * 8 + (lane & 7)) * 64 + wave * 4 + (lane & 8) * 4;
    LAS char* fr = lds + L_FIN + (lane & 31) * 64 + (lane >> 5) * 32;
    unsigned oo = (lane & 32) ? 0x7fffffffu : oi * 2;
    const bool dn_lane = wave * 64 + lane < 28 * 8;
    unsigned dvo = dn_lane ? l16 + 8192 : 0x7fffffffu;
    u32x4 td = dn_lane ? tb : u32x4{0u, 0u, 0u, 0u};
    float xf = bf_lo(xv);
    PIN_V(fw); PIN_V(fr); PIN_V(oo); PIN_V(xf); PIN_V(dvo); PIN_V(td);
    const rsrc_t ro = make_rsrc(p_out, NB * DM * 2);
    const unsigned so = WS_RP + bid * RP_BOX + wave * 1024;
    u32x4 a0, a1;
    SPIN(SPIN_MAX) {
      a1 = ld_sc1(rws, dvo, so) ^ td;
      a0 = ld_sc1(rws, l16, so) ^ tb;
      if (wave_none(((or4(a0) | or4(a1)) & 255u) != 0u)) break;
      MEM_FENCE();
    }
    lds_st<float>(fw, 0, fold_pair(asf4(a0), asf4(a1)));
    WG_BARRIER();
    if (wave == 2) {
      const f32x4 t = lds_ld<f32x4>(fr, 0) + lds_ld<f32x4>(fr, 16);
      const f32x2 t2 = f32x2{t[0], t[1]} + f32x2{t[2], t[3]};
      const float u = round_bf16(t2[0] + t2[1]);
      auto sw = __builtin_amdgcn_permlane32_swap(asu(u), asu(u), false, false);
      const float sv = asf(sw[0]), dn = asf(sw[1]);
      const float g = round_bf16(dn * __builtin_amdgcn_rcpf(1.0f + __builtin_amdgcn_exp2f(-dn * LOG2E)));
      const float res = xf + round_bf16(sv * g);
      __builtin_amdgcn_raw_buffer_store_b16((u16)pack_bf16(res, res), ro, oo, 0, 0);
    }
  }
}

}

extern "C" void kernel_launch(void* const* d_in, const int* in_sizes, int n_in, void* d_out, int out_size, void* d_ws,
                              size_t ws_size, hipStream_t stream) {
  fused_decode<<<256, 512, 0, stream>>>((const u16*)d_in[0], (const u16*)d_in[1], (const u16*)d_in[2], (const u16*)d_in[3],
                                        (const u16*)d_in[4], (const u16*)d_in[6], (const u16*)d_in[7], (const u16*)d_in[8],
                                        (u16*)d_out, (char*)d_ws);
}
```

```cpp
#define PROBE 0
#define PROBE_W 0
#include <hip/hip_runtime.h>
#include <hip/hip_bf16.h>
#include <stdint.h>

#define DIAG 0
#define CP 10750
#define MLAT 7
#define RD 0
#define FPW 2
#define P0 50
#define DT 56
#define PL 60
#define PLD 70
#define STG 14
#define EAUX 16
#define RSL 0
#define OSL 0
#define WS_OFF 0
#define KV_AUX 2
#define W_AUX 2

namespace {

typedef unsigned short u16;
typedef unsigned long long u64;
typedef unsigned u32x2 __attribute__((ext_vector_type(2)));
typedef unsigned u32x4 __attribute__((ext_vector_type(4)));
typedef unsigned u32x16 __attribute__((ext_vector_type(16)));
typedef float f32x2 __attribute__((ext_vector_type(2)));
typedef float f32x4 __attribute__((ext_vector_type(4)));
typedef short s16x4 __attribute__((ext_vector_type(4)));
typedef __bf16 bf16x2 __attribute__((ext_vector_type(2)));
typedef __bf16 bf16x8 __attribute__((ext_vector_type(8)));
#define LAS __attribute__((address_space(3)))
typedef __amdgpu_buffer_rsrc_t rsrc_t;

constexpr int NB = 4, DM = 448, SK = 8192, WIN = 2048, FF = 1792, QW = 4096;
constexpr int AUX_SC1 = 16;
constexpr unsigned SPIN_MAX = 1u << 22;
constexpr float LOG2E = 1.4426950408889634f;

constexpr unsigned WS_EPOCH = 0;
constexpr unsigned WS_QP = 4096;
constexpr unsigned WS_GP = WS_QP + 8 * 4 * 4096;
constexpr unsigned WS_OP = WS_GP + 28 * 4096;
constexpr unsigned WS_ML = WS_OP + 64 * 8192;
constexpr unsigned WS_RP = WS_ML + 4096;
constexpr unsigned RP_BOX = 92 * 128;
constexpr unsigned WS_END = WS_RP + 56 * RP_BOX;
constexpr unsigned WS_DIAG = 8u << 20;

constexpr int L_V = 0;
constexpr int L_TILE = L_V;
constexpr int L_XN = L_V + 32768;
constexpr int L_FIN = L_V;
constexpr int L_Q = 131072;
constexpr int L_P = L_Q + 1024;
constexpr int L_A = L_P + 1024;
constexpr int L_SYNC = L_A + 512;
constexpr int L_ZERO = L_SYNC;
constexpr int F_XN = L_SYNC + 16, F_EP = L_SYNC + 32, F_Q = L_SYNC + 48, L_PROG = L_SYNC + 64, F_PV = L_SYNC + 80, F_T = L_SYNC + 96;
constexpr int L_F = L_SYNC + 128;
constexpr int L_MX = L_SYNC + 384;
constexpr int L_LS = L_MX + 32;
constexpr int L_END = L_SYNC + 512;

#define SCHED_FENCE() __builtin_amdgcn_sched_barrier(0)
#define MEM_FENCE() asm volatile("" ::: "memory")
#define WG_BARRIER() do { SCHED_FENCE(); asm volatile("s_waitcnt lgkmcnt(0)\n\ts_barrier" ::: "memory"); SCHED_FENCE(); } while (0)
#define WG_BARRIER_BARE() do { SCHED_FENCE(); asm volatile("s_barrier" ::: "memory"); SCHED_FENCE(); } while (0)
#define VM_WAIT(n) do { SCHED_FENCE(); asm volatile("s_waitcnt vmcnt(%0)" :: "i"(n) : "memory"); SCHED_FENCE(); } while (0)
#define VM_DRAIN() do { SCHED_FENCE(); __builtin_amdgcn_s_waitcnt(0x0F70); SCHED_FENCE(); MEM_FENCE(); } while (0)
#define NAP() __builtin_amdgcn_s_sleep(1)
#define PACE(t) do { SCHED_FENCE(); pace_until(t_go + (t)); SCHED_FENCE(); } while (0)
#define PIN_V(x) asm volatile("" : "+v"(x))
#define PIN_S(x) asm volatile("" : "+s"(x))
#define LSPIN() _Pragma("nounroll") for (;;)
#define SPIN(limit) _Pragma("nounroll") for (unsigned s_ = 0; s_ < (limit); ++s_)

__device__ __forceinline__ void pace_until(unsigned when) {
  asm volatile("1:\n\ts_memrealtime vcc\n\ts_waitcnt lgkmcnt(0)\n\ts_sub_i32 vcc_lo, vcc_lo, %0\n\ts_cmp_lt_i32 vcc_lo, 0\n\ts_cbranch_scc1 1b"
               :: "s"(when) : "vcc", "scc", "memory");
}
__device__ __forceinline__ float asf(unsigned u) { return __builtin_bit_cast(float, u); }
__device__ __forceinline__ unsigned asu(float f) { return __builtin_bit_cast(unsigned, f); }
__device__ __forceinline__ unsigned pack_bf16(float lo, float hi) {
  f32x2 f = {lo, hi};
  return __builtin_bit_cast(unsigned, __builtin_convertvector(f, bf16x2));
}
__device__ __forceinline__ float round_bf16(float v) { return asf(pack_bf16(0.f, v)); }
__device__ __forceinline__ float bf_lo(unsigned u) { return asf(u << 16); }
__device__ __forceinline__ float bf_hi(unsigned u) { return asf(u & 0xffff0000u); }
template <int CTRL> __device__ __forceinline__ float dpp(float x) {
  return asf((unsigned)__builtin_amdgcn_mov_dpp((int)asu(x), CTRL, 0xf, 0xf, true));
}
constexpr int XOR1 = 0xB1, XOR2 = 0x4E, XOR7 = 0x141, XOR8 = 0x128;
__device__ __forceinline__ float fold16(float a, float b) {
  auto s = __builtin_amdgcn_permlane16_swap(asu(a), asu(b), false, false);
  return asf(s[0]) + asf(s[1]);
}
__device__ __forceinline__ float fold32(float a, float b) {
  auto s = __builtin_amdgcn_permlane32_swap(asu(a), asu(b), false, false);
  return asf(s[0]) + asf(s[1]);
}
__device__ __forceinline__ float max16(float a, float b) {
  auto s = __builtin_amdgcn_permlane16_swap(asu(a), asu(b), false, false);
  return fmaxf(asf(s[0]), asf(s[1]));
}
__device__ __forceinline__ float max32(float a, float b) {
  auto s = __builtin_amdgcn_permlane32_swap(asu(a), asu(b), false, false);
  return fmaxf(asf(s[0]), asf(s[1]));
}
__device__ __forceinline__ float row_sum(float v) {
  v += dpp<XOR8>(v); v += dpp<XOR7>(v); v += dpp<XOR2>(v); v += dpp<XOR1>(v);
  return v;
}
__device__ __forceinline__ float wave_sum(float v) { v = fold32(v, v); return row_sum(fold16(v, v)); }
__device__ __forceinline__ float row_max(float v) {
  v = fmaxf(v, dpp<XOR8>(v)); v = fmaxf(v, dpp<XOR7>(v)); v = fmaxf(v, dpp<XOR2>(v)); v = fmaxf(v, dpp<XOR1>(v));
  return v;
}
__device__ __forceinline__ float fold_pair(f32x4 a, f32x4 b) {
  const float a0 = fold32(a[0], a[2]), a1 = fold32(a[1], a[3]), b0 = fold32(b[0], b[2]), b1 = fold32(b[1], b[3]);
  SCHED_FENCE();
  const float za = fold16(a0, a1), zb = fold16(b0, b1);
  const bool hi = __builtin_amdgcn_inverse_ballot_w64(0xff00ff00ff00ff00ull);
  return (hi ? zb : za) + dpp<XOR8>(hi ? za : zb);
}
__device__ __forceinline__ rsrc_t make_rsrc(u64 p, unsigned bytes) {
  return __builtin_amdgcn_make_buffer_rsrc((void*)p, 0, bytes, 0x00020000);
}
__device__ __forceinline__ u32x4 tag4(f32x4 v, unsigned tag) {
  u32x4 r;
  r[0] = __builtin_amdgcn_perm(asu(v[0]), tag, 0x07060500u);
  r[1] = __builtin_amdgcn_perm(asu(v[1]), tag, 0x07060501u);
  r[2] = __builtin_amdgcn_perm(asu(v[2]), tag, 0x07060502u);
  r[3] = __builtin_amdgcn_perm(asu(v[3]), tag, 0x07060503u);
  return r;
}
__device__ __forceinline__ unsigned next_tag(unsigned epoch) {
  const unsigned e = __builtin_amdgcn_readfirstlane(epoch);
  return ((e & 0x7f7f7f7fu) + 0x07050301u) ^ (e & 0x80808080u);
}
__device__ __forceinline__ u32x4 tag_bytes(unsigned tag) { return u32x4{tag & 255u, (tag >> 8) & 255u, (tag >> 16) & 255u, tag >> 24}; }
__device__ __forceinline__ u32x4 tag_nibbles(unsigned tag) { return u32x4{tag & 15u, (tag >> 8) & 15u, (tag >> 16) & 15u, (tag >> 24) & 15u}; }
__device__ __forceinline__ u32x4 tag4_fine(f32x4 v, unsigned tag) {
  return ((__builtin_bit_cast(u32x4, v) + 8u) & 0xfffffff0u) | tag_nibbles(tag);
}
__device__ __forceinline__ unsigned or4(u32x4 v) { return v[0] | v[1] | v[2] | v[3]; }
__device__ __forceinline__ f32x4 asf4(u32x4 v) { return __builtin_bit_cast(f32x4, v); }
__device__ __forceinline__ u32x4 ld_sc1(rsrc_t r, unsigned voff, unsigned soff) {
  return __builtin_amdgcn_raw_buffer_load_b128(r, voff, soff, AUX_SC1);
}
__device__ __forceinline__ void st_sc1(u32x4 v, rsrc_t r, unsigned voff, unsigned soff) {
  __builtin_amdgcn_raw_buffer_store_b128(v, r, voff, soff, AUX_SC1);
}
__device__ __forceinline__ bool wave_none(bool bad) { return __builtin_amdgcn_ballot_w64(bad) == 0ull; }

template <typename T> __device__ __forceinline__ T lds_ld(LAS char* l, int off) { return *(LAS T*)(l + off); }
template <typename T> __device__ __forceinline__ void lds_st(LAS char* l, int off, T v) { *(LAS T*)(l + off) = v; }
__device__ __forceinline__ void lds_flag(LAS char* l, int off, unsigned v) {
  MEM_FENCE();
  *(volatile LAS unsigned*)(l + off) = v;
  MEM_FENCE();
}
__device__ __forceinline__ u32x4 lds_peek(LAS char* l, int off) { return *(volatile LAS u32x4*)(l + off); }
__device__ __forceinline__ void lds_wait_all(LAS char* l, int off) {
  LSPIN() { const u32x4 f = lds_peek(l, off); if (wave_none((f[0] & f[1] & f[2] & f[3]) == 0u)) break; NAP(); }
  MEM_FENCE();
}
__device__ __forceinline__ bool unset4(u32x4 v) { return max(max(v[0], v[1]), max(v[2], v[3])) == 0xffffffffu; }

__device__ __forceinline__ void tile_fetch(rsrc_t r, unsigned voff, unsigned soff, unsigned step, LAS char* img) {
  __builtin_amdgcn_raw_ptr_buffer_load_lds(r, (LAS void*)img, 16, voff, soff, 0, W_AUX);
  __builtin_amdgcn_raw_ptr_buffer_load_lds(r, (LAS void*)img, 16, voff, soff + step, 1024, W_AUX);
  __builtin_amdgcn_raw_ptr_buffer_load_lds(r, (LAS void*)img, 16, voff, soff + 2 * step, 2048, W_AUX);
  __builtin_amdgcn_raw_ptr_buffer_load_lds(r, (LAS void*)img, 16, voff, soff + 3 * step, 3072, W_AUX);
}
template <int GAP> __device__ __forceinline__ bf16x8 tr_frag(LAS char* a) {
  const s16x4 lo = __builtin_amdgcn_ds_read_tr16_b64_v4i16((LAS s16x4*)a);
  const s16x4 hi = __builtin_amdgcn_ds_read_tr16_b64_v4i16((LAS s16x4*)(a + GAP));
  return __builtin_bit_cast(bf16x8, __builtin_shufflevector(lo, hi, 0, 1, 2, 3, 4, 5, 6, 7));
}
__device__ __forceinline__ void tile_frags(LAS char* img, int lane, bf16x8 (&w)[4]) {
  const int row = lane >> 2, p = lane & 3;
#pragma unroll
  for (int t = 0; t < 4; ++t) w[t] = tr_frag<2048>(img + row * 128 + (((2 * t + (p >> 1)) ^ (row & 6)) << 4) + 8 * (p & 1));
}
__device__ __forceinline__ f32x4 mma(bf16x8 a, bf16x8 b, f32x4 c) { return __builtin_amdgcn_mfma_f32_16x16x32_bf16(a, b, c, 0, 0, 0); }
__device__ __forceinline__ bf16x8 asb(u32x4 v) { return __builtin_bit_cast(bf16x8, v); }

#define STAMP(k) do { if ((k) == PROBE) { SCHED_FENCE(); pst = __builtin_amdgcn_s_memrealtime(); SCHED_FENCE(); } } while (0)
#define STAMP_OUT() do { if (wave == PROBE_W && pst) while (__builtin_amdgcn_s_memrealtime() < pst + 3000) __builtin_amdgcn_s_sleep(2); } while (0)

__global__ void __launch_bounds__(512, 2)
fused_decode(const u16* x, const u16* kc, const u16* vc, const u16* wq, const u16* wo, const u16* wg, const u16* wu,
             const u16* wd, u16* out, char* ws) {
  __shared__ __attribute__((aligned(16))) char lds_raw[L_END];
  LAS char* lds = (LAS char*)lds_raw;
  u64 pst = 0;
  STAMP(0);
  const int bid = blockIdx.x, lane = threadIdx.x & 63, wave = __builtin_amdgcn_readfirstlane(threadIdx.x >> 6);
  const int hk = bid & 7, idx = bid >> 3, w4 = wave & 3;
  const bool loader = wave >= 4;
  const u64 p_x = (u64)x, p_k = (u64)kc, p_v = (u64)vc, p_wq = (u64)wq, p_wo = (u64)wo, p_wg = (u64)wg, p_wu = (u64)wu, p_wd = (u64)wd;
  const u64 p_out = (u64)out, p_ws = (u64)ws;
#define ARGS_IN "s"(p_x), "s"(p_k), "s"(p_v), "s"(p_wq), "s"(p_wo), "s"(p_wg), "s"(p_wu), "s"(p_wd), "s"(p_out), "s"(p_ws)
#define KV_OFF ((((idx >> 3) * 8 + hk) * SK + (SK - WIN) + (idx & 7) * 256) * 512 + w4 * 32768)
#define SEQ ((unsigned)(0x12075643120ull >> ((idx & 3) * 4)))
#define T_OFF (P0 + (3 - (idx >> 3)) * STG)
#define is_wo (idx >= 16)
#define late (idx >= 9)
#define is_red (idx < 7)
#define wo_rg (((hk + 1 + ((idx >> 1) & 3)) & 7) * 8 + ((idx >> 1) & 7))
#define dn_un (bid - 72)
#define late_rg (is_wo ? wo_rg : dn_un >> 1)
#define late_ch ((is_wo ? idx : dn_un) & 1)
#define oi ((bid & 3) * DM + (bid >> 2) * 32 + (lane & 31))
#define rws make_rsrc(p_ws, WS_END)
  const unsigned l16 = lane * 16;
  unsigned tag = 0, xv = 0, cpf = 0, t_k = 0;

  if (!loader) {
    const unsigned iv = (lane & 16) ? 0xffffffffu : 0u;
    lds_st<u32x2>(lds, L_SYNC + lane * 8, u32x2{iv, iv});
    if (CP) cpf = __builtin_amdgcn_raw_buffer_load_b32(make_rsrc(__builtin_amdgcn_s_getpc() & ~127ull, CP), 0, (idx * 4 + w4) * 128, 0);
    asm volatile("" :: ARGS_IN);
    WG_BARRIER();
    t_k = (unsigned)__builtin_amdgcn_s_memrealtime();
    PIN_S(t_k);
    STAMP(1);
  }
  MEM_FENCE();
  if (loader) {
    const bool is_q = idx < 16, is_gate = idx < 23, has_early = idx < 30;
    const int half = hk & 1;
    int et_q = idx + 16 * ((idx + (hk >> 1)) & 3), et_f = (idx - (is_gate ? 16 : 23)) * 4 + (hk >> 1);
    asm("" : "+s"(et_q), "+s"(et_f));
    const int etile = is_q ? et_q : et_f;
    const int r8 = lane >> 3;
    LAS char* img = lds + L_TILE + w4 * 4096;
    const unsigned stride = is_q ? QW * 2 : FF * 2;
    unsigned evo = r8 * stride + (((lane & 7) ^ (r8 & 6)) << 4), eso = (half * 224 + w4 * 32) * stride + etile * 128;
    unsigned ebytes = has_early ? DM * stride : 0, eso2 = eso + 128 * stride, estep = 8 * stride - 1024;
    PIN_V(evo);
    asm volatile("" : "+s"(eso), "+s"(estep), "+s"(ebytes), "+s"(eso2));
    SCHED_FENCE();
    asm volatile("" :: ARGS_IN : "memory");
    STAMP(20);
    {
      const rsrc_t rw = make_rsrc(is_q ? p_wq : (is_gate ? p_wg : p_wu), ebytes);
      tile_fetch(rw, evo, eso, estep, img);
      if (w4 < 3) tile_fetch(rw, evo, eso2, estep, img + 4 * 4096);
    }
    WG_BARRIER_BARE();
    const unsigned t_go = (unsigned)__builtin_amdgcn_s_memrealtime() + T_OFF;
    SCHED_FENCE();
    STAMP(22);
    const u32x4 xr = __builtin_amdgcn_raw_buffer_load_b128(make_rsrc(p_x, NB * DM * 2), l16, w4 * DM * 2, 0);
    const unsigned epoch = __builtin_amdgcn_raw_buffer_load_b32(rws, 0, WS_EPOCH, EAUX);
    SCHED_FENCE();
    STAMP(1);
    {
      if (PROBE == 21) VM_WAIT(1);
      STAMP(21);
      float v[8], t = 0.f;
#pragma unroll
      for (int k = 0; k < 4; ++k) { v[2 * k] = bf_lo(xr[k]); v[2 * k + 1] = bf_hi(xr[k]); t += v[2 * k] + v[2 * k + 1]; }
      const float mean = wave_sum(lane < 56 ? t : 0.f) * (1.0f / DM);
      t = 0.f;
#pragma unroll
      for (int k = 0; k < 8; ++k) { const float d = v[k] - mean; t = fmaf(d, d, t); }
      const float var = round_bf16(wave_sum(lane < 56 ? t : 0.f) * (1.0f / DM)), mb = round_bf16(mean);
      const float rc = __builtin_amdgcn_rcpf(round_bf16(__builtin_amdgcn_sqrtf(round_bf16(var + 1e-5f))));
      u32x4 xn;
#pragma unroll
      for (int k = 0; k < 4; ++k) {
        const unsigned d = pack_bf16(v[2 * k] - mb, v[2 * k + 1] - mb);
        xn[k] = pack_bf16(bf_lo(d) * rc, bf_hi(d) * rc);
      }
      lds_st<u32x4>(lds, L_XN + w4 * 1024 + lane * 16, xn);
      lds_flag(lds, F_XN + w4 * 4, 1u);
      lds_wait_all(lds, F_XN);
      tag = next_tag(epoch);
      PIN_S(tag);
      const int bt = lane & 3, g = lane >> 4, jj = (lane >> 2) & 3;
      const int xo = L_XN + bt * 1024 + (half * 224 + w4 * 32 + 4 * g) * 2;
      const u32x2 a0 = lds_ld<u32x2>(lds, xo), a1 = lds_ld<u32x2>(lds, xo + 32);
      const u32x2 a2 = lds_ld<u32x2>(lds, xo + 256), a3 = lds_ld<u32x2>(lds, xo + 288);
      const u32x4 act0 = {a0[0], a0[1], a1[0], a1[1]}, act1 = {a2[0], a2[1], a3[0], a3[1]}, zero = {0u, 0u, 0u, 0u};
      VM_DRAIN();
      bf16x8 wf0[4], wf1[4];
      tile_frags(img, lane, wf0);
      f32x4 acc = {0.f, 0.f, 0.f, 0.f};
      if (w4 < 3) {
        tile_frags(img + 4 * 4096, lane, wf1);
#pragma unroll
        for (int t = 0; t < 4; ++t) acc = mma(wf1[t], asb(jj == t ? act1 : zero), acc);
      }
#pragma unroll
      for (int t = 0; t < 4; ++t) acc = mma(wf0[t], asb(jj == t ? act0 : zero), acc);
      lds_st<f32x4>(img, bt * 256 + ((lane & 12) + g) * 16, acc);
      lds_flag(lds, F_EP + w4 * 4, 1u);
      if (w4 == 3 && has_early) {
        const unsigned voff = is_q ? WS_QP + (etile >> 3) * 16384 + (lane >> 4) * 4096 + (etile & 6) * 512 + half * 512 + (etile & 1) * 256 + (lane & 15) * 16
                                   : WS_GP + etile * 4096 + (is_gate ? 0 : 2048) + half * 1024 + l16;
        lds_wait_all(lds, F_EP);
        f32x4 sum = lds_ld<f32x4>(lds, L_TILE + lane * 16);
#pragma unroll
        for (int p = 1; p < 4; ++p) sum += lds_ld<f32x4>(lds, L_TILE + p * 4096 + lane * 16);
        st_sc1(tag4_fine(sum, tag), rws, voff, 0);
      }
    }
    STAMP(2);
    const rsrc_t rv = make_rsrc(p_v + KV_OFF, 32768);
    const unsigned hf = lane >> 5;
    unsigned vo0 = (lane ^ hf) * 16, vo1 = (lane ^ (2 + hf)) * 16, vo2 = (lane ^ (4 + hf)) * 16, vo3 = (lane ^ (6 + hf)) * 16;
    const int lcol = late_ch * 4 + w4;
    const bool has_tile = late && lcol < 7;
    const rsrc_t rl = make_rsrc(is_wo ? p_wo : p_wd, (is_wo ? QW : FF) * DM * 2);
    unsigned lvo = r8 * (DM * 2) + (((lane & 7) ^ (r8 >> 1)) << 4), lso = late_rg * (64 * DM * 2) + lcol * 128;
    unsigned rvo = (((lane >> 3) & 1) * 4 + (lane >> 4)) * RP_BOX + (lane & 7) * 16;
    unsigned rso = WS_RP + lcol * 8 * RP_BOX + ((is_wo ? 0 : 64) + late_rg) * 128;
    LAS char *aa[4], *ta[4];
#pragma unroll
    for (int t = 0; t < 4; ++t) {
      aa[t] = lds + ((lane & 3) == t ? L_A + ((lane >> 2) & 3) * 64 + (lane >> 4) * 16 : L_ZERO);
      ta[t] = img + (lane >> 2) * 128 + (((lane & 2) * 2 + (t ^ ((lane >> 3) & 3))) << 4) + 8 * (lane & 1);
      PIN_V(aa[t]); PIN_V(ta[t]);
    }
    PIN_V(vo0); PIN_V(vo1); PIN_V(vo2); PIN_V(vo3); PIN_V(lvo); PIN_V(rvo); PIN_S(lso); PIN_S(rso);
    WG_BARRIER();
    STAMP(3);
    _Pragma("nounroll") for (int g = 0; g < 8; ++g) {
      const unsigned so = ((SEQ >> (4 * g)) & 7) << 12;
      PACE((8 + g) * DT);
      LAS void* dst = (LAS void*)(lds + L_V + (g * 4 + w4) * 4096);
      __builtin_amdgcn_raw_ptr_buffer_load_lds(rv, dst, 16, vo0, so, 0, KV_AUX);
      __builtin_amdgcn_raw_ptr_buffer_load_lds(rv, dst, 16, vo1, so, 1024, KV_AUX);
      __builtin_amdgcn_raw_ptr_buffer_load_lds(rv, dst, 16, vo2, so, 2048, KV_AUX);
      __builtin_amdgcn_raw_ptr_buffer_load_lds(rv, dst, 16, vo3, so, 3072, KV_AUX);
      VM_WAIT(8);
      lds_flag(lds, L_PROG + w4 * 4, g + 1);
    }
    STAMP(4);
    VM_WAIT(4);
    lds_flag(lds, L_PROG + w4 * 4, 9);
    const bool before = has_tile && is_wo;
    if (!before) { VM_WAIT(0); STAMP(5); WG_BARRIER_BARE(); }
    if (has_tile) {
      LSPIN() { const u32x4 f = lds_peek(lds, F_PV); if (wave_none((f[0] & f[1]) == 0u)) break; }
      PACE(15 * DT + (is_wo ? PL : PLD));
      tile_fetch(rl, lvo, lso, 8 * DM * 2 - 1024, img);
      tile_fetch(rl, lvo, lso + 32 * DM * 2, 8 * DM * 2 - 1024, img + 4 * 4096);
      if (before) {
        VM_WAIT(8);
        STAMP(5);
        WG_BARRIER_BARE();
        VM_WAIT(FPW);
        lds_flag(lds, F_T + w4 * 4, 1u);
      }
      VM_DRAIN();
      bf16x8 wf[8];
#pragma unroll
      for (int t = 0; t < 4; ++t) { wf[t] = tr_frag<2048>(ta[t]); wf[4 + t] = tr_frag<2048>(ta[t] + 4 * 4096); }
      STAMP(8);
      if (is_wo) WG_BARRIER();
      STAMP(10);
      u32x4 act[8];
#pragma unroll
      for (int t = 0; t < 4; ++t) { act[2 * t] = lds_ld<u32x4>(aa[t], 0); act[2 * t + 1] = lds_ld<u32x4>(aa[t], 256); }
      SCHED_FENCE();
      f32x4 acc = {0.f, 0.f, 0.f, 0.f};
#pragma unroll
      for (int t = 0; t < 4; ++t) { acc = mma(asb(act[2 * t]), wf[t], acc); acc = mma(asb(act[2 * t + 1]), wf[4 + t], acc); }
      st_sc1(tag4(acc, tag), rws, rvo, rso);
      STAMP(11);
    } else if (is_wo) {
      WG_BARRIER_BARE();
    }
  }
  int role = wave;
  PIN_S(role);
  if (role < 4) {
    u32x4 kr[32], qv;
#define qso (WS_QP + hk * 16384 + (idx >> 3) * 4096 + w4 * 1024)
    const unsigned kvo = (lane & 7) * 512 + ((lane >> 3) & 1) * 64 + (lane >> 4) * 16, seq = SEQ;
    const rsrc_t rk = make_rsrc(p_k + KV_OFF, 32768);
    const unsigned t_go = t_k + T_OFF;
#pragma unroll
    for (int g = 0; g < 8; ++g) {
      const unsigned so = ((seq >> (4 * g)) & 7) << 12;
      PACE(g * DT);
#pragma unroll
      for (int i = 0; i < 4; ++i) kr[g * 4 + i] = __builtin_amdgcn_raw_buffer_load_b128(rk, kvo + i * 128, so, KV_AUX);
    }
    SCHED_FENCE();
    const unsigned kepoch = __builtin_amdgcn_raw_buffer_load_b32(rws, 0, WS_EPOCH, EAUX);
    qv = ld_sc1(rws, l16, qso);
    STAMP(2);
    WG_BARRIER_BARE();
    STAMP(3);
    tag = next_tag(kepoch);
    asm volatile("" : "+s"(tag) : "v"(cpf));
    SPIN(SPIN_MAX) {
      qv ^= tag_nibbles(tag);
      if (wave_none((or4(qv) & 15u) != 0u)) break;
      MEM_FENCE();
      qv = ld_sc1(rws, l16, qso);
    }
    lds_st<unsigned>(lds, L_Q + w4 * 256 + (lane & 31) * 8 + (lane >> 5) * 4,
                     pack_bf16(fold32(asf(qv[0]), asf(qv[2])), fold32(asf(qv[1]), asf(qv[3]))));
    lds_flag(lds, F_Q + w4 * 4, 1u);
    lds_wait_all(lds, F_Q);
    STAMP(4);
    float m0, m1;
    {
      const int qa = L_Q + (lane & 1) * 512 + ((lane >> 1) & 1) * 64 + (lane >> 4) * 16;
      f32x4 c0, c1;
      {
        u32x4 qf[4];
#pragma unroll
        for (int s = 0; s < 4; ++s) qf[s] = lds_ld<u32x4>(lds, qa + s * 128);
        f32x4 c[8];
#pragma unroll
        for (int t = 0; t < 8; ++t) c[t] = mma(asb(qf[0]), asb(kr[t * 4]), f32x4{0.f, 0.f, 0.f, 0.f});
#pragma unroll
        for (int s = 1; s < 4; ++s)
#pragma unroll
          for (int t = 0; t < 8; ++t) c[t] = mma(asb(qf[s]), asb(kr[t * 4 + s]), c[t]);
        const bool g1 = lane & 16, g2 = lane & 32;
        c0 = g2 ? (g1 ? c[3] : c[2]) : (g1 ? c[1] : c[0]);
        c1 = g2 ? (g1 ? c[7] : c[6]) : (g1 ? c[5] : c[4]);
      }
      const bool cb = lane & 8;
      const float r0 = (cb ? c1[2] : c0[0]) + dpp<XOR8>(cb ? c0[2] : c1[0]);
      const float r1 = (cb ? c1[3] : c0[1]) + dpp<XOR8>(cb ? c0[3] : c1[1]);
      const unsigned sp = pack_bf16(r0, r1);
      const float s0 = bf_lo(sp) * 0.0625f, s1 = bf_hi(sp) * 0.0625f;
      float mw = max32(s0, s1);
      mw = row_max(max16(mw, mw));
      *(volatile LAS unsigned*)(lds + L_MX + (lane >> 5) * 16 + w4 * 4) = asu(mw) & 0xfffffffeu;
      u32x4 x0, x1;
      LSPIN() { x0 = lds_peek(lds, L_MX); x1 = lds_peek(lds, L_MX + 16); if (wave_none(unset4(x0) | unset4(x1))) break; }
      m0 = fmaxf(fmaxf(asf(x0[0]), asf(x0[1])), fmaxf(asf(x0[2]), asf(x0[3])));
      m1 = fmaxf(fmaxf(asf(x1[0]), asf(x1[1])), fmaxf(asf(x1[2]), asf(x1[3])));
      const float p0 = __builtin_amdgcn_exp2f((s0 - m0) * LOG2E), p1 = __builtin_amdgcn_exp2f((s1 - m1) * LOG2E);
      const unsigned pk = pack_bf16(p0, p1);
      const int po = L_P + (((lane >> 3) & 1) * 4 + (lane >> 4)) * 64 + ((w4 & 1) * 2 + ((lane >> 2) & 1)) * 16 + ((w4 >> 1) * 4 + (lane & 3)) * 2;
      lds_st<u16>(lds, po, (u16)pk);
      lds_st<u16>(lds, po + 512, (u16)(pk >> 16));
      float l = fold32(p0, p1);
      l = row_sum(fold16(l, l));
      MEM_FENCE();
      *(volatile LAS unsigned*)(lds + L_LS + (lane >> 5) * 16 + w4 * 4) = asu(l) & 0xfffffffeu;
    }
    u32x4 ls;
    LSPIN() { ls = lds_peek(lds, L_LS + (lane >> 5) * 16); if (wave_none(unset4(ls))) break; }
    MEM_FENCE();
    STAMP(5);
    u32x4 tb = tag_bytes(tag);
    unsigned oso = WS_OP + wo_rg * 8192 + w4 * 2048;
    LAS char* ad = lds + L_A + (lane & 4) * 64 + w4 * 64 + ((lane & 1) * 2 + ((lane >> 3) & 1)) * 16 + ((lane & 2) * 2 + (lane >> 4)) * 2;
    LAS char* fa = lds + L_F + w4 * 32 + (lane >> 3) * 4;
    PIN_S(tb); PIN_S(oso); PIN_V(ad); PIN_V(fa);
    if (w4 < 2) {
      const int g = lane >> 4, row = lane >> 2, p = lane & 3, tl = lane & 7;
      LAS char* va[8];
#pragma unroll
      for (int t = 0; t < 8; ++t) va[t] = lds + L_V + row * 512 + ((w4 * 16 + (p >> 1) * 8 + (t ^ (row & 7))) << 4) + 8 * (p & 1);
      LAS char* pa = lds + L_P + ((lane >> 3) & 1) * 512 + g * 16;
      unsigned ovo = (((lane >> 3) & 1) * 4 + (g >> 1)) * 8192 + (g & 1) * 1024 + tl * 16;
      unsigned pso = WS_OP + (hk * 8 + w4 * 2) * 8192 + (idx >> 3) * 2048 + (idx & 7) * 128;
      PIN_V(ovo); PIN_S(pso);
      f32x4 o = {0.f, 0.f, 0.f, 0.f};
      u32x4 pm[8];
      {
        const u32x4 pl = lds_ld<u32x4>(pa, 0);
#pragma unroll
        for (int t = 0; t < 8; ++t) pm[t] = tl == t ? pl : u32x4{0u, 0u, 0u, 0u};
      }
      _Pragma("nounroll") for (int ks = 0; ks < 7; ++ks) {
        LSPIN() {
          const u32x4 pr = lds_peek(lds, L_PROG);
          if (wave_none(min(min(pr[0], pr[1]), min(pr[2], pr[3])) <= (unsigned)(ks + 2))) break;
        }
        lds_flag(lds, F_PV + w4 * 4, ks >= 2 ? 1u : 0u);
        pa += 64;
        const u32x4 pl = lds_ld<u32x4>(pa, 0);
        bf16x8 vf[8];
#pragma unroll
        for (int t = 0; t < 8; ++t) vf[t] = tr_frag<8192>(va[t]);
        SCHED_FENCE();
#pragma unroll
        for (int t = 0; t < 8; ++t) {
          o = mma(vf[t], asb(pm[t]), o);
          SCHED_FENCE();
          pm[t] = tl == t ? pl : u32x4{0u, 0u, 0u, 0u};
          va[t] += 16384;
          PIN_V(pm[t]); PIN_V(va[t]);
          SCHED_FENCE();
        }
      }
      STAMP(6);
      WG_BARRIER_BARE();
      {
        bf16x8 vf[8];
#pragma unroll
        for (int t = 0; t < 8; ++t) vf[t] = tr_frag<8192>(va[t]);
        SCHED_FENCE();
#pragma unroll
        for (int t = 0; t < 8; ++t) o = mma(vf[t], asb(pm[t]), o);
      }
      st_sc1(tag4(o, tag), rws, ovo, pso);
      STAMP(7);
    } else if (w4 == 2) {
      if (late && !is_wo) {
        u32x4 gv[4];
        SPIN(SPIN_MAX) {
          unsigned bad = 0;
#pragma unroll
          for (int u = 0; u < 4; ++u) gv[u] = ld_sc1(rws, l16 + u * 1024, WS_GP + (dn_un >> 1) * 4096);
#pragma unroll
          for (int u = 0; u < 4; ++u) { gv[u] ^= tag_nibbles(tag); bad |= or4(gv[u]); }
          if (wave_none((bad & 15u) != 0u)) break;
          MEM_FENCE();
        }
        const f32x4 gs = asf4(gv[0]) + asf4(gv[1]), us = asf4(gv[2]) + asf4(gv[3]);
        u32x2 hv;
#pragma unroll
        for (int e = 0; e < 2; ++e) {
          const unsigned gt = pack_bf16(gs[2 * e], gs[2 * e + 1]), up = pack_bf16(us[2 * e], us[2 * e + 1]);
          const float g0 = bf_lo(gt), g1 = bf_hi(gt);
          hv[e] = pack_bf16(g0 * (g0 > 0.f ? 1.f : 0.f) * bf_lo(up), g1 * (g1 > 0.f ? 1.f : 0.f) * bf_hi(up));
        }
        lds_st<u32x2>(lds, L_A + (lane & 8) * 32 + (lane >> 4) * 64 + (lane & 3) * 16 + (lane & 4) * 2, hv);
      } else if (is_red) {
        xv = __builtin_amdgcn_raw_buffer_load_b16(make_rsrc(p_x, NB * DM * 2), oi * 2, 0, 0);
        PIN_V(xv);
      }
      WG_BARRIER();
    } else {
      const float lsum = (asf(ls[0]) + asf(ls[1])) + (asf(ls[2]) + asf(ls[3]));
      const float l1 = asf(__builtin_amdgcn_readlane(asu(lsum), 32));
      if (lane == 0) st_sc1(tag4(f32x4{m0, lsum, m1, l1}, tag), rws, 0, WS_ML + (hk * 32 + idx) * 16);
      const unsigned mso = is_wo ? WS_ML + (wo_rg >> 3) * 512 : WS_END;
      LSPIN() { if (wave_none(*(volatile LAS unsigned*)(lds + L_PROG) < (unsigned)MLAT)) break; __builtin_amdgcn_s_sleep(4); }
      u32x4 mv = ld_sc1(rws, (lane & 31) * 16, mso);
      WG_BARRIER_BARE();
      mv ^= tb;
      PIN_V(mv);
      if (is_wo) {
        SPIN(SPIN_MAX) {
          if (wave_none((or4(mv) & 255u) != 0u)) break;
          MEM_FENCE();
          mv = ld_sc1(rws, (lane & 31) * 16, mso) ^ tb;
        }
        const float m = asf((idx & 8) ? mv[2] : mv[0]);
        float mx = fmaxf(m, dpp<XOR7>(m));
        mx = fmaxf(mx, dpp<XOR2>(mx));
        mx = fmaxf(mx, dpp<XOR1>(mx));
        const float e = __builtin_amdgcn_exp2f((m - mx) * LOG2E);
        float lt = e * asf((idx & 8) ? mv[3] : mv[1]);
        lt += dpp<XOR7>(lt);
        lt += dpp<XOR2>(lt);
        lt += dpp<XOR1>(lt);
        *(volatile LAS unsigned*)(lds + L_F + (lane & 31) * 4) = asu(e * __builtin_amdgcn_rcpf(lt)) & 0xfffffffeu;
      } else if (bid == 56) {
        SPIN(SPIN_MAX) {
          unsigned bad = 0;
#pragma unroll
          for (int u = 0; u < 4; ++u) bad |= or4(ld_sc1(rws, l16 + u * 1024, WS_ML) ^ tb);
          if (wave_none((bad & 255u) != 0u)) break;
          MEM_FENCE();
        }
        if (lane == 0) __builtin_amdgcn_raw_buffer_store_b32(tag, rws, 0, WS_EPOCH, AUX_SC1);
      }
    }
    if (is_wo) {
      float f;
      LSPIN() {
        f = *(volatile LAS float*)fa;
        if (wave_none((asu(f) == 0xffffffffu) | (*(volatile LAS unsigned*)(lds + F_T) == 0u))) break;
      }
      STAMP(8);
      u32x4 o0, o1;
      MEM_FENCE();
      SPIN(SPIN_MAX) {
        o0 = ld_sc1(rws, l16, oso) ^ tb;
        o1 = ld_sc1(rws, l16 + 1024, oso) ^ tb;
        if (wave_none(((or4(o0) | or4(o1)) & 255u) != 0u)) break;
        if (OSL) __builtin_amdgcn_s_sleep(OSL);
        MEM_FENCE();
      }
      STAMP(9);
      const float o = fold_pair(asf4(o0) * f, asf4(o1) * f);
      lds_st<u16>(ad, 0, (u16)pack_bf16(o, o));
      WG_BARRIER();
    }
  }

  if (is_red) {
    const u32x4 tb = tag_bytes(tag);
    LAS char* fw = lds + L_FIN + ((lane >> 4) * 8 + (lane & 7)) * 64 + wave * 4 + (lane & 8) * 4;
    LAS char* fr = lds + L_FIN + (lane & 31) * 64 + (lane >> 5) * 32;
    unsigned oo = (lane & 32) ? 0x7fffffffu : oi * 2;
    const bool dn_lane = wave * 64 + lane < 28 * 8;
    unsigned dvo = dn_lane ? l16 + 8192 : 0x7fffffffu;
    u32x4 td = dn_lane ? tb : u32x4{0u, 0u, 0u, 0u};
    float xf = bf_lo(xv);
    PIN_V(fw); PIN_V(fr); PIN_V(oo); PIN_V(xf); PIN_V(dvo); PIN_V(td);
    const rsrc_t ro = make_rsrc(p_out, NB * DM * 2);
    const unsigned so = WS_RP + bid * RP_BOX + wave * 1024;
    u32x4 a0, a1;
    if (RD) __builtin_amdgcn_s_sleep(RD);
    STAMP(8);
    SPIN(SPIN_MAX) {
      a1 = ld_sc1(rws, dvo, so) ^ td;
      a0 = ld_sc1(rws, l16, so) ^ tb;
      if (wave_none(((or4(a0) | or4(a1)) & 255u) != 0u)) break;
      if (RSL) __builtin_amdgcn_s_sleep(RSL);
      MEM_FENCE();
    }
    STAMP(9);
    lds_st<float>(fw, 0, fold_pair(asf4(a0), asf4(a1)));
    WG_BARRIER();
    STAMP(10);
    if (wave == 2) {
      const f32x4 t = lds_ld<f32x4>(fr, 0) + lds_ld<f32x4>(fr, 16);
      const f32x2 t2 = f32x2{t[0], t[1]} + f32x2{t[2], t[3]};
      const float u = round_bf16(t2[0] + t2[1]);
      auto sw = __builtin_amdgcn_permlane32_swap(asu(u), asu(u), false, false);
      const float sv = asf(sw[0]), dn = asf(sw[1]);
      const float g = round_bf16(dn * __builtin_amdgcn_rcpf(1.0f + __builtin_amdgcn_exp2f(-dn * LOG2E)));
      const float res = xf + round_bf16(sv * g);
      __builtin_amdgcn_raw_buffer_store_b16((u16)pack_bf16(res, res), ro, oo, 0, 0);
      STAMP(11);
    }
  }
  STAMP_OUT();
}

}

extern "C" void kernel_launch(void* const* d_in, const int* in_sizes, int n_in, void* d_out, int out_size, void* d_ws,
                              size_t ws_size, hipStream_t stream) {
  fused_decode<<<256, 512, 0, stream>>>((const u16*)d_in[0], (const u16*)d_in[1], (const u16*)d_in[2], (const u16*)d_in[3],
                                        (const u16*)d_in[4], (const u16*)d_in[6], (const u16*)d_in[7], (const u16*)d_in[8],
                                        (u16*)d_out, (char*)d_ws + WS_OFF);
}
```

```cpp
#define PROBE 0
#define PROBE_W 0
#include <hip/hip_runtime.h>
#include <hip/hip_bf16.h>
#include <stdint.h>

#define DIAG 0
#define CP 10750
#define MLAT 7
#define RD 0
#define FPW 2
#define P0 50
#define DT 56
#define PL 60
#define PLD 70
#define STG 14
#define EAUX 16
#define RSL 0
#define OSL 0
#define WS_OFF 0
#define KV_AUX 2
#define W_AUX 2

namespace {

typedef unsigned short u16;
typedef unsigned long long u64;
typedef unsigned u32x2 __attribute__((ext_vector_type(2)));
typedef unsigned u32x4 __attribute__((ext_vector_type(4)));
typedef unsigned u32x16 __attribute__((ext_vector_type(16)));
typedef float f32x2 __attribute__((ext_vector_type(2)));
typedef float f32x4 __attribute__((ext_vector_type(4)));
typedef short s16x4 __attribute__((ext_vector_type(4)));
typedef __bf16 bf16x2 __attribute__((ext_vector_type(2)));
typedef __bf16 bf16x8 __attribute__((ext_vector_type(8)));
#define LAS __attribute__((address_space(3)))
typedef __amdgpu_buffer_rsrc_t rsrc_t;

constexpr int NB = 4, DM = 448, SK = 8192, WIN = 2048, FF = 1792, QW = 4096;
constexpr int AUX_SC1 = 16;
constexpr unsigned SPIN_MAX = 1u << 22;
constexpr float LOG2E = 1.4426950408889634f;

constexpr unsigned WS_EPOCH = 0;
constexpr unsigned WS_QP = 4096;
constexpr unsigned WS_GP = WS_QP + 8 * 4 * 4096;
constexpr unsigned WS_OP = WS_GP + 28 * 4096;
constexpr unsigned WS_ML = WS_OP + 64 * 8192;
constexpr unsigned WS_RP = WS_ML + 4096;
constexpr unsigned RP_BOX = 92 * 128;
constexpr unsigned WS_END = WS_RP + 56 * RP_BOX;
constexpr unsigned WS_DIAG = 8u << 20;

constexpr int L_V = 0;
constexpr int L_TILE = L_V;
constexpr int L_XN = L_V + 32768;
constexpr int L_FIN = L_V;
constexpr int L_Q = 131072;
constexpr int L_P = L_Q + 1024;
constexpr int L_A = L_P + 1024;
constexpr int L_SYNC = L_A + 512;
constexpr int L_ZERO = L_SYNC;
constexpr int F_XN = L_SYNC + 16, F_EP = L_SYNC + 32, F_Q = L_SYNC + 48, L_PROG = L_SYNC + 64, F_PV = L_SYNC + 80, F_T = L_SYNC + 96;
constexpr int L_F = L_SYNC + 128;
constexpr int L_MX = L_SYNC + 384;
constexpr int L_LS = L_MX + 32;
constexpr int L_END = L_SYNC + 512;

#define SCHED_FENCE() __builtin_amdgcn_sched_barrier(0)
#define MEM_FENCE() asm volatile("" ::: "memory")
#define WG_BARRIER() do { SCHED_FENCE(); asm volatile("s_waitcnt lgkmcnt(0)\n\ts_barrier" ::: "memory"); SCHED_FENCE(); } while (0)
#define WG_BARRIER_BARE() do { SCHED_FENCE(); asm volatile("s_barrier" ::: "memory"); SCHED_FENCE(); } while (0)
#define VM_WAIT(n) do { SCHED_FENCE(); asm volatile("s_waitcnt vmcnt(%0)" :: "i"(n) : "memory"); SCHED_FENCE(); } while (0)
#define VM_DRAIN() do { SCHED_FENCE(); __builtin_amdgcn_s_waitcnt(0x0F70); SCHED_FENCE(); MEM_FENCE(); } while (0)
#define NAP() __builtin_amdgcn_s_sleep(1)
#define PACE(t) do { SCHED_FENCE(); pace_until(t_go + (t)); SCHED_FENCE(); } while (0)
#define PIN_V(x) asm volatile("" : "+v"(x))
#define PIN_S(x) asm volatile("" : "+s"(x))
#define LSPIN() _Pragma("nounroll") for (;;)
#define SPIN(limit) _Pragma("nounroll") for (unsigned s_ = 0; s_ < (limit); ++s_)

__device__ __forceinline__ void pace_until(unsigned when) {
  asm volatile("1:\n\ts_memrealtime vcc\n\ts_waitcnt lgkmcnt(0)\n\ts_sub_i32 vcc_lo, vcc_lo, %0\n\ts_cmp_lt_i32 vcc_lo, 0\n\ts_cbranch_scc1 1b"
               :: "s"(when) : "vcc", "scc", "memory");
}
__device__ __forceinline__ float asf(unsigned u) { return __builtin_bit_cast(float, u); }
__device__ __forceinline__ unsigned asu(float f) { return __builtin_bit_cast(unsigned, f); }
__device__ __forceinline__ unsigned pack_bf16(float lo, float hi) {
  f32x2 f = {lo, hi};
  return __builtin_bit_cast(unsigned, __builtin_convertvector(f, bf16x2));
}
__device__ __forceinline__ float round_bf16(float v) { return asf(pack_bf16(0.f, v)); }
__device__ __forceinline__ float bf_lo(unsigned u) { return asf(u << 16); }
__device__ __forceinline__ float bf_hi(unsigned u) { return asf(u & 0xffff0000u); }
template <int CTRL> __device__ __forceinline__ float dpp(float x) {
  return asf((unsigned)__builtin_amdgcn_mov_dpp((int)asu(x), CTRL, 0xf, 0xf, true));
}
constexpr int XOR1 = 0xB1, XOR2 = 0x4E, XOR7 = 0x141, XOR8 = 0x128;
__device__ __forceinline__ float fold16(float a, float b) {
  auto s = __builtin_amdgcn_permlane16_swap(asu(a), asu(b), false, false);
  return asf(s[0]) + asf(s[1]);
}
__device__ __forceinline__ float fold32(float a, float b) {
  auto s = __builtin_amdgcn_permlane32_swap(asu(a), asu(b), false, false);
  return asf(s[0]) + asf(s[1]);
}
__device__ __forceinline__ float max16(float a, float b) {
  auto s = __builtin_amdgcn_permlane16_swap(asu(a), asu(b), false, false);
  return fmaxf(asf(s[0]), asf(s[1]));
}
__device__ __forceinline__ float max32(float a, float b) {
  auto s = __builtin_amdgcn_permlane32_swap(asu(a), asu(b), false, false);
  return fmaxf(asf(s[0]), asf(s[1]));
}
__device__ __forceinline__ float row_sum(float v) {
  v += dpp<XOR8>(v); v += dpp<XOR7>(v); v += dpp<XOR2>(v); v += dpp<XOR1>(v);
  return v;
}
__device__ __forceinline__ float wave_sum(float v) { v = fold32(v, v); return row_sum(fold16(v, v)); }
__device__ __forceinline__ float row_max(float v) {
  v = fmaxf(v, dpp<XOR8>(v)); v = fmaxf(v, dpp<XOR7>(v)); v = fmaxf(v, dpp<XOR2>(v)); v = fmaxf(v, dpp<XOR1>(v));
  return v;
}
__device__ __forceinline__ float fold_pair(f32x4 a, f32x4 b) {
  const float a0 = fold32(a[0], a[2]), a1 = fold32(a[1], a[3]), b0 = fold32(b[0], b[2]), b1 = fold32(b[1], b[3]);
  SCHED_FENCE();
  const float za = fold16(a0, a1), zb = fold16(b0, b1);
  const bool hi = __builtin_amdgcn_inverse_ballot_w64(0xff00ff00ff00ff00ull);
  return (hi ? zb : za) + dpp<XOR8>(hi ? za : zb);
}
__device__ __forceinline__ rsrc_t make_rsrc(u64 p, unsigned bytes) {
  return __builtin_amdgcn_make_buffer_rsrc((void*)p, 0, bytes, 0x00020000);
}
__device__ __forceinline__ u32x4 tag4(f32x4 v, unsigned tag) {
  u32x4 r;
  r[0] = __builtin_amdgcn_perm(asu(v[0]), tag, 0x07060500u);
  r[1] = __builtin_amdgcn_perm(asu(v[1]), tag, 0x07060501u);
  r[2] = __builtin_amdgcn_perm(asu(v[2]), tag, 0x07060502u);
  r[3] = __builtin_amdgcn_perm(asu(v[3]), tag, 0x07060503u);
  return r;
}
__device__ __forceinline__ unsigned next_tag(unsigned epoch) {
  const unsigned e = __builtin_amdgcn_readfirstlane(epoch);
  return ((e & 0x7f7f7f7fu) + 0x07050301u) ^ (e & 0x80808080u);
}
__device__ __forceinline__ u32x4 tag_bytes(unsigned tag) { return u32x4{tag & 255u, (tag >> 8) & 255u, (tag >> 16) & 255u, tag >> 24}; }
__device__ __forceinline__ u32x4 tag_nibbles(unsigned tag) { return u32x4{tag & 15u, (tag >> 8) & 15u, (tag >> 16) & 15u, (tag >> 24) & 15u}; }
__device__ __forceinline__ u32x4 tag4_fine(f32x4 v, unsigned tag) {
  return ((__builtin_bit_cast(u32x4, v) + 8u) & 0xfffffff0u) | tag_nibbles(tag);
}
__device__ __forceinline__ unsigned or4(u32x4 v) { return v[0] | v[1] | v[2] | v[3]; }
__device__ __forceinline__ f32x4 asf4(u32x4 v) { return __builtin_bit_cast(f32x4, v); }
__device__ __forceinline__ u32x4 ld_sc1(rsrc_t r, unsigned voff, unsigned soff) {
  return __builtin_amdgcn_raw_buffer_load_b128(r, voff, soff, AUX_SC1);
}
__device__ __forceinline__ void st_sc1(u32x4 v, rsrc_t r, unsigned voff, unsigned soff) {
  __builtin_amdgcn_raw_buffer_store_b128(v, r, voff, soff, AUX_SC1);
}
__device__ __forceinline__ bool wave_none(bool bad) { return __builtin_amdgcn_ballot_w64(bad) == 0ull; }

template <typename T> __device__ __forceinline__ T lds_ld(LAS char* l, int off) { return *(LAS T*)(l + off); }
template <typename T> __device__ __forceinline__ void lds_st(LAS char* l, int off, T v) { *(LAS T*)(l + off) = v; }
__device__ __forceinline__ void lds_flag(LAS char* l, int off, unsigned v) {
  MEM_FENCE();
  *(volatile LAS unsigned*)(l + off) = v;
  MEM_FENCE();
}
__device__ __forceinline__ u32x4 lds_peek(LAS char* l, int off) { return *(volatile LAS u32x4*)(l + off); }
__device__ __forceinline__ void lds_wait_all(LAS char* l, int off) {
  LSPIN() { const u32x4 f = lds_peek(l, off); if (wave_none((f[0] & f[1] & f[2] & f[3]) == 0u)) break; NAP(); }
  MEM_FENCE();
}
__device__ __forceinline__ bool unset4(u32x4 v) { return max(max(v[0], v[1]), max(v[2], v[3])) == 0xffffffffu; }

__device__ __forceinline__ void tile_fetch(rsrc_t r, unsigned voff, unsigned soff, unsigned step, LAS char* img) {
  __builtin_amdgcn_raw_ptr_buffer_load_lds(r, (LAS void*)img, 16, voff, soff, 0, W_AUX);
  __builtin_amdgcn_raw_ptr_buffer_load_lds(r, (LAS void*)img, 16, voff, soff + step, 1024, W_AUX);
  __builtin_amdgcn_raw_ptr_buffer_load_lds(r, (LAS void*)img, 16, voff, soff + 2 * step, 2048, W_AUX);
  __builtin_amdgcn_raw_ptr_buffer_load_lds(r, (LAS void*)img, 16, voff, soff + 3 * step, 3072, W_AUX);
}
template <int GAP> __device__ __forceinline__ bf16x8 tr_frag(LAS char* a) {
  const s16x4 lo = __builtin_amdgcn_ds_read_tr16_b64_v4i16((LAS s16x4*)a);
  const s16x4 hi = __builtin_amdgcn_ds_read_tr16_b64_v4i16((LAS s16x4*)(a + GAP));
  return __builtin_bit_cast(bf16x8, __builtin_shufflevector(lo, hi, 0, 1, 2, 3, 4, 5, 6, 7));
}
__device__ __forceinline__ void tile_frags(LAS char* img, int lane, bf16x8 (&w)[4]) {
  const int row = lane >> 2, p = lane & 3;
#pragma unroll
  for (int t = 0; t < 4; ++t) w[t] = tr_frag<2048>(img + row * 128 + (((2 * t + (p >> 1)) ^ (row & 6)) << 4) + 8 * (p & 1));
}
__device__ __forceinline__ f32x4 mma(bf16x8 a, bf16x8 b, f32x4 c) { return __builtin_amdgcn_mfma_f32_16x16x32_bf16(a, b, c, 0, 0, 0); }
__device__ __forceinline__ bf16x8 asb(u32x4 v) { return __builtin_bit_cast(bf16x8, v); }

#define STAMP(k) do { if ((k) == PROBE) { SCHED_FENCE(); pst = __builtin_amdgcn_s_memrealtime(); SCHED_FENCE(); } } while (0)
#define STAMP_OUT() do { if (wave == PROBE_W && pst) while (__builtin_amdgcn_s_memrealtime() < pst + 3000) __builtin_amdgcn_s_sleep(2); } while (0)

__global__ void __launch_bounds__(512, 2)
fused_decode(const u16* x, const u16* kc, const u16* vc, const u16* wq, const u16* wo, const u16* wg, const u16* wu,
             const u16* wd, u16* out, char* ws) {
  __shared__ __attribute__((aligned(16))) char lds_raw[L_END];
  LAS char* lds = (LAS char*)lds_raw;
  u64 pst = 0;
  asm volatile("s_memrealtime %0\n\ts_waitcnt lgkmcnt(0)" : "=s"(pst));
  SCHED_FENCE();
  const int bid = blockIdx.x, lane = threadIdx.x & 63, wave = __builtin_amdgcn_readfirstlane(threadIdx.x >> 6);
  const int hk = bid & 7, idx = bid >> 3, w4 = wave & 3;
  const bool loader = wave >= 4;
  const __attribute__((address_space(4))) u64* ka = (const __attribute__((address_space(4))) u64*)__builtin_amdgcn_kernarg_segment_ptr();
  asm volatile("" : "+s"(ka));
  const u64 p_x = ka[0], p_k = ka[1], p_v = ka[2], p_wq = ka[3], p_wo = ka[4], p_wg = ka[5], p_wu = ka[6], p_wd = ka[7], p_out = ka[8], p_ws = ka[9];
#define ARGS_IN "s"(p_x), "s"(p_k), "s"(p_v), "s"(p_wq), "s"(p_wo), "s"(p_wg), "s"(p_wu), "s"(p_wd), "s"(p_out), "s"(p_ws)
#define KV_OFF ((((idx >> 3) * 8 + hk) * SK + (SK - WIN) + (idx & 7) * 256) * 512 + w4 * 32768)
#define SEQ ((unsigned)(0x12075643120ull >> ((idx & 3) * 4)))
#define T_OFF (P0 + (3 - (idx >> 3)) * STG)
#define is_wo (idx >= 16)
#define late (idx >= 9)
#define is_red (idx < 7)
#define wo_rg (((hk + 1 + ((idx >> 1) & 3)) & 7) * 8 + ((idx >> 1) & 7))
#define dn_un (bid - 72)
#define late_rg (is_wo ? wo_rg : dn_un >> 1)
#define late_ch ((is_wo ? idx : dn_un) & 1)
#define oi ((bid & 3) * DM + (bid >> 2) * 32 + (lane & 31))
#define rws make_rsrc(p_ws, WS_END)
  const unsigned l16 = lane * 16;
  unsigned tag = 0, xv = 0, cpf = 0, t_k = 0;

  if (!loader) {
    const unsigned iv = (lane & 16) ? 0xffffffffu : 0u;
    lds_st<u32x2>(lds, L_SYNC + lane * 8, u32x2{iv, iv});
    if (CP) cpf = __builtin_amdgcn_raw_buffer_load_b32(make_rsrc(__builtin_amdgcn_s_getpc() & ~127ull, CP), 0, (idx * 4 + w4) * 128, 0);
    asm volatile("" :: ARGS_IN);
    WG_BARRIER();
    t_k = (unsigned)__builtin_amdgcn_s_memrealtime();
    PIN_S(t_k);
    STAMP(1);
  }
  MEM_FENCE();
  if (loader) {
    const bool is_q = idx < 16, is_gate = idx < 23, has_early = idx < 30;
    const int half = hk & 1;
    int et_q = idx + 16 * ((idx + (hk >> 1)) & 3), et_f = (idx - (is_gate ? 16 : 23)) * 4 + (hk >> 1);
    asm("" : "+s"(et_q), "+s"(et_f));
    const int etile = is_q ? et_q : et_f;
    const int r8 = lane >> 3;
    LAS char* img = lds + L_TILE + w4 * 4096;
    const unsigned stride = is_q ? QW * 2 : FF * 2;
    unsigned evo = r8 * stride + (((lane & 7) ^ (r8 & 6)) << 4), eso = (half * 224 + w4 * 32) * stride + etile * 128;
    unsigned ebytes = has_early ? DM * stride : 0, eso2 = eso + 128 * stride, estep = 8 * stride - 1024;
    PIN_V(evo);
    asm volatile("" : "+s"(eso), "+s"(estep), "+s"(ebytes), "+s"(eso2));
    SCHED_FENCE();
    asm volatile("" :: ARGS_IN : "memory");
    STAMP(20);
    {
      const rsrc_t rw = make_rsrc(is_q ? p_wq : (is_gate ? p_wg : p_wu), ebytes);
      tile_fetch(rw, evo, eso, estep, img);
      if (w4 < 3) tile_fetch(rw, evo, eso2, estep, img + 4 * 4096);
    }
    WG_BARRIER_BARE();
    const unsigned t_go = (unsigned)__builtin_amdgcn_s_memrealtime() + T_OFF;
    SCHED_FENCE();
    STAMP(22);
    const u32x4 xr = __builtin_amdgcn_raw_buffer_load_b128(make_rsrc(p_x, NB * DM * 2), l16, w4 * DM * 2, 0);
    const unsigned epoch = __builtin_amdgcn_raw_buffer_load_b32(rws, 0, WS_EPOCH, EAUX);
    SCHED_FENCE();
    STAMP(1);
    {
      if (PROBE == 21) VM_WAIT(1);
      STAMP(21);
      float v[8], t = 0.f;
#pragma unroll
      for (int k = 0; k < 4; ++k) { v[2 * k] = bf_lo(xr[k]); v[2 * k + 1] = bf_hi(xr[k]); t += v[2 * k] + v[2 * k + 1]; }
      const float mean = wave_sum(lane < 56 ? t : 0.f) * (1.0f / DM);
      t = 0.f;
#pragma unroll
      for (int k = 0; k < 8; ++k) { const float d = v[k] - mean; t = fmaf(d, d, t); }
      const float var = round_bf16(wave_sum(lane < 56 ? t : 0.f) * (1.0f / DM)), mb = round_bf16(mean);
      const float rc = __builtin_amdgcn_rcpf(round_bf16(__builtin_amdgcn_sqrtf(round_bf16(var + 1e-5f))));
      u32x4 xn;
#pragma unroll
      for (int k = 0; k < 4; ++k) {
        const unsigned d = pack_bf16(v[2 * k] - mb, v[2 * k + 1] - mb);
        xn[k] = pack_bf16(bf_lo(d) * rc, bf_hi(d) * rc);
      }
      lds_st<u32x4>(lds, L_XN + w4 * 1024 + lane * 16, xn);
      lds_flag(lds, F_XN + w4 * 4, 1u);
      lds_wait_all(lds, F_XN);
      tag = next_tag(epoch);
      PIN_S(tag);
      const int bt = lane & 3, g = lane >> 4, jj = (lane >> 2) & 3;
      const int xo = L_XN + bt * 1024 + (half * 224 + w4 * 32 + 4 * g) * 2;
      const u32x2 a0 = lds_ld<u32x2>(lds, xo), a1 = lds_ld<u32x2>(lds, xo + 32);
      const u32x2 a2 = lds_ld<u32x2>(lds, xo + 256), a3 = lds_ld<u32x2>(lds, xo + 288);
      const u32x4 act0 = {a0[0], a0[1], a1[0], a1[1]}, act1 = {a2[0], a2[1], a3[0], a3[1]}, zero = {0u, 0u, 0u, 0u};
      VM_DRAIN();
      bf16x8 wf0[4], wf1[4];
      tile_frags(img, lane, wf0);
      f32x4 acc = {0.f, 0.f, 0.f, 0.f};
      if (w4 < 3) {
        tile_frags(img + 4 * 4096, lane, wf1);
#pragma unroll
        for (int t = 0; t < 4; ++t) acc = mma(wf1[t], asb(jj == t ? act1 : zero), acc);
      }
#pragma unroll
      for (int t = 0; t < 4; ++t) acc = mma(wf0[t], asb(jj == t ? act0 : zero), acc);
      lds_st<f32x4>(img, bt * 256 + ((lane & 12) + g) * 16, acc);
      lds_flag(lds, F_EP + w4 * 4, 1u);
      if (w4 == 3 && has_early) {
        const unsigned voff = is_q ? WS_QP + (etile >> 3) * 16384 + (lane >> 4) * 4096 + (etile & 6) * 512 + half * 512 + (etile & 1) * 256 + (lane & 15) * 16
                                   : WS_GP + etile * 4096 + (is_gate ? 0 : 2048) + half * 1024 + l16;
        lds_wait_all(lds, F_EP);
        f32x4 sum = lds_ld<f32x4>(lds, L_TILE + lane * 16);
#pragma unroll
        for (int p = 1; p < 4; ++p) sum += lds_ld<f32x4>(lds, L_TILE + p * 4096 + lane * 16);
        st_sc1(tag4_fine(sum, tag), rws, voff, 0);
      }
    }
    STAMP(2);
    const rsrc_t rv = make_rsrc(p_v + KV_OFF, 32768);
    const unsigned hf = lane >> 5;
    unsigned vo0 = (lane ^ hf) * 16, vo1 = (lane ^ (2 + hf)) * 16, vo2 = (lane ^ (4 + hf)) * 16, vo3 = (lane ^ (6 + hf)) * 16;
    const int lcol = late_ch * 4 + w4;
    const bool has_tile = late && lcol < 7;
    const rsrc_t rl = make_rsrc(is_wo ? p_wo : p_wd, (is_wo ? QW : FF) * DM * 2);
    unsigned lvo = r8 * (DM * 2) + (((lane & 7) ^ (r8 >> 1)) << 4), lso = late_rg * (64 * DM * 2) + lcol * 128;
    unsigned rvo = (((lane >> 3) & 1) * 4 + (lane >> 4)) * RP_BOX + (lane & 7) * 16;
    unsigned rso = WS_RP + lcol * 8 * RP_BOX + ((is_wo ? 0 : 64) + late_rg) * 128;
    LAS char *aa[4], *ta[4];
#pragma unroll
    for (int t = 0; t < 4; ++t) {
      aa[t] = lds + ((lane & 3) == t ? L_A + ((lane >> 2) & 3) * 64 + (lane >> 4) * 16 : L_ZERO);
      ta[t] = img + (lane >> 2) * 128 + (((lane & 2) * 2 + (t ^ ((lane >> 3) & 3))) << 4) + 8 * (lane & 1);
      PIN_V(aa[t]); PIN_V(ta[t]);
    }
    PIN_V(vo0); PIN_V(vo1); PIN_V(vo2); PIN_V(vo3); PIN_V(lvo); PIN_V(rvo); PIN_S(lso); PIN_S(rso);
    WG_BARRIER();
    STAMP(3);
    _Pragma("nounroll") for (int g = 0; g < 8; ++g) {
      const unsigned so = ((SEQ >> (4 * g)) & 7) << 12;
      PACE((8 + g) * DT);
      LAS void* dst = (LAS void*)(lds + L_V + (g * 4 + w4) * 4096);
      __builtin_amdgcn_raw_ptr_buffer_load_lds(rv, dst, 16, vo0, so, 0, KV_AUX);
      __builtin_amdgcn_raw_ptr_buffer_load_lds(rv, dst, 16, vo1, so, 1024, KV_AUX);
      __builtin_amdgcn_raw_ptr_buffer_load_lds(rv, dst, 16, vo2, so, 2048, KV_AUX);
      __builtin_amdgcn_raw_ptr_buffer_load_lds(rv, dst, 16, vo3, so, 3072, KV_AUX);
      VM_WAIT(8);
      lds_flag(lds, L_PROG + w4 * 4, g + 1);
    }
    STAMP(4);
    VM_WAIT(4);
    lds_flag(lds, L_PROG + w4 * 4, 9);
    const bool before = has_tile && is_wo;
    if (!before) { VM_WAIT(0); STAMP(5); WG_BARRIER_BARE(); }
    if (has_tile) {
      LSPIN() { const u32x4 f = lds_peek(lds, F_PV); if (wave_none((f[0] & f[1]) == 0u)) break; }
      PACE(15 * DT + (is_wo ? PL : PLD));
      tile_fetch(rl, lvo, lso, 8 * DM * 2 - 1024, img);
      tile_fetch(rl, lvo, lso + 32 * DM * 2, 8 * DM * 2 - 1024, img + 4 * 4096);
      if (before) {
        VM_WAIT(8);
        STAMP(5);
        WG_BARRIER_BARE();
        VM_WAIT(FPW);
        lds_flag(lds, F_T + w4 * 4, 1u);
      }
      VM_DRAIN();
      bf16x8 wf[8];
#pragma unroll
      for (int t = 0; t < 4; ++t) { wf[t] = tr_frag<2048>(ta[t]); wf[4 + t] = tr_frag<2048>(ta[t] + 4 * 4096); }
      STAMP(8);
      if (is_wo) WG_BARRIER();
      STAMP(10);
      u32x4 act[8];
#pragma unroll
      for (int t = 0; t < 4; ++t) { act[2 * t] = lds_ld<u32x4>(aa[t], 0); act[2 * t + 1] = lds_ld<u32x4>(aa[t], 256); }
      SCHED_FENCE();
      f32x4 acc = {0.f, 0.f, 0.f, 0.f};
#pragma unroll
      for (int t = 0; t < 4; ++t) { acc = mma(asb(act[2 * t]), wf[t], acc); acc = mma(asb(act[2 * t + 1]), wf[4 + t], acc); }
      st_sc1(tag4(acc, tag), rws, rvo, rso);
      STAMP(11);
    } else if (is_wo) {
      WG_BARRIER_BARE();
    }
  }
  int role = wave;
  PIN_S(role);
  if (role < 4) {
    u32x4 kr[32], qv;
#define qso (WS_QP + hk * 16384 + (idx >> 3) * 4096 + w4 * 1024)
    const unsigned kvo = (lane & 7) * 512 + ((lane >> 3) & 1) * 64 + (lane >> 4) * 16, seq = SEQ;
    const rsrc_t rk = make_rsrc(p_k + KV_OFF, 32768);
    const unsigned t_go = t_k + T_OFF;
#pragma unroll
    for (int g = 0; g < 8; ++g) {
      const unsigned so = ((seq >> (4 * g)) & 7) << 12;
      PACE(g * DT);
#pragma unroll
      for (int i = 0; i < 4; ++i) kr[g * 4 + i] = __builtin_amdgcn_raw_buffer_load_b128(rk, kvo + i * 128, so, KV_AUX);
    }
    SCHED_FENCE();
    const unsigned kepoch = __builtin_amdgcn_raw_buffer_load_b32(rws, 0, WS_EPOCH, EAUX);
    qv = ld_sc1(rws, l16, qso);
    STAMP(2);
    WG_BARRIER_BARE();
    STAMP(3);
    tag = next_tag(kepoch);
    asm volatile("" : "+s"(tag) : "v"(cpf));
    SPIN(SPIN_MAX) {
      qv ^= tag_nibbles(tag);
      if (wave_none((or4(qv) & 15u) != 0u)) break;
      MEM_FENCE();
      qv = ld_sc1(rws, l16, qso);
    }
    lds_st<unsigned>(lds, L_Q + w4 * 256 + (lane & 31) * 8 + (lane >> 5) * 4,
                     pack_bf16(fold32(asf(qv[0]), asf(qv[2])), fold32(asf(qv[1]), asf(qv[3]))));
    lds_flag(lds, F_Q + w4 * 4, 1u);
    lds_wait_all(lds, F_Q);
    STAMP(4);
    float m0, m1;
    {
      const int qa = L_Q + (lane & 1) * 512 + ((lane >> 1) & 1) * 64 + (lane >> 4) * 16;
      f32x4 c0, c1;
      {
        u32x4 qf[4];
#pragma unroll
        for (int s = 0; s < 4; ++s) qf[s] = lds_ld<u32x4>(lds, qa + s * 128);
        f32x4 c[8];
#pragma unroll
        for (int t = 0; t < 8; ++t) c[t] = mma(asb(qf[0]), asb(kr[t * 4]), f32x4{0.f, 0.f, 0.f, 0.f});
#pragma unroll
        for (int s = 1; s < 4; ++s)
#pragma unroll
          for (int t = 0; t < 8; ++t) c[t] = mma(asb(qf[s]), asb(kr[t * 4 + s]), c[t]);
        const bool g1 = lane & 16, g2 = lane & 32;
        c0 = g2 ? (g1 ? c[3] : c[2]) : (g1 ? c[1] : c[0]);
        c1 = g2 ? (g1 ? c[7] : c[6]) : (g1 ? c[5] : c[4]);
      }
      const bool cb = lane & 8;
      const float r0 = (cb ? c1[2] : c0[0]) + dpp<XOR8>(cb ? c0[2] : c1[0]);
      const float r1 = (cb ? c1[3] : c0[1]) + dpp<XOR8>(cb ? c0[3] : c1[1]);
      const unsigned sp = pack_bf16(r0, r1);
      const float s0 = bf_lo(sp) * 0.0625f, s1 = bf_hi(sp) * 0.0625f;
      float mw = max32(s0, s1);
      mw = row_max(max16(mw, mw));
      *(volatile LAS unsigned*)(lds + L_MX + (lane >> 5) * 16 + w4 * 4) = asu(mw) & 0xfffffffeu;
      u32x4 x0, x1;
      LSPIN() { x0 = lds_peek(lds, L_MX); x1 = lds_peek(lds, L_MX + 16); if (wave_none(unset4(x0) | unset4(x1))) break; }
      m0 = fmaxf(fmaxf(asf(x0[0]), asf(x0[1])), fmaxf(asf(x0[2]), asf(x0[3])));
      m1 = fmaxf(fmaxf(asf(x1[0]), asf(x1[1])), fmaxf(asf(x1[2]), asf(x1[3])));
      const float p0 = __builtin_amdgcn_exp2f((s0 - m0) * LOG2E), p1 = __builtin_amdgcn_exp2f((s1 - m1) * LOG2E);
      const unsigned pk = pack_bf16(p0, p1);
      const int po = L_P + (((lane >> 3) & 1) * 4 + (lane >> 4)) * 64 + ((w4 & 1) * 2 + ((lane >> 2) & 1)) * 16 + ((w4 >> 1) * 4 + (lane & 3)) * 2;
      lds_st<u16>(lds, po, (u16)pk);
      lds_st<u16>(lds, po + 512, (u16)(pk >> 16));
      float l = fold32(p0, p1);
      l = row_sum(fold16(l, l));
      MEM_FENCE();
      *(volatile LAS unsigned*)(lds + L_LS + (lane >> 5) * 16 + w4 * 4) = asu(l) & 0xfffffffeu;
    }
    u32x4 ls;
    LSPIN() { ls = lds_peek(lds, L_LS + (lane >> 5) * 16); if (wave_none(unset4(ls))) break; }
    MEM_FENCE();
    STAMP(5);
    u32x4 tb = tag_bytes(tag);
    unsigned oso = WS_OP + wo_rg * 8192 + w4 * 2048;
    LAS char* ad = lds + L_A + (lane & 4) * 64 + w4 * 64 + ((lane & 1) * 2 + ((lane >> 3) & 1)) * 16 + ((lane & 2) * 2 + (lane >> 4)) * 2;
    LAS char* fa = lds + L_F + w4 * 32 + (lane >> 3) * 4;
    PIN_S(tb); PIN_S(oso); PIN_V(ad); PIN_V(fa);
    if (w4 < 2) {
      const int g = lane >> 4, row = lane >> 2, p = lane & 3, tl = lane & 7;
      LAS char* va[8];
#pragma unroll
      for (int t = 0; t < 8; ++t) va[t] = lds + L_V + row * 512 + ((w4 * 16 + (p >> 1) * 8 + (t ^ (row & 7))) << 4) + 8 * (p & 1);
      LAS char* pa = lds + L_P + ((lane >> 3) & 1) * 512 + g * 16;
      unsigned ovo = (((lane >> 3) & 1) * 4 + (g >> 1)) * 8192 + (g & 1) * 1024 + tl * 16;
      unsigned pso = WS_OP + (hk * 8 + w4 * 2) * 8192 + (idx >> 3) * 2048 + (idx & 7) * 128;
      PIN_V(ovo); PIN_S(pso);
      f32x4 o = {0.f, 0.f, 0.f, 0.f};
      u32x4 pm[8];
      {
        const u32x4 pl = lds_ld<u32x4>(pa, 0);
#pragma unroll
        for (int t = 0; t < 8; ++t) pm[t] = tl == t ? pl : u32x4{0u, 0u, 0u, 0u};
      }
      _Pragma("nounroll") for (int ks = 0; ks < 7; ++ks) {
        LSPIN() {
          const u32x4 pr = lds_peek(lds, L_PROG);
          if (wave_none(min(min(pr[0], pr[1]), min(pr[2], pr[3])) <= (unsigned)(ks + 2))) break;
        }
        lds_flag(lds, F_PV + w4 * 4, ks >= 2 ? 1u : 0u);
        pa += 64;
        const u32x4 pl = lds_ld<u32x4>(pa, 0);
        bf16x8 vf[8];
#pragma unroll
        for (int t = 0; t < 8; ++t) vf[t] = tr_frag<8192>(va[t]);
        SCHED_FENCE();
#pragma unroll
        for (int t = 0; t < 8; ++t) {
          o = mma(vf[t], asb(pm[t]), o);
          SCHED_FENCE();
          pm[t] = tl == t ? pl : u32x4{0u, 0u, 0u, 0u};
          va[t] += 16384;
          PIN_V(pm[t]); PIN_V(va[t]);
          SCHED_FENCE();
        }
      }
      STAMP(6);
      WG_BARRIER_BARE();
      {
        bf16x8 vf[8];
#pragma unroll
        for (int t = 0; t < 8; ++t) vf[t] = tr_frag<8192>(va[t]);
        SCHED_FENCE();
#pragma unroll
        for (int t = 0; t < 8; ++t) o = mma(vf[t], asb(pm[t]), o);
      }
      st_sc1(tag4(o, tag), rws, ovo, pso);
      STAMP(7);
    } else if (w4 == 2) {
      if (late && !is_wo) {
        u32x4 gv[4];
        SPIN(SPIN_MAX) {
          unsigned bad = 0;
#pragma unroll
          for (int u = 0; u < 4; ++u) gv[u] = ld_sc1(rws, l16 + u * 1024, WS_GP + (dn_un >> 1) * 4096);
#pragma unroll
          for (int u = 0; u < 4; ++u) { gv[u] ^= tag_nibbles(tag); bad |= or4(gv[u]); }
          if (wave_none((bad & 15u) != 0u)) break;
          MEM_FENCE();
        }
        const f32x4 gs = asf4(gv[0]) + asf4(gv[1]), us = asf4(gv[2]) + asf4(gv[3]);
        u32x2 hv;
#pragma unroll
        for (int e = 0; e < 2; ++e) {
          const unsigned gt = pack_bf16(gs[2 * e], gs[2 * e + 1]), up = pack_bf16(us[2 * e], us[2 * e + 1]);
          const float g0 = bf_lo(gt), g1 = bf_hi(gt);
          hv[e] = pack_bf16(g0 * (g0 > 0.f ? 1.f : 0.f) * bf_lo(up), g1 * (g1 > 0.f ? 1.f : 0.f) * bf_hi(up));
        }
        lds_st<u32x2>(lds, L_A + (lane & 8) * 32 + (lane >> 4) * 64 + (lane & 3) * 16 + (lane & 4) * 2, hv);
      } else if (is_red) {
        xv = __builtin_amdgcn_raw_buffer_load_b16(make_rsrc(p_x, NB * DM * 2), oi * 2, 0, 0);
        PIN_V(xv);
      }
      WG_BARRIER();
    } else {
      const float lsum = (asf(ls[0]) + asf(ls[1])) + (asf(ls[2]) + asf(ls[3]));
      const float l1 = asf(__builtin_amdgcn_readlane(asu(lsum), 32));
      if (lane == 0) st_sc1(tag4(f32x4{m0, lsum, m1, l1}, tag), rws, 0, WS_ML + (hk * 32 + idx) * 16);
      const unsigned mso = is_wo ? WS_ML + (wo_rg >> 3) * 512 : WS_END;
      LSPIN() { if (wave_none(*(volatile LAS unsigned*)(lds + L_PROG) < (unsigned)MLAT)) break; __builtin_amdgcn_s_sleep(4); }
      u32x4 mv = ld_sc1(rws, (lane & 31) * 16, mso);
      WG_BARRIER_BARE();
      mv ^= tb;
      PIN_V(mv);
      if (is_wo) {
        SPIN(SPIN_MAX) {
          if (wave_none((or4(mv) & 255u) != 0u)) break;
          MEM_FENCE();
          mv = ld_sc1(rws, (lane & 31) * 16, mso) ^ tb;
        }
        const float m = asf((idx & 8) ? mv[2] : mv[0]);
        float mx = fmaxf(m, dpp<XOR7>(m));
        mx = fmaxf(mx, dpp<XOR2>(mx));
        mx = fmaxf(mx, dpp<XOR1>(mx));
        const float e = __builtin_amdgcn_exp2f((m - mx) * LOG2E);
        float lt = e * asf((idx & 8) ? mv[3] : mv[1]);
        lt += dpp<XOR7>(lt);
        lt += dpp<XOR2>(lt);
        lt += dpp<XOR1>(lt);
        *(volatile LAS unsigned*)(lds + L_F + (lane & 31) * 4) = asu(e * __builtin_amdgcn_rcpf(lt)) & 0xfffffffeu;
      } else if (bid == 56) {
        SPIN(SPIN_MAX) {
          unsigned bad = 0;
#pragma unroll
          for (int u = 0; u < 4; ++u) bad |= or4(ld_sc1(rws, l16 + u * 1024, WS_ML) ^ tb);
          if (wave_none((bad & 255u) != 0u)) break;
          MEM_FENCE();
        }
        if (lane == 0) __builtin_amdgcn_raw_buffer_store_b32(tag, rws, 0, WS_EPOCH, AUX_SC1);
      }
    }
    if (is_wo) {
      float f;
      LSPIN() {
        f = *(volatile LAS float*)fa;
        if (wave_none((asu(f) == 0xffffffffu) | (*(volatile LAS unsigned*)(lds + F_T) == 0u))) break;
      }
      STAMP(8);
      u32x4 o0, o1;
      MEM_FENCE();
      SPIN(SPIN_MAX) {
        o0 = ld_sc1(rws, l16, oso) ^ tb;
        o1 = ld_sc1(rws, l16 + 1024, oso) ^ tb;
        if (wave_none(((or4(o0) | or4(o1)) & 255u) != 0u)) break;
        if (OSL) __builtin_amdgcn_s_sleep(OSL);
        MEM_FENCE();
      }
      STAMP(9);
      const float o = fold_pair(asf4(o0) * f, asf4(o1) * f);
      lds_st<u16>(ad, 0, (u16)pack_bf16(o, o));
      WG_BARRIER();
    }
  }

  if (is_red) {
    const u32x4 tb = tag_bytes(tag);
    LAS char* fw = lds + L_FIN + ((lane >> 4) * 8 + (lane & 7)) * 64 + wave * 4 + (lane & 8) * 4;
    LAS char* fr = lds + L_FIN + (lane & 31) * 64 + (lane >> 5) * 32;
    unsigned oo = (lane & 32) ? 0x7fffffffu : oi * 2;
    const bool dn_lane = wave * 64 + lane < 28 * 8;
    unsigned dvo = dn_lane ? l16 + 8192 : 0x7fffffffu;
    u32x4 td = dn_lane ? tb : u32x4{0u, 0u, 0u, 0u};
    float xf = bf_lo(xv);
    PIN_V(fw); PIN_V(fr); PIN_V(oo); PIN_V(xf); PIN_V(dvo); PIN_V(td);
    const rsrc_t ro = make_rsrc(p_out, NB * DM * 2);
    const unsigned so = WS_RP + bid * RP_BOX + wave * 1024;
    u32x4 a0, a1;
    if (RD) __builtin_amdgcn_s_sleep(RD);
    STAMP(8);
    SPIN(SPIN_MAX) {
      a1 = ld_sc1(rws, dvo, so) ^ td;
      a0 = ld_sc1(rws, l16, so) ^ tb;
      if (wave_none(((or4(a0) | or4(a1)) & 255u) != 0u)) break;
      if (RSL) __builtin_amdgcn_s_sleep(RSL);
      MEM_FENCE();
    }
    STAMP(9);
    lds_st<float>(fw, 0, fold_pair(asf4(a0), asf4(a1)));
    WG_BARRIER();
    STAMP(10);
    if (wave == 2) {
      const f32x4 t = lds_ld<f32x4>(fr, 0) + lds_ld<f32x4>(fr, 16);
      const f32x2 t2 = f32x2{t[0], t[1]} + f32x2{t[2], t[3]};
      const float u = round_bf16(t2[0] + t2[1]);
      auto sw = __builtin_amdgcn_permlane32_swap(asu(u), asu(u), false, false);
      const float sv = asf(sw[0]), dn = asf(sw[1]);
      const float g = round_bf16(dn * __builtin_amdgcn_rcpf(1.0f + __builtin_amdgcn_exp2f(-dn * LOG2E)));
      const float res = xf + round_bf16(sv * g);
      __builtin_amdgcn_raw_buffer_store_b16((u16)pack_bf16(res, res), ro, oo, 0, 0);
      STAMP(11);
    }
  }
  STAMP_OUT();
}

}

extern "C" void kernel_launch(void* const* d_in, const int* in_sizes, int n_in, void* d_out, int out_size, void* d_ws,
                              size_t ws_size, hipStream_t stream) {
  fused_decode<<<256, 512, 0, stream>>>((const u16*)d_in[0], (const u16*)d_in[1], (const u16*)d_in[2], (const u16*)d_in[3],
                                        (const u16*)d_in[4], (const u16*)d_in[6], (const u16*)d_in[7], (const u16*)d_in[8],
                                        (u16*)d_out, (char*)d_ws + WS_OFF);
}
```

```cpp
#include <hip/hip_runtime.h>
#include <hip/hip_bf16.h>
#include <stdint.h>

namespace {

typedef unsigned short u16;
typedef unsigned long long u64;
typedef unsigned u32x2 __attribute__((ext_vector_type(2)));
typedef unsigned u32x4 __attribute__((ext_vector_type(4)));
typedef float f32x2 __attribute__((ext_vector_type(2)));
typedef float f32x4 __attribute__((ext_vector_type(4)));
typedef short s16x4 __attribute__((ext_vector_type(4)));
typedef __bf16 bf16x2 __attribute__((ext_vector_type(2)));
typedef __bf16 bf16x8 __attribute__((ext_vector_type(8)));
#define LAS __attribute__((address_space(3)))
typedef __amdgpu_buffer_rsrc_t rsrc_t;

constexpr int NB = 4, DM = 448, SK = 8192, WIN = 2048, FF = 1792, QW = 4096;
constexpr int AUX_SC1 = 16, AUX_NT = 2;
constexpr unsigned SPIN_MAX = 1u << 22;
constexpr unsigned CODE_BYTES = 11560;
constexpr int T_FIRST = 58;
constexpr int T_STAGGER = 14;
constexpr int T_STEP = 54;
constexpr int T_WO = 64, T_DOWN = 70;
constexpr float LOG2E = 1.4426950408889634f;

constexpr unsigned WS_EPOCH = 0;
constexpr unsigned WS_QP = 4096;
constexpr unsigned WS_GP = WS_QP + 8 * 4 * 4096;
constexpr unsigned WS_OP = WS_GP + 28 * 4096;
constexpr unsigned WS_ML = WS_OP + 64 * 8192;
constexpr unsigned WS_RP = WS_ML + 4096;
constexpr unsigned RP_BOX = 92 * 128;
constexpr unsigned WS_END = WS_RP + 56 * RP_BOX;

constexpr int L_V = 0;
constexpr int L_TILE = L_V;
constexpr int L_XN = L_V + 32768;
constexpr int L_FIN = L_V;
constexpr int L_Q = 131072;
constexpr int L_P = L_Q + 1024;
constexpr int L_A = L_P + 1024;
constexpr int L_SYNC = L_A + 512;
constexpr int L_ZERO = L_SYNC;
constexpr int F_XN = L_SYNC + 16, F_EP = L_SYNC + 32, F_Q = L_SYNC + 48, L_PROG = L_SYNC + 64, F_PV = L_SYNC + 80, F_T = L_SYNC + 96;
constexpr int L_F = L_SYNC + 128;
constexpr int L_MX = L_SYNC + 384;
constexpr int L_LS = L_MX + 32;
constexpr int L_END = L_SYNC + 512;

#define SCHED_FENCE() __builtin_amdgcn_sched_barrier(0)
#define MEM_FENCE() asm volatile("" ::: "memory")
#define WG_BARRIER() do { SCHED_FENCE(); asm volatile("s_waitcnt lgkmcnt(0)\n\ts_barrier" ::: "memory"); SCHED_FENCE(); } while (0)
#define WG_BARRIER_BARE() do { SCHED_FENCE(); asm volatile("s_barrier" ::: "memory"); SCHED_FENCE(); } while (0)
#define VM_WAIT(n) do { SCHED_FENCE(); asm volatile("s_waitcnt vmcnt(%0)" :: "i"(n) : "memory"); SCHED_FENCE(); } while (0)
#define VM_DRAIN() do { SCHED_FENCE(); __builtin_amdgcn_s_waitcnt(0x0F70); SCHED_FENCE(); MEM_FENCE(); } while (0)
#define NAP() __builtin_amdgcn_s_sleep(1)
#define PACE(t) do { SCHED_FENCE(); pace_until(t_go + (t)); SCHED_FENCE(); } while (0)
#define PIN_V(x) asm volatile("" : "+v"(x))
#define PIN_S(x) asm volatile("" : "+s"(x))
#define LSPIN() _Pragma("nounroll") for (;;)
#define SPIN(limit) _Pragma("nounroll") for (unsigned s_ = 0; s_ < (limit); ++s_)

__device__ __forceinline__ void pace_until(unsigned when) {
  asm volatile("1:\n\ts_memrealtime vcc\n\ts_waitcnt lgkmcnt(0)\n\ts_sub_i32 vcc_lo, vcc_lo, %0\n\ts_cmp_lt_i32 vcc_lo, 0\n\ts_cbranch_scc1 1b"
               :: "s"(when) : "vcc", "scc", "memory");
}
__device__ __forceinline__ float asf(unsigned u) { return __builtin_bit_cast(float, u); }
__device__ __forceinline__ unsigned asu(float f) { return __builtin_bit_cast(unsigned, f); }
__device__ __forceinline__ unsigned pack_bf16(float lo, float hi) {
  f32x2 f = {lo, hi};
  return __builtin_bit_cast(unsigned, __builtin_convertvector(f, bf16x2));
}
__device__ __forceinline__ float round_bf16(float v) { return asf(pack_bf16(0.f, v)); }
__device__ __forceinline__ float bf_lo(unsigned u) { return asf(u << 16); }
__device__ __forceinline__ float bf_hi(unsigned u) { return asf(u & 0xffff0000u); }
template <int CTRL> __device__ __forceinline__ float dpp(float x) {
  return asf((unsigned)__builtin_amdgcn_mov_dpp((int)asu(x), CTRL, 0xf, 0xf, true));
}
constexpr int XOR1 = 0xB1, XOR2 = 0x4E, XOR7 = 0x141, XOR8 = 0x128;
__device__ __forceinline__ float fold16(float a, float b) {
  auto s = __builtin_amdgcn_permlane16_swap(asu(a), asu(b), false, false);
  return asf(s[0]) + asf(s[1]);
}
__device__ __forceinline__ float fold32(float a, float b) {
  auto s = __builtin_amdgcn_permlane32_swap(asu(a), asu(b), false, false);
  return asf(s[0]) + asf(s[1]);
}
__device__ __forceinline__ float max16(float a, float b) {
  auto s = __builtin_amdgcn_permlane16_swap(asu(a), asu(b), false, false);
  return fmaxf(asf(s[0]), asf(s[1]));
}
__device__ __forceinline__ float max32(float a, float b) {
  auto s = __builtin_amdgcn_permlane32_swap(asu(a), asu(b), false, false);
  return fmaxf(asf(s[0]), asf(s[1]));
}
__device__ __forceinline__ float row_sum(float v) {
  v += dpp<XOR8>(v); v += dpp<XOR7>(v); v += dpp<XOR2>(v); v += dpp<XOR1>(v);
  return v;
}
__device__ __forceinline__ float wave_sum(float v) { v = fold32(v, v); return row_sum(fold16(v, v)); }
__device__ __forceinline__ float row_max(float v) {
  v = fmaxf(v, dpp<XOR8>(v)); v = fmaxf(v, dpp<XOR7>(v)); v = fmaxf(v, dpp<XOR2>(v)); v = fmaxf(v, dpp<XOR1>(v));
  return v;
}
__device__ __forceinline__ float fold_pair(f32x4 a, f32x4 b) {
  const float a0 = fold32(a[0], a[2]), a1 = fold32(a[1], a[3]), b0 = fold32(b[0], b[2]), b1 = fold32(b[1], b[3]);
  SCHED_FENCE();
  const float za = fold16(a0, a1), zb = fold16(b0, b1);
  const bool hi = __builtin_amdgcn_inverse_ballot_w64(0xff00ff00ff00ff00ull);
  return (hi ? zb : za) + dpp<XOR8>(hi ? za : zb);
}
__device__ __forceinline__ rsrc_t make_rsrc(u64 p, unsigned bytes) {
  return __builtin_amdgcn_make_buffer_rsrc((void*)p, 0, bytes, 0x00020000);
}
__device__ __forceinline__ u32x4 tag4(f32x4 v, unsigned tag) {
  u32x4 r;
  r[0] = __builtin_amdgcn_perm(asu(v[0]), tag, 0x07060500u);
  r[1] = __builtin_amdgcn_perm(asu(v[1]), tag, 0x07060501u);
  r[2] = __builtin_amdgcn_perm(asu(v[2]), tag, 0x07060502u);
  r[3] = __builtin_amdgcn_perm(asu(v[3]), tag, 0x07060503u);
  return r;
}
__device__ __forceinline__ unsigned next_tag(unsigned epoch) {
  const unsigned e = __builtin_amdgcn_readfirstlane(epoch);
  return ((e & 0x7f7f7f7fu) + 0x07050301u) ^ (e & 0x80808080u);
}
__device__ __forceinline__ u32x4 tag_bytes(unsigned tag) { return u32x4{tag & 255u, (tag >> 8) & 255u, (tag >> 16) & 255u, tag >> 24}; }
__device__ __forceinline__ u32x4 tag4_block(f32x4 v, unsigned tag) {
  const u32x4 a = __builtin_bit_cast(u32x4, v) & 0x7fffffffu, tb = tag_bytes(tag);
  const unsigned e = max(max(a[0], a[1]), max(a[2], a[3])) >> 23;
  u32x4 r;
#pragma unroll
  for (int k = 0; k < 4; ++k) {
    const float m = fminf(fmaxf(__builtin_rintf(__builtin_amdgcn_ldexpf(v[k], 147 - (int)e)), -2097152.f), 2097151.f);
    r[k] = ((unsigned)(int)m << 10) | (((e >> (2 * k)) & 3u) << 8) | tb[k];
  }
  return r;
}
__device__ __forceinline__ f32x4 block_values(u32x4 y) {
  const unsigned e = ((y[0] >> 8) & 3u) | ((y[1] >> 6) & 12u) | ((y[2] >> 4) & 48u) | ((y[3] >> 2) & 192u);
  f32x4 v;
#pragma unroll
  for (int k = 0; k < 4; ++k) v[k] = e == 255u ? asf(0x7fc00000u) : __builtin_amdgcn_ldexpf((float)((int)y[k] >> 10), (int)e - 147);
  return v;
}
__device__ __forceinline__ unsigned or4(u32x4 v) { return v[0] | v[1] | v[2] | v[3]; }
__device__ __forceinline__ f32x4 asf4(u32x4 v) { return __builtin_bit_cast(f32x4, v); }
__device__ __forceinline__ u32x4 ld_sc1(rsrc_t r, unsigned voff, unsigned soff) {
  return __builtin_amdgcn_raw_buffer_load_b128(r, voff, soff, AUX_SC1);
}
__device__ __forceinline__ void st_sc1(u32x4 v, rsrc_t r, unsigned voff, unsigned soff) {
  __builtin_amdgcn_raw_buffer_store_b128(v, r, voff, soff, AUX_SC1);
}
__device__ __forceinline__ bool wave_none(bool bad) { return __builtin_amdgcn_ballot_w64(bad) == 0ull; }

template <typename T> __device__ __forceinline__ T lds_ld(LAS char* l, int off) { return *(LAS T*)(l + off); }
template <typename T> __device__ __forceinline__ void lds_st(LAS char* l, int off, T v) { *(LAS T*)(l + off) = v; }
__device__ __forceinline__ void lds_flag(LAS char* l, int off, unsigned v) {
  MEM_FENCE();
  *(volatile LAS unsigned*)(l + off) = v;
  MEM_FENCE();
}
__device__ __forceinline__ u32x4 lds_peek(LAS char* l, int off) { return *(volatile LAS u32x4*)(l + off); }
__device__ __forceinline__ void lds_wait_all(LAS char* l, int off) {
  LSPIN() { const u32x4 f = lds_peek(l, off); if (wave_none((f[0] & f[1] & f[2] & f[3]) == 0u)) break; NAP(); }
  MEM_FENCE();
}
__device__ __forceinline__ bool unset4(u32x4 v) { return max(max(v[0], v[1]), max(v[2], v[3])) == 0xffffffffu; }

__device__ __forceinline__ void tile_fetch(rsrc_t r, unsigned voff, unsigned soff, unsigned step, LAS char* img) {
  __builtin_amdgcn_raw_ptr_buffer_load_lds(r, (LAS void*)img, 16, voff, soff, 0, AUX_NT);
  __builtin_amdgcn_raw_ptr_buffer_load_lds(r, (LAS void*)img, 16, voff, soff + step, 1024, AUX_NT);
  __builtin_amdgcn_raw_ptr_buffer_load_lds(r, (LAS void*)img, 16, voff, soff + 2 * step, 2048, AUX_NT);
  __builtin_amdgcn_raw_ptr_buffer_load_lds(r, (LAS void*)img, 16, voff, soff + 3 * step, 3072, AUX_NT);
}
template <int GAP> __device__ __forceinline__ bf16x8 tr_frag(LAS char* a) {
  const s16x4 lo = __builtin_amdgcn_ds_read_tr16_b64_v4i16((LAS s16x4*)a);
  const s16x4 hi = __builtin_amdgcn_ds_read_tr16_b64_v4i16((LAS s16x4*)(a + GAP));
  return __builtin_bit_cast(bf16x8, __builtin_shufflevector(lo, hi, 0, 1, 2, 3, 4, 5, 6, 7));
}
__device__ __forceinline__ void tile_frags(LAS char* img, int lane, bf16x8 (&w)[4]) {
  const int row = lane >> 2, p = lane & 3;
#pragma unroll
  for (int t = 0; t < 4; ++t) w[t] = tr_frag<2048>(img + row * 128 + (((2 * t + (p >> 1)) ^ (row & 6)) << 4) + 8 * (p & 1));
}
__device__ __forceinline__ f32x4 mma(bf16x8 a, bf16x8 b, f32x4 c) { return __builtin_amdgcn_mfma_f32_16x16x32_bf16(a, b, c, 0, 0, 0); }
__device__ __forceinline__ bf16x8 asb(u32x4 v) { return __builtin_bit_cast(bf16x8, v); }

__global__ void __launch_bounds__(512, 2)
fused_decode(const u16* x, const u16* kc, const u16* vc, const u16* wq, const u16* wo, const u16* wg, const u16* wu,
             const u16* wd, u16* out, char* ws) {
  __shared__ __attribute__((aligned(16))) char lds_raw[L_END];
  LAS char* lds = (LAS char*)lds_raw;
  const int bid = blockIdx.x, lane = threadIdx.x & 63, wave = __builtin_amdgcn_readfirstlane(threadIdx.x >> 6);
  const int hk = bid & 7, idx = bid >> 3, w4 = wave & 3;
  const bool loader = wave >= 4;
  const u64 p_x = (u64)x, p_k = (u64)kc, p_v = (u64)vc, p_wq = (u64)wq, p_wo = (u64)wo, p_wg = (u64)wg, p_wu = (u64)wu, p_wd = (u64)wd;
  const u64 p_out = (u64)out, p_ws = (u64)ws;
#define ARGS_IN "s"(p_x), "s"(p_k), "s"(p_v), "s"(p_wq), "s"(p_wo), "s"(p_wg), "s"(p_wu), "s"(p_wd), "s"(p_out), "s"(p_ws)
#define KV_OFF ((((idx >> 3) * 8 + hk) * SK + (SK - WIN) + (idx & 7) * 256) * 512 + w4 * 32768)
#define SEQ ((unsigned)(0x12075643120ull >> ((idx & 3) * 4)))
#define T_OFF (T_FIRST + (3 - (idx >> 3)) * T_STAGGER)
#define is_wo (idx >= 16)
#define late (idx >= 9)
#define is_red (idx < 7)
#define wo_rg (((hk + 1 + ((idx >> 1) & 3)) & 7) * 8 + ((idx >> 1) & 7))
#define dn_un (bid - 72)
#define late_rg (is_wo ? wo_rg : dn_un >> 1)
#define late_ch ((is_wo ? idx : dn_un) & 1)
#define oi ((bid & 3) * DM + (bid >> 2) * 32 + (lane & 31))
#define rws make_rsrc(p_ws, WS_END)
  const unsigned l16 = lane * 16;
  unsigned tag = 0, xv = 0, cpf = 0, t_k = 0;

  if (!loader) {
    const unsigned iv = (lane & 16) ? 0xffffffffu : 0u;
    lds_st<u32x2>(lds, L_SYNC + lane * 8, u32x2{iv, iv});
    cpf = __builtin_amdgcn_raw_buffer_load_b32(make_rsrc(__builtin_amdgcn_s_getpc() & ~127ull, CODE_BYTES), 0, (idx * 4 + w4) * 128, 0);
    asm volatile("" :: ARGS_IN);
    WG_BARRIER();
    t_k = (unsigned)__builtin_amdgcn_s_memrealtime();
    PIN_S(t_k);
  }
  MEM_FENCE();
  if (loader) {
    const bool is_q = idx < 16, is_gate = idx < 23, has_early = idx < 30;
    const int half = hk & 1;
    int et_q = idx + 16 * ((idx + (hk >> 1)) & 3), et_f = (idx - (is_gate ? 16 : 23)) * 4 + (hk >> 1);
    asm("" : "+s"(et_q), "+s"(et_f));
    const int etile = is_q ? et_q : et_f;
    const int r8 = lane >> 3;
    LAS char* img = lds + L_TILE + w4 * 4096;
    const unsigned stride = is_q ? QW * 2 : FF * 2;
    unsigned evo = r8 * stride + (((lane & 7) ^ (r8 & 6)) << 4), eso = (half * 224 + w4 * 32) * stride + etile * 128;
    unsigned ebytes = has_early ? DM * stride : 0, eso2 = eso + 128 * stride, estep = 8 * stride - 1024;
    PIN_V(evo);
    asm volatile("" : "+s"(eso), "+s"(estep), "+s"(ebytes), "+s"(eso2));
    SCHED_FENCE();
    asm volatile("" :: ARGS_IN : "memory");
    {
      const rsrc_t rw = make_rsrc(is_q ? p_wq : (is_gate ? p_wg : p_wu), ebytes);
      tile_fetch(rw, evo, eso, estep, img);
      if (w4 < 3) tile_fetch(rw, evo, eso2, estep, img + 4 * 4096);
    }
    WG_BARRIER_BARE();
    const unsigned t_go = (unsigned)__builtin_amdgcn_s_memrealtime() + T_OFF;
    SCHED_FENCE();
    const u32x4 xr = __builtin_amdgcn_raw_buffer_load_b128(make_rsrc(p_x, NB * DM * 2), l16, w4 * DM * 2, 0);
    const unsigned epoch = __builtin_amdgcn_raw_buffer_load_b32(rws, 0, WS_EPOCH, AUX_SC1);
    SCHED_FENCE();
    {
      float v[8], t = 0.f;
#pragma unroll
      for (int k = 0; k < 4; ++k) { v[2 * k] = bf_lo(xr[k]); v[2 * k + 1] = bf_hi(xr[k]); t += v[2 * k] + v[2 * k + 1]; }
      const float mean = wave_sum(lane < 56 ? t : 0.f) * (1.0f / DM);
      t = 0.f;
#pragma unroll
      for (int k = 0; k < 8; ++k) { const float d = v[k] - mean; t = fmaf(d, d, t); }
      const float var = round_bf16(wave_sum(lane < 56 ? t : 0.f) * (1.0f / DM)), mb = round_bf16(mean);
      const float rc = __builtin_amdgcn_rcpf(round_bf16(__builtin_amdgcn_sqrtf(round_bf16(var + 1e-5f))));
      u32x4 xn;
#pragma unroll
      for (int k = 0; k < 4; ++k) {
        const unsigned d = pack_bf16(v[2 * k] - mb, v[2 * k + 1] - mb);
        xn[k] = pack_bf16(bf_lo(d) * rc, bf_hi(d) * rc);
      }
      lds_st<u32x4>(lds, L_XN + w4 * 1024 + lane * 16, xn);
      lds_flag(lds, F_XN + w4 * 4, 1u);
      lds_wait_all(lds, F_XN);
      tag = next_tag(epoch);
      PIN_S(tag);
      const int bt = lane & 3, g = lane >> 4, jj = (lane >> 2) & 3;
      const int xo = L_XN + bt * 1024 + (half * 224 + w4 * 32 + 4 * g) * 2;
      const u32x2 a0 = lds_ld<u32x2>(lds, xo), a1 = lds_ld<u32x2>(lds, xo + 32);
      const u32x2 a2 = lds_ld<u32x2>(lds, xo + 256), a3 = lds_ld<u32x2>(lds, xo + 288);
      const u32x4 act0 = {a0[0], a0[1], a1[0], a1[1]}, act1 = {a2[0], a2[1], a3[0], a3[1]}, zero = {0u, 0u, 0u, 0u};
      VM_DRAIN();
      bf16x8 wf0[4], wf1[4];
      tile_frags(img, lane, wf0);
      f32x4 acc = {0.f, 0.f, 0.f, 0.f};
      if (w4 < 3) {
        tile_frags(img + 4 * 4096, lane, wf1);
#pragma unroll
        for (int t = 0; t < 4; ++t) acc = mma(wf1[t], asb(jj == t ? act1 : zero), acc);
      }
#pragma unroll
      for (int t = 0; t < 4; ++t) acc = mma(wf0[t], asb(jj == t ? act0 : zero), acc);
      lds_st<f32x4>(img, bt * 256 + ((lane & 12) + g) * 16, acc);
      lds_flag(lds, F_EP + w4 * 4, 1u);
      if (w4 == 3 && has_early) {
        const unsigned voff = is_q ? WS_QP + (etile >> 3) * 16384 + (lane >> 4) * 4096 + (etile & 6) * 512 + half * 512 + (etile & 1) * 256 + (lane & 15) * 16
                                   : WS_GP + etile * 4096 + (is_gate ? 0 : 2048) + half * 1024 + l16;
        lds_wait_all(lds, F_EP);
        f32x4 sum = lds_ld<f32x4>(lds, L_TILE + lane * 16);
#pragma unroll
        for (int p = 1; p < 4; ++p) sum += lds_ld<f32x4>(lds, L_TILE + p * 4096 + lane * 16);
        st_sc1(tag4_block(sum, tag), rws, voff, 0);
      }
    }
    const rsrc_t rv = make_rsrc(p_v + KV_OFF, 32768);
    const unsigned hf = lane >> 5;
    unsigned vo0 = (lane ^ hf) * 16, vo1 = (lane ^ (2 + hf)) * 16, vo2 = (lane ^ (4 + hf)) * 16, vo3 = (lane ^ (6 + hf)) * 16;
    const int lcol = late_ch * 4 + w4;
    const bool has_tile = late && lcol < 7;
    const rsrc_t rl = make_rsrc(is_wo ? p_wo : p_wd, (is_wo ? QW : FF) * DM * 2);
    unsigned lvo = r8 * (DM * 2) + (((lane & 7) ^ (r8 >> 1)) << 4), lso = late_rg * (64 * DM * 2) + lcol * 128;
    unsigned rvo = (((lane >> 3) & 1) * 4 + (lane >> 4)) * RP_BOX + (lane & 7) * 16;
    unsigned rso = WS_RP + lcol * 8 * RP_BOX + ((is_wo ? 0 : 64) + late_rg) * 128;
    LAS char *aa[4], *ta[4];
#pragma unroll
    for (int t = 0; t < 4; ++t) {
      aa[t] = lds + ((lane & 3) == t ? L_A + ((lane >> 2) & 3) * 64 + (lane >> 4) * 16 : L_ZERO);
      ta[t] = img + (lane >> 2) * 128 + (((lane & 2) * 2 + (t ^ ((lane >> 3) & 3))) << 4) + 8 * (lane & 1);
      PIN_V(aa[t]); PIN_V(ta[t]);
    }
    PIN_V(vo0); PIN_V(vo1); PIN_V(vo2); PIN_V(vo3); PIN_V(lvo); PIN_V(rvo); PIN_S(lso); PIN_S(rso);
    WG_BARRIER();
    _Pragma("nounroll") for (int g = 0; g < 8; ++g) {
      const unsigned so = ((SEQ >> (4 * g)) & 7) << 12;
      PACE((8 + g) * T_STEP);
      LAS void* dst = (LAS void*)(lds + L_V + (g * 4 + w4) * 4096);
      __builtin_amdgcn_raw_ptr_buffer_load_lds(rv, dst, 16, vo0, so, 0, AUX_NT);
      __builtin_amdgcn_raw_ptr_buffer_load_lds(rv, dst, 16, vo1, so, 1024, AUX_NT);
      __builtin_amdgcn_raw_ptr_buffer_load_lds(rv, dst, 16, vo2, so, 2048, AUX_NT);
      __builtin_amdgcn_raw_ptr_buffer_load_lds(rv, dst, 16, vo3, so, 3072, AUX_NT);
      VM_WAIT(8);
      lds_flag(lds, L_PROG + w4 * 4, g + 1);
    }
    VM_WAIT(4);
    lds_flag(lds, L_PROG + w4 * 4, 9);
    const bool before = has_tile && is_wo;
    if (!before) { VM_WAIT(0); WG_BARRIER_BARE(); }
    if (has_tile) {
      LSPIN() { const u32x4 f = lds_peek(lds, F_PV); if (wave_none((f[0] & f[1]) == 0u)) break; }
      PACE(15 * T_STEP + (is_wo ? T_WO : T_DOWN));
      tile_fetch(rl, lvo, lso, 8 * DM * 2 - 1024, img);
      tile_fetch(rl, lvo, lso + 32 * DM * 2, 8 * DM * 2 - 1024, img + 4 * 4096);
      if (before) {
        VM_WAIT(8);
        WG_BARRIER_BARE();
        VM_WAIT(4);
        lds_flag(lds, F_T + w4 * 4, 1u);
      }
      VM_DRAIN();
      bf16x8 wf[8];
#pragma unroll
      for (int t = 0; t < 4; ++t) { wf[t] = tr_frag<2048>(ta[t]); wf[4 + t] = tr_frag<2048>(ta[t] + 4 * 4096); }
      if (is_wo) WG_BARRIER();
      u32x4 act[8];
#pragma unroll
      for (int t = 0; t < 4; ++t) { act[2 * t] = lds_ld<u32x4>(aa[t], 0); act[2 * t + 1] = lds_ld<u32x4>(aa[t], 256); }
      SCHED_FENCE();
      f32x4 acc = {0.f, 0.f, 0.f, 0.f};
#pragma unroll
      for (int t = 0; t < 4; ++t) { acc = mma(asb(act[2 * t]), wf[t], acc); acc = mma(asb(act[2 * t + 1]), wf[4 + t], acc); }
      st_sc1(tag4(acc, tag), rws, rvo, rso);
    } else if (is_wo) {
      WG_BARRIER_BARE();
    }
  }
  int role = wave;
  PIN_S(role);
  if (role < 4) {
    u32x4 kr[32], qv;
#define qso (WS_QP + hk * 16384 + (idx >> 3) * 4096 + w4 * 1024)
    const unsigned kvo = (lane & 7) * 512 + ((lane >> 3) & 1) * 64 + (lane >> 4) * 16, seq = SEQ;
    const rsrc_t rk = make_rsrc(p_k + KV_OFF, 32768);
    const unsigned t_go = t_k + T_OFF;
#pragma unroll
    for (int g = 0; g < 8; ++g) {
      const unsigned so = ((seq >> (4 * g)) & 7) << 12;
      PACE(g * T_STEP);
#pragma unroll
      for (int i = 0; i < 4; ++i) kr[g * 4 + i] = __builtin_amdgcn_raw_buffer_load_b128(rk, kvo + i * 128, so, AUX_NT);
    }
    SCHED_FENCE();
    const unsigned kepoch = __builtin_amdgcn_raw_buffer_load_b32(rws, 0, WS_EPOCH, AUX_SC1);
    qv = ld_sc1(rws, l16, qso);
    WG_BARRIER_BARE();
    tag = next_tag(kepoch);
    asm volatile("" : "+s"(tag) : "v"(cpf));
    SPIN(SPIN_MAX) {
      if (wave_none((or4(qv ^ tag_bytes(tag)) & 255u) != 0u)) break;
      MEM_FENCE();
      qv = ld_sc1(rws, l16, qso);
    }
    const f32x4 qp = block_values(qv);
    lds_st<unsigned>(lds, L_Q + w4 * 256 + (lane & 31) * 8 + (lane >> 5) * 4, pack_bf16(fold32(qp[0], qp[2]), fold32(qp[1], qp[3])));
    lds_flag(lds, F_Q + w4 * 4, 1u);
    lds_wait_all(lds, F_Q);
    float m0, m1;
    {
      const int qa = L_Q + (lane & 1) * 512 + ((lane >> 1) & 1) * 64 + (lane >> 4) * 16;
      f32x4 c0, c1;
      {
        u32x4 qf[4];
#pragma unroll
        for (int s = 0; s < 4; ++s) qf[s] = lds_ld<u32x4>(lds, qa + s * 128);
        f32x4 c[8];
#pragma unroll
        for (int t = 0; t < 8; ++t) c[t] = mma(asb(qf[0]), asb(kr[t * 4]), f32x4{0.f, 0.f, 0.f, 0.f});
#pragma unroll
        for (int s = 1; s < 4; ++s)
#pragma unroll
          for (int t = 0; t < 8; ++t) c[t] = mma(asb(qf[s]), asb(kr[t * 4 + s]), c[t]);
        const bool g1 = lane & 16, g2 = lane & 32;
        c0 = g2 ? (g1 ? c[3] : c[2]) : (g1 ? c[1] : c[0]);
        c1 = g2 ? (g1 ? c[7] : c[6]) : (g1 ? c[5] : c[4]);
      }
      const bool cb = lane & 8;
      const float r0 = (cb ? c1[2] : c0[0]) + dpp<XOR8>(cb ? c0[2] : c1[0]);
      const float r1 = (cb ? c1[3] : c0[1]) + dpp<XOR8>(cb ? c0[3] : c1[1]);
      const unsigned sp = pack_bf16(r0, r1);
      const float s0 = bf_lo(sp) * 0.0625f, s1 = bf_hi(sp) * 0.0625f;
      float mw = max32(s0, s1);
      mw = row_max(max16(mw, mw));
      *(volatile LAS unsigned*)(lds + L_MX + (lane >> 5) * 16 + w4 * 4) = asu(mw) & 0xfffffffeu;
      u32x4 x0, x1;
      LSPIN() { x0 = lds_peek(lds, L_MX); x1 = lds_peek(lds, L_MX + 16); if (wave_none(unset4(x0) | unset4(x1))) break; }
      m0 = fmaxf(fmaxf(asf(x0[0]), asf(x0[1])), fmaxf(asf(x0[2]), asf(x0[3])));
      m1 = fmaxf(fmaxf(asf(x1[0]), asf(x1[1])), fmaxf(asf(x1[2]), asf(x1[3])));
      const float p0 = __builtin_amdgcn_exp2f((s0 - m0) * LOG2E), p1 = __builtin_amdgcn_exp2f((s1 - m1) * LOG2E);
      const unsigned pk = pack_bf16(p0, p1);
      const int po = L_P + (((lane >> 3) & 1) * 4 + (lane >> 4)) * 64 + ((w4 & 1) * 2 + ((lane >> 2) & 1)) * 16 + ((w4 >> 1) * 4 + (lane & 3)) * 2;
      lds_st<u16>(lds, po, (u16)pk);
      lds_st<u16>(lds, po + 512, (u16)(pk >> 16));
      float l = fold32(p0, p1);
      l = row_sum(fold16(l, l));
      MEM_FENCE();
      *(volatile LAS unsigned*)(lds + L_LS + (lane >> 5) * 16 + w4 * 4) = asu(l) & 0xfffffffeu;
    }
    u32x4 ls;
    LSPIN() { ls = lds_peek(lds, L_LS + (lane >> 5) * 16); if (wave_none(unset4(ls))) break; }
    MEM_FENCE();
    u32x4 tb = tag_bytes(tag);
    unsigned oso = WS_OP + wo_rg * 8192 + w4 * 2048;
    LAS char* ad = lds + L_A + (lane & 4) * 64 + w4 * 64 + ((lane & 1) * 2 + ((lane >> 3) & 1)) * 16 + ((lane & 2) * 2 + (lane >> 4)) * 2;
    LAS char* fa = lds + L_F + w4 * 32 + (lane >> 3) * 4;
    PIN_S(tb); PIN_S(oso); PIN_V(ad); PIN_V(fa);
    if (w4 < 2) {
      const int g = lane >> 4, row = lane >> 2, p = lane & 3, tl = lane & 7;
      LAS char* va[8];
#pragma unroll
      for (int t = 0; t < 8; ++t) va[t] = lds + L_V + row * 512 + ((w4 * 16 + (p >> 1) * 8 + (t ^ (row & 7))) << 4) + 8 * (p & 1);
      LAS char* pa = lds + L_P + ((lane >> 3) & 1) * 512 + g * 16;
      unsigned ovo = (((lane >> 3) & 1) * 4 + (g >> 1)) * 8192 + (g & 1) * 1024 + tl * 16;
      unsigned pso = WS_OP + (hk * 8 + w4 * 2) * 8192 + (idx >> 3) * 2048 + (idx & 7) * 128;
      PIN_V(ovo); PIN_S(pso);
      f32x4 o = {0.f, 0.f, 0.f, 0.f};
      u32x4 pm[8];
      {
        const u32x4 pl = lds_ld<u32x4>(pa, 0);
#pragma unroll
        for (int t = 0; t < 8; ++t) pm[t] = tl == t ? pl : u32x4{0u, 0u, 0u, 0u};
      }
      _Pragma("nounroll") for (int ks = 0; ks < 7; ++ks) {
        LSPIN() {
          const u32x4 pr = lds_peek(lds, L_PROG);
          if (wave_none(min(min(pr[0], pr[1]), min(pr[2], pr[3])) <= (unsigned)(ks + 2))) break;
        }
        lds_flag(lds, F_PV + w4 * 4, ks >= 2 ? 1u : 0u);
        pa += 64;
        const u32x4 pl = lds_ld<u32x4>(pa, 0);
        bf16x8 vf[8];
#pragma unroll
        for (int t = 0; t < 8; ++t) vf[t] = tr_frag<8192>(va[t]);
        SCHED_FENCE();
#pragma unroll
        for (int t = 0; t < 8; ++t) {
          o = mma(vf[t], asb(pm[t]), o);
          SCHED_FENCE();
          pm[t] = tl == t ? pl : u32x4{0u, 0u, 0u, 0u};
          va[t] += 16384;
          PIN_V(pm[t]); PIN_V(va[t]);
          SCHED_FENCE();
        }
      }
      WG_BARRIER_BARE();
      {
        bf16x8 vf[8];
#pragma unroll
        for (int t = 0; t < 8; ++t) vf[t] = tr_frag<8192>(va[t]);
        SCHED_FENCE();
#pragma unroll
        for (int t = 0; t < 8; ++t) o = mma(vf[t], asb(pm[t]), o);
      }
      st_sc1(tag4(o, tag), rws, ovo, pso);
    } else if (w4 == 2) {
      if (late && !is_wo) {
        u32x4 gv[4];
        SPIN(SPIN_MAX) {
          unsigned bad = 0;
#pragma unroll
          for (int u = 0; u < 4; ++u) gv[u] = ld_sc1(rws, l16 + u * 1024, WS_GP + (dn_un >> 1) * 4096);
#pragma unroll
          for (int u = 0; u < 4; ++u) bad |= or4(gv[u] ^ tb);
          if (wave_none((bad & 255u) != 0u)) break;
          MEM_FENCE();
        }
        const f32x4 gs = block_values(gv[0]) + block_values(gv[1]), us = block_values(gv[2]) + block_values(gv[3]);
        u32x2 hv;
#pragma unroll
        for (int e = 0; e < 2; ++e) {
          const unsigned gt = pack_bf16(gs[2 * e], gs[2 * e + 1]), up = pack_bf16(us[2 * e], us[2 * e + 1]);
          const float g0 = bf_lo(gt), g1 = bf_hi(gt);
          hv[e] = pack_bf16(g0 * (g0 > 0.f ? 1.f : 0.f) * bf_lo(up), g1 * (g1 > 0.f ? 1.f : 0.f) * bf_hi(up));
        }
        lds_st<u32x2>(lds, L_A + (lane & 8) * 32 + (lane >> 4) * 64 + (lane & 3) * 16 + (lane & 4) * 2, hv);
      } else if (is_red) {
        xv = __builtin_amdgcn_raw_buffer_load_b16(make_rsrc(p_x, NB * DM * 2), oi * 2, 0, 0);
        PIN_V(xv);
      }
      WG_BARRIER();
    } else {
      const float lsum = (asf(ls[0]) + asf(ls[1])) + (asf(ls[2]) + asf(ls[3]));
      const float l1 = asf(__builtin_amdgcn_readlane(asu(lsum), 32));
      if (lane == 0) st_sc1(tag4(f32x4{m0, lsum, m1, l1}, tag), rws, 0, WS_ML + (hk * 32 + idx) * 16);
      const unsigned mso = is_wo ? WS_ML + (wo_rg >> 3) * 512 : WS_END;
      LSPIN() { if (wave_none(*(volatile LAS unsigned*)(lds + L_PROG) < 7u)) break; __builtin_amdgcn_s_sleep(4); }
      u32x4 mv = ld_sc1(rws, (lane & 31) * 16, mso);
      WG_BARRIER_BARE();
      mv ^= tb;
      PIN_V(mv);
      if (is_wo) {
        SPIN(SPIN_MAX) {
          if (wave_none((or4(mv) & 255u) != 0u)) break;
          MEM_FENCE();
          mv = ld_sc1(rws, (lane & 31) * 16, mso) ^ tb;
        }
        const float m = asf((idx & 8) ? mv[2] : mv[0]);
        float mx = fmaxf(m, dpp<XOR7>(m));
        mx = fmaxf(mx, dpp<XOR2>(mx));
        mx = fmaxf(mx, dpp<XOR1>(mx));
        const float e = __builtin_amdgcn_exp2f((m - mx) * LOG2E);
        float lt = e * asf((idx & 8) ? mv[3] : mv[1]);
        lt += dpp<XOR7>(lt);
        lt += dpp<XOR2>(lt);
        lt += dpp<XOR1>(lt);
        *(volatile LAS unsigned*)(lds + L_F + (lane & 31) * 4) = asu(e * __builtin_amdgcn_rcpf(lt)) & 0xfffffffeu;
      } else if (bid == 56) {
        SPIN(SPIN_MAX) {
          unsigned bad = 0;
#pragma unroll
          for (int u = 0; u < 4; ++u) bad |= or4(ld_sc1(rws, l16 + u * 1024, WS_ML) ^ tb);
          if (wave_none((bad & 255u) != 0u)) break;
          MEM_FENCE();
        }
        if (lane == 0) __builtin_amdgcn_raw_buffer_store_b32(tag, rws, 0, WS_EPOCH, AUX_SC1);
      }
    }
    if (is_wo) {
      float f;
      LSPIN() {
        f = *(volatile LAS float*)fa;
        if (wave_none((asu(f) == 0xffffffffu) | (*(volatile LAS unsigned*)(lds + F_T) == 0u))) break;
      }
      u32x4 o0, o1;
      MEM_FENCE();
      SPIN(SPIN_MAX) {
        o0 = ld_sc1(rws, l16, oso) ^ tb;
        o1 = ld_sc1(rws, l16 + 1024, oso) ^ tb;
        if (wave_none(((or4(o0) | or4(o1)) & 255u) != 0u)) break;
        MEM_FENCE();
      }
      const float o = fold_pair(asf4(o0) * f, asf4(o1) * f);
      lds_st<u16>(ad, 0, (u16)pack_bf16(o, o));
      WG_BARRIER();
    }
  }

  if (is_red) {
    const u32x4 tb = tag_bytes(tag);
    LAS char* fw = lds + L_FIN + ((lane >> 4) * 8 + (lane & 7)) * 64 + wave * 4 + (lane & 8) * 4;
    LAS char* fr = lds + L_FIN + (lane & 31) * 64 + (lane >> 5) * 32;
    unsigned oo = (lane & 32) ? 0x7fffffffu : oi * 2;
    const bool dn_lane = wave * 64 + lane < 28 * 8;
    unsigned dvo = dn_lane ? l16 + 8192 : 0x7fffffffu;
    u32x4 td = dn_lane ? tb : u32x4{0u, 0u, 0u, 0u};
    float xf = bf_lo(xv);
    PIN_V(fw); PIN_V(fr); PIN_V(oo); PIN_V(xf); PIN_V(dvo); PIN_V(td);
    const rsrc_t ro = make_rsrc(p_out, NB * DM * 2);
    const unsigned so = WS_RP + bid * RP_BOX + wave * 1024;
    u32x4 a0, a1;
    SPIN(SPIN_MAX) {
      a1 = ld_sc1(rws, dvo, so) ^ td;
      a0 = ld_sc1(rws, l16, so) ^ tb;
      if (wave_none(((or4(a0) | or4(a1)) & 255u) != 0u)) break;
      MEM_FENCE();
    }
    lds_st<float>(fw, 0, fold_pair(asf4(a0), asf4(a1)));
    WG_BARRIER();
    if (wave == 2) {
      const f32x4 t = lds_ld<f32x4>(fr, 0) + lds_ld<f32x4>(fr, 16);
      const f32x2 t2 = f32x2{t[0], t[1]} + f32x2{t[2], t[3]};
      const float u = round_bf16(t2[0] + t2[1]);
      auto sw = __builtin_amdgcn_permlane32_swap(asu(u), asu(u), false, false);
      const float sv = asf(sw[0]), dn = asf(sw[1]);
      const float g = round_bf16(dn * __builtin_amdgcn_rcpf(1.0f + __builtin_amdgcn_exp2f(-dn * LOG2E)));
      const float res = xf + round_bf16(sv * g);
      __builtin_amdgcn_raw_buffer_store_b16((u16)pack_bf16(res, res), ro, oo, 0, 0);
    }
  }
}

}

extern "C" void kernel_launch(void* const* d_in, const int* in_sizes, int n_in, void* d_out, int out_size, void* d_ws,
                              size_t ws_size, hipStream_t stream) {
  fused_decode<<<256, 512, 0, stream>>>((const u16*)d_in[0], (const u16*)d_in[1], (const u16*)d_in[2], (const u16*)d_in[3],
                                        (const u16*)d_in[4], (const u16*)d_in[6], (const u16*)d_in[7], (const u16*)d_in[8],
                                        (u16*)d_out, (char*)d_ws);
}
```

```cpp
#include <hip/hip_runtime.h>
#include <hip/hip_bf16.h>
#include <stdint.h>

namespace {

typedef unsigned short u16;
typedef unsigned long long u64;
typedef unsigned u32x2 __attribute__((ext_vector_type(2)));
typedef unsigned u32x4 __attribute__((ext_vector_type(4)));
typedef float f32x2 __attribute__((ext_vector_type(2)));
typedef float f32x4 __attribute__((ext_vector_type(4)));
typedef short s16x4 __attribute__((ext_vector_type(4)));
typedef __bf16 bf16x2 __attribute__((ext_vector_type(2)));
typedef __bf16 bf16x8 __attribute__((ext_vector_type(8)));
#define LAS __attribute__((address_space(3)))
typedef __amdgpu_buffer_rsrc_t rsrc_t;

constexpr int NB = 4, DM = 448, SK = 8192, WIN = 2048, FF = 1792, QW = 4096;
constexpr int AUX_SC1 = 16, AUX_NT = 2;
constexpr unsigned SPIN_MAX = 1u << 22;
constexpr unsigned CODE_BYTES = 10640;
constexpr int T_FIRST = 50;
constexpr int T_STAGGER = 14;
constexpr int T_STEP = 56;
constexpr int T_WO = 60, T_DOWN = 70;
constexpr float LOG2E = 1.4426950408889634f;

constexpr unsigned WS_EPOCH = 0;
constexpr unsigned WS_QP = 4096;
constexpr unsigned WS_GP = WS_QP + 8 * 4 * 4096;
constexpr unsigned WS_OP = WS_GP + 28 * 4096;
constexpr unsigned WS_ML = WS_OP + 64 * 8192;
constexpr unsigned WS_RP = WS_ML + 4096;
constexpr unsigned RP_BOX = 92 * 128;
constexpr unsigned WS_END = WS_RP + 56 * RP_BOX;

constexpr int L_V = 0;
constexpr int L_TILE = L_V;
constexpr int L_XN = L_V + 32768;
constexpr int L_FIN = L_V;
constexpr int L_Q = 131072;
constexpr int L_P = L_Q + 1024;
constexpr int L_A = L_P + 1024;
constexpr int L_SYNC = L_A + 512;
constexpr int L_ZERO = L_SYNC;
constexpr int F_XN = L_SYNC + 16, F_EP = L_SYNC + 32, F_Q = L_SYNC + 48, L_PROG = L_SYNC + 64, F_PV = L_SYNC + 80, F_T = L_SYNC + 96;
constexpr int L_F = L_SYNC + 128;
constexpr int L_MX = L_SYNC + 384;
constexpr int L_LS = L_MX + 32;
constexpr int L_END = L_SYNC + 512;

#define SCHED_FENCE() __builtin_amdgcn_sched_barrier(0)
#define MEM_FENCE() asm volatile("" ::: "memory")
#define WG_BARRIER() do { SCHED_FENCE(); asm volatile("s_waitcnt lgkmcnt(0)\n\ts_barrier" ::: "memory"); SCHED_FENCE(); } while (0)
#define WG_BARRIER_BARE() do { SCHED_FENCE(); asm volatile("s_barrier" ::: "memory"); SCHED_FENCE(); } while (0)
#define VM_WAIT(n) do { SCHED_FENCE(); asm volatile("s_waitcnt vmcnt(%0)" :: "i"(n) : "memory"); SCHED_FENCE(); } while (0)
#define VM_DRAIN() do { SCHED_FENCE(); __builtin_amdgcn_s_waitcnt(0x0F70); SCHED_FENCE(); MEM_FENCE(); } while (0)
#define NAP() __builtin_amdgcn_s_sleep(1)
#define PACE(t) do { SCHED_FENCE(); pace_until(t_go + (t)); SCHED_FENCE(); } while (0)
#define PIN_V(x) asm volatile("" : "+v"(x))
#define PIN_S(x) asm volatile("" : "+s"(x))
#define LSPIN() _Pragma("nounroll") for (;;)
#define SPIN(limit) _Pragma("nounroll") for (unsigned s_ = 0; s_ < (limit); ++s_)

__device__ __forceinline__ void pace_until(unsigned when) {
  asm volatile("1:\n\ts_memrealtime vcc\n\ts_waitcnt lgkmcnt(0)\n\ts_sub_i32 vcc_lo, vcc_lo, %0\n\ts_cmp_lt_i32 vcc_lo, 0\n\ts_cbranch_scc1 1b"
               :: "s"(when) : "vcc", "scc", "memory");
}
__device__ __forceinline__ float asf(unsigned u) { return __builtin_bit_cast(float, u); }
__device__ __forceinline__ unsigned asu(float f) { return __builtin_bit_cast(unsigned, f); }
__device__ __forceinline__ unsigned pack_bf16(float lo, float hi) {
  f32x2 f = {lo, hi};
  return __builtin_bit_cast(unsigned, __builtin_convertvector(f, bf16x2));
}
__device__ __forceinline__ float round_bf16(float v) { return asf(pack_bf16(0.f, v)); }
__device__ __forceinline__ float bf_lo(unsigned u) { return asf(u << 16); }
__device__ __forceinline__ float bf_hi(unsigned u) { return asf(u & 0xffff0000u); }
template <int CTRL> __device__ __forceinline__ float dpp(float x) {
  return asf((unsigned)__builtin_amdgcn_mov_dpp((int)asu(x), CTRL, 0xf, 0xf, true));
}
constexpr int XOR1 = 0xB1, XOR2 = 0x4E, XOR7 = 0x141, XOR8 = 0x128;
__device__ __forceinline__ float fold16(float a, float b) {
  auto s = __builtin_amdgcn_permlane16_swap(asu(a), asu(b), false, false);
  return asf(s[0]) + asf(s[1]);
}
__device__ __forceinline__ float fold32(float a, float b) {
  auto s = __builtin_amdgcn_permlane32_swap(asu(a), asu(b), false, false);
  return asf(s[0]) + asf(s[1]);
}
__device__ __forceinline__ float max16(float a, float b) {
  auto s = __builtin_amdgcn_permlane16_swap(asu(a), asu(b), false, false);
  return fmaxf(asf(s[0]), asf(s[1]));
}
__device__ __forceinline__ float max32(float a, float b) {
  auto s = __builtin_amdgcn_permlane32_swap(asu(a), asu(b), false, false);
  return fmaxf(asf(s[0]), asf(s[1]));
}
__device__ __forceinline__ float row_sum(float v) {
  v += dpp<XOR8>(v); v += dpp<XOR7>(v); v += dpp<XOR2>(v); v += dpp<XOR1>(v);
  return v;
}
__device__ __forceinline__ float wave_sum(float v) { v = fold32(v, v); return row_sum(fold16(v, v)); }
__device__ __forceinline__ float row_max(float v) {
  v = fmaxf(v, dpp<XOR8>(v)); v = fmaxf(v, dpp<XOR7>(v)); v = fmaxf(v, dpp<XOR2>(v)); v = fmaxf(v, dpp<XOR1>(v));
  return v;
}
__device__ __forceinline__ float fold_pair(f32x4 a, f32x4 b) {
  const float a0 = fold32(a[0], a[2]), a1 = fold32(a[1], a[3]), b0 = fold32(b[0], b[2]), b1 = fold32(b[1], b[3]);
  SCHED_FENCE();
  const float za = fold16(a0, a1), zb = fold16(b0, b1);
  const bool hi = __builtin_amdgcn_inverse_ballot_w64(0xff00ff00ff00ff00ull);
  return (hi ? zb : za) + dpp<XOR8>(hi ? za : zb);
}
__device__ __forceinline__ rsrc_t make_rsrc(u64 p, unsigned bytes) {
  return __builtin_amdgcn_make_buffer_rsrc((void*)p, 0, bytes, 0x00020000);
}
__device__ __forceinline__ u32x4 tag4(f32x4 v, unsigned tag) {
  u32x4 r;
  r[0] = __builtin_amdgcn_perm(asu(v[0]), tag, 0x07060500u);
  r[1] = __builtin_amdgcn_perm(asu(v[1]), tag, 0x07060501u);
  r[2] = __builtin_amdgcn_perm(asu(v[2]), tag, 0x07060502u);
  r[3] = __builtin_amdgcn_perm(asu(v[3]), tag, 0x07060503u);
  return r;
}
__device__ __forceinline__ unsigned next_tag(unsigned epoch) {
  const unsigned e = __builtin_amdgcn_readfirstlane(epoch);
  return ((e & 0x7f7f7f7fu) + 0x07050301u) ^ (e & 0x80808080u);
}
__device__ __forceinline__ u32x4 tag_bytes(unsigned tag) { return u32x4{tag & 255u, (tag >> 8) & 255u, (tag >> 16) & 255u, tag >> 24}; }
__device__ __forceinline__ u32x4 tag_nibbles(unsigned tag) { return u32x4{tag & 15u, (tag >> 8) & 15u, (tag >> 16) & 15u, (tag >> 24) & 15u}; }
__device__ __forceinline__ u32x4 tag4_fine(f32x4 v, unsigned tag) {
  return ((__builtin_bit_cast(u32x4, v) + 8u) & 0xfffffff0u) | tag_nibbles(tag);
}
__device__ __forceinline__ unsigned or4(u32x4 v) { return v[0] | v[1] | v[2] | v[3]; }
__device__ __forceinline__ f32x4 asf4(u32x4 v) { return __builtin_bit_cast(f32x4, v); }
__device__ __forceinline__ u32x4 ld_sc1(rsrc_t r, unsigned voff, unsigned soff) {
  return __builtin_amdgcn_raw_buffer_load_b128(r, voff, soff, AUX_SC1);
}
__device__ __forceinline__ void st_sc1(u32x4 v, rsrc_t r, unsigned voff, unsigned soff) {
  __builtin_amdgcn_raw_buffer_store_b128(v, r, voff, soff, AUX_SC1);
}
__device__ __forceinline__ bool wave_none(bool bad) { return __builtin_amdgcn_ballot_w64(bad) == 0ull; }

template <typename T> __device__ __forceinline__ T lds_ld(LAS char* l, int off) { return *(LAS T*)(l + off); }
template <typename T> __device__ __forceinline__ void lds_st(LAS char* l, int off, T v) { *(LAS T*)(l + off) = v; }
__device__ __forceinline__ void lds_flag(LAS char* l, int off, unsigned v) {
  MEM_FENCE();
  *(volatile LAS unsigned*)(l + off) = v;
  MEM_FENCE();
}
__device__ __forceinline__ u32x4 lds_peek(LAS char* l, int off) { return *(volatile LAS u32x4*)(l + off); }
__device__ __forceinline__ void lds_wait_all(LAS char* l, int off) {
  LSPIN() { const u32x4 f = lds_peek(l, off); if (wave_none((f[0] & f[1] & f[2] & f[3]) == 0u)) break; NAP(); }
  MEM_FENCE();
}
__device__ __forceinline__ bool unset4(u32x4 v) { return max(max(v[0], v[1]), max(v[2], v[3])) == 0xffffffffu; }

__device__ __forceinline__ void tile_fetch(rsrc_t r, unsigned voff, unsigned soff, unsigned step, LAS char* img) {
  __builtin_amdgcn_raw_ptr_buffer_load_lds(r, (LAS void*)img, 16, voff, soff, 0, AUX_NT);
  __builtin_amdgcn_raw_ptr_buffer_load_lds(r, (LAS void*)img, 16, voff, soff + step, 1024, AUX_NT);
  __builtin_amdgcn_raw_ptr_buffer_load_lds(r, (LAS void*)img, 16, voff, soff + 2 * step, 2048, AUX_NT);
  __builtin_amdgcn_raw_ptr_buffer_load_lds(r, (LAS void*)img, 16, voff, soff + 3 * step, 3072, AUX_NT);
}
template <int GAP> __device__ __forceinline__ bf16x8 tr_frag(LAS char* a) {
  const s16x4 lo = __builtin_amdgcn_ds_read_tr16_b64_v4i16((LAS s16x4*)a);
  const s16x4 hi = __builtin_amdgcn_ds_read_tr16_b64_v4i16((LAS s16x4*)(a + GAP));
  return __builtin_bit_cast(bf16x8, __builtin_shufflevector(lo, hi, 0, 1, 2, 3, 4, 5, 6, 7));
}
__device__ __forceinline__ void tile_frags(LAS char* img, int lane, bf16x8 (&w)[4]) {
  const int row = lane >> 2, p = lane & 3;
#pragma unroll
  for (int t = 0; t < 4; ++t) w[t] = tr_frag<2048>(img + row * 128 + (((2 * t + (p >> 1)) ^ (row & 6)) << 4) + 8 * (p & 1));
}
__device__ __forceinline__ f32x4 mma(bf16x8 a, bf16x8 b, f32x4 c) { return __builtin_amdgcn_mfma_f32_16x16x32_bf16(a, b, c, 0, 0, 0); }
__device__ __forceinline__ bf16x8 asb(u32x4 v) { return __builtin_bit_cast(bf16x8, v); }

__global__ void __launch_bounds__(512, 2)
fused_decode(const u16* x, const u16* kc, const u16* vc, const u16* wq, const u16* wo, const u16* wg, const u16* wu,
             const u16* wd, u16* out, char* ws) {
  __shared__ __attribute__((aligned(16))) char lds_raw[L_END];
  LAS char* lds = (LAS char*)lds_raw;
  const int bid = blockIdx.x, lane = threadIdx.x & 63, wave = __builtin_amdgcn_readfirstlane(threadIdx.x >> 6);
  const int hk = bid & 7, idx = bid >> 3, w4 = wave & 3;
  const bool loader = wave >= 4;
  const u64 p_x = (u64)x, p_k = (u64)kc, p_v = (u64)vc, p_wq = (u64)wq, p_wo = (u64)wo, p_wg = (u64)wg, p_wu = (u64)wu, p_wd = (u64)wd;
  const u64 p_out = (u64)out, p_ws = (u64)ws;
#define ARGS_IN "s"(p_x), "s"(p_k), "s"(p_v), "s"(p_wq), "s"(p_wo), "s"(p_wg), "s"(p_wu), "s"(p_wd), "s"(p_out), "s"(p_ws)
#define KV_OFF ((((idx >> 3) * 8 + hk) * SK + (SK - WIN) + (idx & 7) * 256) * 512 + w4 * 32768)
#define SEQ ((unsigned)(0x12075643120ull >> ((idx & 3) * 4)))
#define T_OFF (T_FIRST + (3 - (idx >> 3)) * T_STAGGER)
#define is_wo (idx >= 16)
#define late (idx >= 9)
#define is_red (idx < 7)
#define wo_rg (((hk + 1 + ((idx >> 1) & 3)) & 7) * 8 + ((idx >> 1) & 7))
#define dn_un (bid - 72)
#define late_rg (is_wo ? wo_rg : dn_un >> 1)
#define late_ch ((is_wo ? idx : dn_un) & 1)
#define oi ((bid & 3) * DM + (bid >> 2) * 32 + (lane & 31))
#define rws make_rsrc(p_ws, WS_END)
  const unsigned l16 = lane * 16;
  unsigned tag = 0, xv = 0, cpf = 0, t_k = 0;

  if (!loader) {
    const unsigned iv = (lane & 16) ? 0xffffffffu : 0u;
    lds_st<u32x2>(lds, L_SYNC + lane * 8, u32x2{iv, iv});
    cpf = __builtin_amdgcn_raw_buffer_load_b32(make_rsrc(__builtin_amdgcn_s_getpc() & ~127ull, CODE_BYTES), 0, (idx * 4 + w4) * 128, 0);
    asm volatile("" :: ARGS_IN);
    WG_BARRIER();
    t_k = (unsigned)__builtin_amdgcn_s_memrealtime();
    PIN_S(t_k);
  }
  MEM_FENCE();
  if (loader) {
    const bool is_q = idx < 16, is_gate = idx < 23, has_early = idx < 30;
    const int half = hk & 1;
    int et_q = idx + 16 * ((idx + (hk >> 1)) & 3), et_f = (idx - (is_gate ? 16 : 23)) * 4 + (hk >> 1);
    asm("" : "+s"(et_q), "+s"(et_f));
    const int etile = is_q ? et_q : et_f;
    const int r8 = lane >> 3;
    LAS char* img = lds + L_TILE + w4 * 4096;
    const unsigned stride = is_q ? QW * 2 : FF * 2;
    unsigned evo = r8 * stride + (((lane & 7) ^ (r8 & 6)) << 4), eso = (half * 224 + w4 * 32) * stride + etile * 128;
    unsigned ebytes = has_early ? DM * stride : 0, eso2 = eso + 128 * stride, estep = 8 * stride - 1024;
    PIN_V(evo);
    asm volatile("" : "+s"(eso), "+s"(estep), "+s"(ebytes), "+s"(eso2));
    SCHED_FENCE();
    asm volatile("" :: ARGS_IN : "memory");
    {
      const rsrc_t rw = make_rsrc(is_q ? p_wq : (is_gate ? p_wg : p_wu), ebytes);
      tile_fetch(rw, evo, eso, estep, img);
      if (w4 < 3) tile_fetch(rw, evo, eso2, estep, img + 4 * 4096);
    }
    WG_BARRIER_BARE();
    const unsigned t_go = (unsigned)__builtin_amdgcn_s_memrealtime() + T_OFF;
    SCHED_FENCE();
    const u32x4 xr = __builtin_amdgcn_raw_buffer_load_b128(make_rsrc(p_x, NB * DM * 2), l16, w4 * DM * 2, 0);
    const unsigned epoch = __builtin_amdgcn_raw_buffer_load_b32(rws, 0, WS_EPOCH, AUX_SC1);
    SCHED_FENCE();
    {
      float v[8], t = 0.f;
#pragma unroll
      for (int k = 0; k < 4; ++k) { v[2 * k] = bf_lo(xr[k]); v[2 * k + 1] = bf_hi(xr[k]); t += v[2 * k] + v[2 * k + 1]; }
      const float mean = wave_sum(lane < 56 ? t : 0.f) * (1.0f / DM);
      t = 0.f;
#pragma unroll
      for (int k = 0; k < 8; ++k) { const float d = v[k] - mean; t = fmaf(d, d, t); }
      const float var = round_bf16(wave_sum(lane < 56 ? t : 0.f) * (1.0f / DM)), mb = round_bf16(mean);
      const float rc = __builtin_amdgcn_rcpf(round_bf16(__builtin_amdgcn_sqrtf(round_bf16(var + 1e-5f))));
      u32x4 xn;
#pragma unroll
      for (int k = 0; k < 4; ++k) {
        const unsigned d = pack_bf16(v[2 * k] - mb, v[2 * k + 1] - mb);
        xn[k] = pack_bf16(bf_lo(d) * rc, bf_hi(d) * rc);
      }
      lds_st<u32x4>(lds, L_XN + w4 * 1024 + lane * 16, xn);
      lds_flag(lds, F_XN + w4 * 4, 1u);
      lds_wait_all(lds, F_XN);
      tag = next_tag(epoch);
      PIN_S(tag);
      const int bt = lane & 3, g = lane >> 4, jj = (lane >> 2) & 3;
      const int xo = L_XN + bt * 1024 + (half * 224 + w4 * 32 + 4 * g) * 2;
      const u32x2 a0 = lds_ld<u32x2>(lds, xo), a1 = lds_ld<u32x2>(lds, xo + 32);
      const u32x2 a2 = lds_ld<u32x2>(lds, xo + 256), a3 = lds_ld<u32x2>(lds, xo + 288);
      const u32x4 act0 = {a0[0], a0[1], a1[0], a1[1]}, act1 = {a2[0], a2[1], a3[0], a3[1]}, zero = {0u, 0u, 0u, 0u};
      VM_DRAIN();
      bf16x8 wf0[4], wf1[4];
      tile_frags(img, lane, wf0);
      f32x4 acc = {0.f, 0.f, 0.f, 0.f};
      if (w4 < 3) {
        tile_frags(img + 4 * 4096, lane, wf1);
#pragma unroll
        for (int t = 0; t < 4; ++t) acc = mma(wf1[t], asb(jj == t ? act1 : zero), acc);
      }
#pragma unroll
      for (int t = 0; t < 4; ++t) acc = mma(wf0[t], asb(jj == t ? act0 : zero), acc);
      lds_st<f32x4>(img, bt * 256 + ((lane & 12) + g) * 16, acc);
      lds_flag(lds, F_EP + w4 * 4, 1u);
      if (w4 == 3 && has_early) {
        const unsigned voff = is_q ? WS_QP + (etile >> 3) * 16384 + (lane >> 4) * 4096 + (etile & 6) * 512 + half * 512 + (etile & 1) * 256 + (lane & 15) * 16
                                   : WS_GP + etile * 4096 + (is_gate ? 0 : 2048) + half * 1024 + l16;
        lds_wait_all(lds, F_EP);
        f32x4 sum = lds_ld<f32x4>(lds, L_TILE + lane * 16);
#pragma unroll
        for (int p = 1; p < 4; ++p) sum += lds_ld<f32x4>(lds, L_TILE + p * 4096 + lane * 16);
        st_sc1(tag4_fine(sum, tag), rws, voff, 0);
      }
    }
    const rsrc_t rv = make_rsrc(p_v + KV_OFF, 32768);
    const unsigned hf = lane >> 5;
    unsigned vo0 = (lane ^ hf) * 16, vo1 = (lane ^ (2 + hf)) * 16, vo2 = (lane ^ (4 + hf)) * 16, vo3 = (lane ^ (6 + hf)) * 16;
    const int lcol = late_ch * 4 + w4;
    const bool has_tile = late && lcol < 7;
    const rsrc_t rl = make_rsrc(is_wo ? p_wo : p_wd, (is_wo ? QW : FF) * DM * 2);
    unsigned lvo = r8 * (DM * 2) + (((lane & 7) ^ (r8 >> 1)) << 4), lso = late_rg * (64 * DM * 2) + lcol * 128;
    unsigned rvo = (((lane >> 3) & 1) * 4 + (lane >> 4)) * RP_BOX + (lane & 7) * 16;
    unsigned rso = WS_RP + lcol * 8 * RP_BOX + ((is_wo ? 0 : 64) + late_rg) * 128;
    LAS char *aa[4], *ta[4];
#pragma unroll
    for (int t = 0; t < 4; ++t) {
      aa[t] = lds + ((lane & 3) == t ? L_A + ((lane >> 2) & 3) * 64 + (lane >> 4) * 16 : L_ZERO);
      ta[t] = img + (lane >> 2) * 128 + (((lane & 2) * 2 + (t ^ ((lane >> 3) & 3))) << 4) + 8 * (lane & 1);
      PIN_V(aa[t]); PIN_V(ta[t]);
    }
    PIN_V(vo0); PIN_V(vo1); PIN_V(vo2); PIN_V(vo3); PIN_V(lvo); PIN_V(rvo); PIN_S(lso); PIN_S(rso);
    WG_BARRIER();
    _Pragma("nounroll") for (int g = 0; g < 8; ++g) {
      const unsigned so = ((SEQ >> (4 * g)) & 7) << 12;
      PACE((8 + g) * T_STEP);
      LAS void* dst = (LAS void*)(lds + L_V + (g * 4 + w4) * 4096);
      __builtin_amdgcn_raw_ptr_buffer_load_lds(rv, dst, 16, vo0, so, 0, AUX_NT);
      __builtin_amdgcn_raw_ptr_buffer_load_lds(rv, dst, 16, vo1, so, 1024, AUX_NT);
      __builtin_amdgcn_raw_ptr_buffer_load_lds(rv, dst, 16, vo2, so, 2048, AUX_NT);
      __builtin_amdgcn_raw_ptr_buffer_load_lds(rv, dst, 16, vo3, so, 3072, AUX_NT);
      VM_WAIT(8);
      lds_flag(lds, L_PROG + w4 * 4, g + 1);
    }
    VM_WAIT(4);
    lds_flag(lds, L_PROG + w4 * 4, 9);
    const bool before = has_tile && is_wo;
    if (!before) { VM_WAIT(0); WG_BARRIER_BARE(); }
    if (has_tile) {
      LSPIN() { const u32x4 f = lds_peek(lds, F_PV); if (wave_none((f[0] & f[1]) == 0u)) break; }
      PACE(15 * T_STEP + (is_wo ? T_WO : T_DOWN));
      tile_fetch(rl, lvo, lso, 8 * DM * 2 - 1024, img);
      tile_fetch(rl, lvo, lso + 32 * DM * 2, 8 * DM * 2 - 1024, img + 4 * 4096);
      if (before) {
        VM_WAIT(8);
        WG_BARRIER_BARE();
        VM_WAIT(2);
        lds_flag(lds, F_T + w4 * 4, 1u);
      }
      VM_DRAIN();
      bf16x8 wf[8];
#pragma unroll
      for (int t = 0; t < 4; ++t) { wf[t] = tr_frag<2048>(ta[t]); wf[4 + t] = tr_frag<2048>(ta[t] + 4 * 4096); }
      if (is_wo) WG_BARRIER();
      u32x4 act[8];
#pragma unroll
      for (int t = 0; t < 4; ++t) { act[2 * t] = lds_ld<u32x4>(aa[t], 0); act[2 * t + 1] = lds_ld<u32x4>(aa[t], 256); }
      SCHED_FENCE();
      f32x4 acc = {0.f, 0.f, 0.f, 0.f};
#pragma unroll
      for (int t = 0; t < 4; ++t) { acc = mma(asb(act[2 * t]), wf[t], acc); acc = mma(asb(act[2 * t + 1]), wf[4 + t], acc); }
      st_sc1(tag4(acc, tag), rws, rvo, rso);
    } else if (is_wo) {
      WG_BARRIER_BARE();
    }
  }
  int role = wave;
  PIN_S(role);
  if (role < 4) {
    u32x4 kr[32], qv;
#define qso (WS_QP + hk * 16384 + (idx >> 3) * 4096 + w4 * 1024)
    const unsigned kvo = (lane & 7) * 512 + ((lane >> 3) & 1) * 64 + (lane >> 4) * 16, seq = SEQ;
    const rsrc_t rk = make_rsrc(p_k + KV_OFF, 32768);
    const unsigned t_go = t_k + T_OFF;
#pragma unroll
    for (int g = 0; g < 8; ++g) {
      const unsigned so = ((seq >> (4 * g)) & 7) << 12;
      PACE(g * T_STEP);
#pragma unroll
      for (int i = 0; i < 4; ++i) kr[g * 4 + i] = __builtin_amdgcn_raw_buffer_load_b128(rk, kvo + i * 128, so, AUX_NT);
    }
    SCHED_FENCE();
    const unsigned kepoch = __builtin_amdgcn_raw_buffer_load_b32(rws, 0, WS_EPOCH, AUX_SC1);
    qv = ld_sc1(rws, l16, qso);
    WG_BARRIER_BARE();
    tag = next_tag(kepoch);
    asm volatile("" : "+s"(tag) : "v"(cpf));
    SPIN(SPIN_MAX) {
      qv ^= tag_nibbles(tag);
      if (wave_none((or4(qv) & 15u) != 0u)) break;
      MEM_FENCE();
      qv = ld_sc1(rws, l16, qso);
    }
    lds_st<unsigned>(lds, L_Q + w4 * 256 + (lane & 31) * 8 + (lane >> 5) * 4,
                     pack_bf16(fold32(asf(qv[0]), asf(qv[2])), fold32(asf(qv[1]), asf(qv[3]))));
    lds_flag(lds, F_Q + w4 * 4, 1u);
    lds_wait_all(lds, F_Q);
    float m0, m1;
    {
      const int qa = L_Q + (lane & 1) * 512 + ((lane >> 1) & 1) * 64 + (lane >> 4) * 16;
      f32x4 c0, c1;
      {
        u32x4 qf[4];
#pragma unroll
        for (int s = 0; s < 4; ++s) qf[s] = lds_ld<u32x4>(lds, qa + s * 128);
        f32x4 c[8];
#pragma unroll
        for (int t = 0; t < 8; ++t) c[t] = mma(asb(qf[0]), asb(kr[t * 4]), f32x4{0.f, 0.f, 0.f, 0.f});
#pragma unroll
        for (int s = 1; s < 4; ++s)
#pragma unroll
          for (int t = 0; t < 8; ++t) c[t] = mma(asb(qf[s]), asb(kr[t * 4 + s]), c[t]);
        const bool g1 = lane & 16, g2 = lane & 32;
        c0 = g2 ? (g1 ? c[3] : c[2]) : (g1 ? c[1] : c[0]);
        c1 = g2 ? (g1 ? c[7] : c[6]) : (g1 ? c[5] : c[4]);
      }
      const bool cb = lane & 8;
      const float r0 = (cb ? c1[2] : c0[0]) + dpp<XOR8>(cb ? c0[2] : c1[0]);
      const float r1 = (cb ? c1[3] : c0[1]) + dpp<XOR8>(cb ? c0[3] : c1[1]);
      const unsigned sp = pack_bf16(r0, r1);
      const float s0 = bf_lo(sp) * 0.0625f, s1 = bf_hi(sp) * 0.0625f;
      float mw = max32(s0, s1);
      mw = row_max(max16(mw, mw));
      *(volatile LAS unsigned*)(lds + L_MX + (lane >> 5) * 16 + w4 * 4) = asu(mw) & 0xfffffffeu;
      u32x4 x0, x1;
      LSPIN() { x0 = lds_peek(lds, L_MX); x1 = lds_peek(lds, L_MX + 16); if (wave_none(unset4(x0) | unset4(x1))) break; }
      m0 = fmaxf(fmaxf(asf(x0[0]), asf(x0[1])), fmaxf(asf(x0[2]), asf(x0[3])));
      m1 = fmaxf(fmaxf(asf(x1[0]), asf(x1[1])), fmaxf(asf(x1[2]), asf(x1[3])));
      const float p0 = __builtin_amdgcn_exp2f((s0 - m0) * LOG2E), p1 = __builtin_amdgcn_exp2f((s1 - m1) * LOG2E);
      const unsigned pk = pack_bf16(p0, p1);
      const int po = L_P + (((lane >> 3) & 1) * 4 + (lane >> 4)) * 64 + ((w4 & 1) * 2 + ((lane >> 2) & 1)) * 16 + ((w4 >> 1) * 4 + (lane & 3)) * 2;
      lds_st<u16>(lds, po, (u16)pk);
      lds_st<u16>(lds, po + 512, (u16)(pk >> 16));
      float l = fold32(p0, p1);
      l = row_sum(fold16(l, l));
      MEM_FENCE();
      *(volatile LAS unsigned*)(lds + L_LS + (lane >> 5) * 16 + w4 * 4) = asu(l) & 0xfffffffeu;
    }
    u32x4 ls;
    LSPIN() { ls = lds_peek(lds, L_LS + (lane >> 5) * 16); if (wave_none(unset4(ls))) break; }
    MEM_FENCE();
    u32x4 tb = tag_bytes(tag);
    unsigned oso = WS_OP + wo_rg * 8192 + w4 * 2048;
    LAS char* ad = lds + L_A + (lane & 4) * 64 + w4 * 64 + ((lane & 1) * 2 + ((lane >> 3) & 1)) * 16 + ((lane & 2) * 2 + (lane >> 4)) * 2;
    LAS char* fa = lds + L_F + w4 * 32 + (lane >> 3) * 4;
    PIN_S(tb); PIN_S(oso); PIN_V(ad); PIN_V(fa);
    if (w4 < 2) {
      const int g = lane >> 4, row = lane >> 2, p = lane & 3, tl = lane & 7;
      LAS char* va[8];
#pragma unroll
      for (int t = 0; t < 8; ++t) va[t] = lds + L_V + row * 512 + ((w4 * 16 + (p >> 1) * 8 + (t ^ (row & 7))) << 4) + 8 * (p & 1);
      LAS char* pa = lds + L_P + ((lane >> 3) & 1) * 512 + g * 16;
      unsigned ovo = (((lane >> 3) & 1) * 4 + (g >> 1)) * 8192 + (g & 1) * 1024 + tl * 16;
      unsigned pso = WS_OP + (hk * 8 + w4 * 2) * 8192 + (idx >> 3) * 2048 + (idx & 7) * 128;
      PIN_V(ovo); PIN_S(pso);
      f32x4 o = {0.f, 0.f, 0.f, 0.f};
      u32x4 pm[8];
      {
        const u32x4 pl = lds_ld<u32x4>(pa, 0);
#pragma unroll
        for (int t = 0; t < 8; ++t) pm[t] = tl == t ? pl : u32x4{0u, 0u, 0u, 0u};
      }
      _Pragma("nounroll") for (int ks = 0; ks < 7; ++ks) {
        LSPIN() {
          const u32x4 pr = lds_peek(lds, L_PROG);
          if (wave_none(min(min(pr[0], pr[1]), min(pr[2], pr[3])) <= (unsigned)(ks + 2))) break;
        }
        lds_flag(lds, F_PV + w4 * 4, ks >= 2 ? 1u : 0u);
        pa += 64;
        const u32x4 pl = lds_ld<u32x4>(pa, 0);
        bf16x8 vf[8];
#pragma unroll
        for (int t = 0; t < 8; ++t) vf[t] = tr_frag<8192>(va[t]);
        SCHED_FENCE();
#pragma unroll
        for (int t = 0; t < 8; ++t) {
          o = mma(vf[t], asb(pm[t]), o);
          SCHED_FENCE();
          pm[t] = tl == t ? pl : u32x4{0u, 0u, 0u, 0u};
          va[t] += 16384;
          PIN_V(pm[t]); PIN_V(va[t]);
          SCHED_FENCE();
        }
      }
      WG_BARRIER_BARE();
      {
        bf16x8 vf[8];
#pragma unroll
        for (int t = 0; t < 8; ++t) vf[t] = tr_frag<8192>(va[t]);
        SCHED_FENCE();
#pragma unroll
        for (int t = 0; t < 8; ++t) o = mma(vf[t], asb(pm[t]), o);
      }
      st_sc1(tag4(o, tag), rws, ovo, pso);
    } else if (w4 == 2) {
      if (late && !is_wo) {
        u32x4 gv[4];
        SPIN(SPIN_MAX) {
          unsigned bad = 0;
#pragma unroll
          for (int u = 0; u < 4; ++u) gv[u] = ld_sc1(rws, l16 + u * 1024, WS_GP + (dn_un >> 1) * 4096);
#pragma unroll
          for (int u = 0; u < 4; ++u) { gv[u] ^= tag_nibbles(tag); bad |= or4(gv[u]); }
          if (wave_none((bad & 15u) != 0u)) break;
          MEM_FENCE();
        }
        const f32x4 gs = asf4(gv[0]) + asf4(gv[1]), us = asf4(gv[2]) + asf4(gv[3]);
        u32x2 hv;
#pragma unroll
        for (int e = 0; e < 2; ++e) {
          const unsigned gt = pack_bf16(gs[2 * e], gs[2 * e + 1]), up = pack_bf16(us[2 * e], us[2 * e + 1]);
          const float g0 = bf_lo(gt), g1 = bf_hi(gt);
          hv[e] = pack_bf16(g0 * (g0 > 0.f ? 1.f : 0.f) * bf_lo(up), g1 * (g1 > 0.f ? 1.f : 0.f) * bf_hi(up));
        }
        lds_st<u32x2>(lds, L_A + (lane & 8) * 32 + (lane >> 4) * 64 + (lane & 3) * 16 + (lane & 4) * 2, hv);
      } else if (is_red) {
        xv = __builtin_amdgcn_raw_buffer_load_b16(make_rsrc(p_x, NB * DM * 2), oi * 2, 0, 0);
        PIN_V(xv);
      }
      WG_BARRIER();
    } else {
      const float lsum = (asf(ls[0]) + asf(ls[1])) + (asf(ls[2]) + asf(ls[3]));
      const float l1 = asf(__builtin_amdgcn_readlane(asu(lsum), 32));
      if (lane == 0) st_sc1(tag4(f32x4{m0, lsum, m1, l1}, tag), rws, 0, WS_ML + (hk * 32 + idx) * 16);
      const unsigned mso = is_wo ? WS_ML + (wo_rg >> 3) * 512 : WS_END;
      LSPIN() { if (wave_none(*(volatile LAS unsigned*)(lds + L_PROG) < 7u)) break; __builtin_amdgcn_s_sleep(4); }
      u32x4 mv = ld_sc1(rws, (lane & 31) * 16, mso);
      WG_BARRIER_BARE();
      mv ^= tb;
      PIN_V(mv);
      if (is_wo) {
        SPIN(SPIN_MAX) {
          if (wave_none((or4(mv) & 255u) != 0u)) break;
          MEM_FENCE();
          mv = ld_sc1(rws, (lane & 31) * 16, mso) ^ tb;
        }
        const float m = asf((idx & 8) ? mv[2] : mv[0]);
        float mx = fmaxf(m, dpp<XOR7>(m));
        mx = fmaxf(mx, dpp<XOR2>(mx));
        mx = fmaxf(mx, dpp<XOR1>(mx));
        const float e = __builtin_amdgcn_exp2f((m - mx) * LOG2E);
        float lt = e * asf((idx & 8) ? mv[3] : mv[1]);
        lt += dpp<XOR7>(lt);
        lt += dpp<XOR2>(lt);
        lt += dpp<XOR1>(lt);
        *(volatile LAS unsigned*)(lds + L_F + (lane & 31) * 4) = asu(e * __builtin_amdgcn_rcpf(lt)) & 0xfffffffeu;
      } else if (bid == 56) {
        SPIN(SPIN_MAX) {
          unsigned bad = 0;
#pragma unroll
          for (int u = 0; u < 4; ++u) bad |= or4(ld_sc1(rws, l16 + u * 1024, WS_ML) ^ tb);
          if (wave_none((bad & 255u) != 0u)) break;
          MEM_FENCE();
        }
        if (lane == 0) __builtin_amdgcn_raw_buffer_store_b32(tag, rws, 0, WS_EPOCH, AUX_SC1);
      }
    }
    if (is_wo) {
      float f;
      LSPIN() {
        f = *(volatile LAS float*)fa;
        if (wave_none((asu(f) == 0xffffffffu) | (*(volatile LAS unsigned*)(lds + F_T) == 0u))) break;
      }
      u32x4 o0, o1;
      MEM_FENCE();
      SPIN(SPIN_MAX) {
        o0 = ld_sc1(rws, l16, oso) ^ tb;
        o1 = ld_sc1(rws, l16 + 1024, oso) ^ tb;
        if (wave_none(((or4(o0) | or4(o1)) & 255u) != 0u)) break;
        MEM_FENCE();
      }
      const float o = fold_pair(asf4(o0) * f, asf4(o1) * f);
      lds_st<u16>(ad, 0, (u16)pack_bf16(o, o));
      WG_BARRIER();
    }
  }

  if (is_red) {
    const u32x4 tb = tag_bytes(tag);
    LAS char* fw = lds + L_FIN + ((lane >> 4) * 8 + (lane & 7)) * 64 + wave * 4 + (lane & 8) * 4;
    LAS char* fr = lds + L_FIN + (lane & 31) * 64 + (lane >> 5) * 32;
    unsigned oo = (lane & 32) ? 0x7fffffffu : oi * 2;
    const bool dn_lane = wave * 64 + lane < 28 * 8;
    unsigned dvo = dn_lane ? l16 + 8192 : 0x7fffffffu;
    u32x4 td = dn_lane ? tb : u32x4{0u, 0u, 0u, 0u};
    float xf = bf_lo(xv);
    PIN_V(fw); PIN_V(fr); PIN_V(oo); PIN_V(xf); PIN_V(dvo); PIN_V(td);
    const rsrc_t ro = make_rsrc(p_out, NB * DM * 2);
    const unsigned so = WS_RP + bid * RP_BOX + wave * 1024;
    u32x4 a0, a1;
    SPIN(SPIN_MAX) {
      a1 = ld_sc1(rws, dvo, so) ^ td;
      a0 = ld_sc1(rws, l16, so) ^ tb;
      if (wave_none(((or4(a0) | or4(a1)) & 255u) != 0u)) break;
      MEM_FENCE();
    }
    lds_st<float>(fw, 0, fold_pair(asf4(a0), asf4(a1)));
    WG_BARRIER();
    if (wave == 2) {
      const f32x4 t = lds_ld<f32x4>(fr, 0) + lds_ld<f32x4>(fr, 16);
      const f32x2 t2 = f32x2{t[0], t[1]} + f32x2{t[2], t[3]};
      const float u = round_bf16(t2[0] + t2[1]);
      auto sw = __builtin_amdgcn_permlane32_swap(asu(u), asu(u), false, false);
      const float sv = asf(sw[0]), dn = asf(sw[1]);
      const float g = round_bf16(dn * __builtin_amdgcn_rcpf(1.0f + __builtin_amdgcn_exp2f(-dn * LOG2E)));
      const float res = xf + round_bf16(sv * g);
      __builtin_amdgcn_raw_buffer_store_b16((u16)pack_bf16(res, res), ro, oo, 0, 0);
    }
  }
}

}

extern "C" void kernel_launch(void* const* d_in, const int* in_sizes, int n_in, void* d_out, int out_size, void* d_ws,
                              size_t ws_size, hipStream_t stream) {
  fused_decode<<<256, 512, 0, stream>>>((const u16*)d_in[0], (const u16*)d_in[1], (const u16*)d_in[2], (const u16*)d_in[3],
                                        (const u16*)d_in[4], (const u16*)d_in[6], (const u16*)d_in[7], (const u16*)d_in[8],
                                        (u16*)d_out, (char*)d_ws);
}
```

```cpp
#include <hip/hip_runtime.h>
#include <hip/hip_bf16.h>
#include <stdint.h>

namespace {

typedef unsigned short u16;
typedef unsigned long long u64;
typedef unsigned u32x2 __attribute__((ext_vector_type(2)));
typedef unsigned u32x4 __attribute__((ext_vector_type(4)));
typedef float f32x2 __attribute__((ext_vector_type(2)));
typedef float f32x4 __attribute__((ext_vector_type(4)));
typedef short s16x4 __attribute__((ext_vector_type(4)));
typedef __bf16 bf16x2 __attribute__((ext_vector_type(2)));
typedef __bf16 bf16x8 __attribute__((ext_vector_type(8)));
#define LAS __attribute__((address_space(3)))
typedef __amdgpu_buffer_rsrc_t rsrc_t;

constexpr int NB = 4, DM = 448, SK = 8192, WIN = 2048, FF = 1792, QW = 4096;
constexpr int AUX_SC1 = 16, AUX_NT = 2;
constexpr unsigned SPIN_MAX = 1u << 22;
constexpr unsigned CODE_BYTES = 11560;
constexpr int T_FIRST = 52;
constexpr int T_STAGGER = 14;
constexpr int T_STEP = 56;
constexpr int T_WO = 64, T_DOWN = 70;
constexpr float LOG2E = 1.4426950408889634f;

constexpr unsigned WS_EPOCH = 0;
constexpr unsigned WS_QP = 4096;
constexpr unsigned WS_GP = WS_QP + 8 * 4 * 4096;
constexpr unsigned WS_OP = WS_GP + 28 * 4096;
constexpr unsigned WS_ML = WS_OP + 64 * 8192;
constexpr unsigned WS_RP = WS_ML + 4096;
constexpr unsigned RP_BOX = 92 * 128;
constexpr unsigned WS_END = WS_RP + 56 * RP_BOX;

constexpr int L_V = 0;
constexpr int L_TILE = L_V;
constexpr int L_XN = L_V + 32768;
constexpr int L_FIN = L_V;
constexpr int L_Q = 131072;
constexpr int L_P = L_Q + 1024;
constexpr int L_A = L_P + 1024;
constexpr int L_SYNC = L_A + 512;
constexpr int L_ZERO = L_SYNC;
constexpr int F_XN = L_SYNC + 16, F_EP = L_SYNC + 32, F_Q = L_SYNC + 48, L_PROG = L_SYNC + 64, F_PV = L_SYNC + 80, F_T = L_SYNC + 96;
constexpr int L_F = L_SYNC + 128;
constexpr int L_MX = L_SYNC + 384;
constexpr int L_LS = L_MX + 32;
constexpr int L_END = L_SYNC + 512;

#define SCHED_FENCE() __builtin_amdgcn_sched_barrier(0)
#define MEM_FENCE() asm volatile("" ::: "memory")
#define WG_BARRIER() do { SCHED_FENCE(); asm volatile("s_waitcnt lgkmcnt(0)\n\ts_barrier" ::: "memory"); SCHED_FENCE(); } while (0)
#define WG_BARRIER_BARE() do { SCHED_FENCE(); asm volatile("s_barrier" ::: "memory"); SCHED_FENCE(); } while (0)
#define VM_WAIT(n) do { SCHED_FENCE(); asm volatile("s_waitcnt vmcnt(%0)" :: "i"(n) : "memory"); SCHED_FENCE(); } while (0)
#define VM_DRAIN() do { SCHED_FENCE(); __builtin_amdgcn_s_waitcnt(0x0F70); SCHED_FENCE(); MEM_FENCE(); } while (0)
#define NAP() __builtin_amdgcn_s_sleep(1)
#define PACE(t) do { SCHED_FENCE(); pace_until(t_go + (t)); SCHED_FENCE(); } while (0)
#define PIN_V(x) asm volatile("" : "+v"(x))
#define PIN_S(x) asm volatile("" : "+s"(x))
#define LSPIN() _Pragma("nounroll") for (;;)
#define SPIN(limit) _Pragma("nounroll") for (unsigned s_ = 0; s_ < (limit); ++s_)

__device__ __forceinline__ void pace_until(unsigned when) {
  asm volatile("1:\n\ts_memrealtime vcc\n\ts_waitcnt lgkmcnt(0)\n\ts_sub_i32 vcc_lo, vcc_lo, %0\n\ts_cmp_lt_i32 vcc_lo, 0\n\ts_cbranch_scc1 1b"
               :: "s"(when) : "vcc", "scc", "memory");
}
__device__ __forceinline__ float asf(unsigned u) { return __builtin_bit_cast(float, u); }
__device__ __forceinline__ unsigned asu(float f) { return __builtin_bit_cast(unsigned, f); }
__device__ __forceinline__ unsigned pack_bf16(float lo, float hi) {
  f32x2 f = {lo, hi};
  return __builtin_bit_cast(unsigned, __builtin_convertvector(f, bf16x2));
}
__device__ __forceinline__ float round_bf16(float v) { return asf(pack_bf16(0.f, v)); }
__device__ __forceinline__ float bf_lo(unsigned u) { return asf(u << 16); }
__device__ __forceinline__ float bf_hi(unsigned u) { return asf(u & 0xffff0000u); }
template <int CTRL> __device__ __forceinline__ float dpp(float x) {
  return asf((unsigned)__builtin_amdgcn_mov_dpp((int)asu(x), CTRL, 0xf, 0xf, true));
}
constexpr int XOR1 = 0xB1, XOR2 = 0x4E, XOR7 = 0x141, XOR8 = 0x128;
__device__ __forceinline__ float fold16(float a, float b) {
  auto s = __builtin_amdgcn_permlane16_swap(asu(a), asu(b), false, false);
  return asf(s[0]) + asf(s[1]);
}
__device__ __forceinline__ float fold32(float a, float b) {
  auto s = __builtin_amdgcn_permlane32_swap(asu(a), asu(b), false, false);
  return asf(s[0]) + asf(s[1]);
}
__device__ __forceinline__ float max16(float a, float b) {
  auto s = __builtin_amdgcn_permlane16_swap(asu(a), asu(b), false, false);
  return fmaxf(asf(s[0]), asf(s[1]));
}
__device__ __forceinline__ float max32(float a, float b) {
  auto s = __builtin_amdgcn_permlane32_swap(asu(a), asu(b), false, false);
  return fmaxf(asf(s[0]), asf(s[1]));
}
__device__ __forceinline__ float row_sum(float v) {
  v += dpp<XOR8>(v); v += dpp<XOR7>(v); v += dpp<XOR2>(v); v += dpp<XOR1>(v);
  return v;
}
__device__ __forceinline__ float wave_sum(float v) { v = fold32(v, v); return row_sum(fold16(v, v)); }
__device__ __forceinline__ float row_max(float v) {
  v = fmaxf(v, dpp<XOR8>(v)); v = fmaxf(v, dpp<XOR7>(v)); v = fmaxf(v, dpp<XOR2>(v)); v = fmaxf(v, dpp<XOR1>(v));
  return v;
}
__device__ __forceinline__ float fold_pair(f32x4 a, f32x4 b) {
  const float a0 = fold32(a[0], a[2]), a1 = fold32(a[1], a[3]), b0 = fold32(b[0], b[2]), b1 = fold32(b[1], b[3]);
  SCHED_FENCE();
  const float za = fold16(a0, a1), zb = fold16(b0, b1);
  const bool hi = __builtin_amdgcn_inverse_ballot_w64(0xff00ff00ff00ff00ull);
  return (hi ? zb : za) + dpp<XOR8>(hi ? za : zb);
}
__device__ __forceinline__ rsrc_t make_rsrc(u64 p, unsigned bytes) {
  return __builtin_amdgcn_make_buffer_rsrc((void*)p, 0, bytes, 0x00020000);
}
__device__ __forceinline__ u32x4 tag4(f32x4 v, unsigned tag) {
  u32x4 r;
  r[0] = __builtin_amdgcn_perm(asu(v[0]), tag, 0x07060500u);
  r[1] = __builtin_amdgcn_perm(asu(v[1]), tag, 0x07060501u);
  r[2] = __builtin_amdgcn_perm(asu(v[2]), tag, 0x07060502u);
  r[3] = __builtin_amdgcn_perm(asu(v[3]), tag, 0x07060503u);
  return r;
}
__device__ __forceinline__ unsigned next_tag(unsigned epoch) {
  const unsigned e = __builtin_amdgcn_readfirstlane(epoch);
  return ((e & 0x7f7f7f7fu) + 0x07050301u) ^ (e & 0x80808080u);
}
__device__ __forceinline__ u32x4 tag_bytes(unsigned tag) { return u32x4{tag & 255u, (tag >> 8) & 255u, (tag >> 16) & 255u, tag >> 24}; }
__device__ __forceinline__ u32x4 tag4_block(f32x4 v, unsigned tag) {
  const u32x4 a = __builtin_bit_cast(u32x4, v) & 0x7fffffffu, tb = tag_bytes(tag);
  const unsigned e = max(max(a[0], a[1]), max(a[2], a[3])) >> 23;
  u32x4 r;
#pragma unroll
  for (int k = 0; k < 4; ++k) {
    const float m = fminf(fmaxf(__builtin_rintf(__builtin_amdgcn_ldexpf(v[k], 147 - (int)e)), -2097152.f), 2097151.f);
    r[k] = ((unsigned)(int)m << 10) | (((e >> (2 * k)) & 3u) << 8) | tb[k];
  }
  return r;
}
__device__ __forceinline__ f32x4 block_values(u32x4 y) {
  const unsigned e = ((y[0] >> 8) & 3u) | ((y[1] >> 6) & 12u) | ((y[2] >> 4) & 48u) | ((y[3] >> 2) & 192u);
  f32x4 v;
#pragma unroll
  for (int k = 0; k < 4; ++k) v[k] = e == 255u ? asf(0x7fc00000u) : __builtin_amdgcn_ldexpf((float)((int)y[k] >> 10), (int)e - 147);
  return v;
}
__device__ __forceinline__ unsigned or4(u32x4 v) { return v[0] | v[1] | v[2] | v[3]; }
__device__ __forceinline__ f32x4 asf4(u32x4 v) { return __builtin_bit_cast(f32x4, v); }
__device__ __forceinline__ u32x4 ld_sc1(rsrc_t r, unsigned voff, unsigned soff) {
  return __builtin_amdgcn_raw_buffer_load_b128(r, voff, soff, AUX_SC1);
}
__device__ __forceinline__ void st_sc1(u32x4 v, rsrc_t r, unsigned voff, unsigned soff) {
  __builtin_amdgcn_raw_buffer_store_b128(v, r, voff, soff, AUX_SC1);
}
__device__ __forceinline__ bool wave_none(bool bad) { return __builtin_amdgcn_ballot_w64(bad) == 0ull; }

template <typename T> __device__ __forceinline__ T lds_ld(LAS char* l, int off) { return *(LAS T*)(l + off); }
template <typename T> __device__ __forceinline__ void lds_st(LAS char* l, int off, T v) { *(LAS T*)(l + off) = v; }
__device__ __forceinline__ void lds_flag(LAS char* l, int off, unsigned v) {
  MEM_FENCE();
  *(volatile LAS unsigned*)(l + off) = v;
  MEM_FENCE();
}
__device__ __forceinline__ u32x4 lds_peek(LAS char* l, int off) { return *(volatile LAS u32x4*)(l + off); }
__device__ __forceinline__ void lds_wait_all(LAS char* l, int off) {
  LSPIN() { const u32x4 f = lds_peek(l, off); if (wave_none((f[0] & f[1] & f[2] & f[3]) == 0u)) break; NAP(); }
  MEM_FENCE();
}
__device__ __forceinline__ bool unset4(u32x4 v) { return max(max(v[0], v[1]), max(v[2], v[3])) == 0xffffffffu; }

__device__ __forceinline__ void tile_fetch(rsrc_t r, unsigned voff, unsigned soff, unsigned step, LAS char* img) {
  __builtin_amdgcn_raw_ptr_buffer_load_lds(r, (LAS void*)img, 16, voff, soff, 0, AUX_NT);
  __builtin_amdgcn_raw_ptr_buffer_load_lds(r, (LAS void*)img, 16, voff, soff + step, 1024, AUX_NT);
  __builtin_amdgcn_raw_ptr_buffer_load_lds(r, (LAS void*)img, 16, voff, soff + 2 * step, 2048, AUX_NT);
  __builtin_amdgcn_raw_ptr_buffer_load_lds(r, (LAS void*)img, 16, voff, soff + 3 * step, 3072, AUX_NT);
}
template <int GAP> __device__ __forceinline__ bf16x8 tr_frag(LAS char* a) {
  const s16x4 lo = __builtin_amdgcn_ds_read_tr16_b64_v4i16((LAS s16x4*)a);
  const s16x4 hi = __builtin_amdgcn_ds_read_tr16_b64_v4i16((LAS s16x4*)(a + GAP));
  return __builtin_bit_cast(bf16x8, __builtin_shufflevector(lo, hi, 0, 1, 2, 3, 4, 5, 6, 7));
}
__device__ __forceinline__ void tile_frags(LAS char* img, int lane, bf16x8 (&w)[4]) {
  const int row = lane >> 2, p = lane & 3;
#pragma unroll
  for (int t = 0; t < 4; ++t) w[t] = tr_frag<2048>(img + row * 128 + (((2 * t + (p >> 1)) ^ (row & 6)) << 4) + 8 * (p & 1));
}
__device__ __forceinline__ f32x4 mma(bf16x8 a, bf16x8 b, f32x4 c) { return __builtin_amdgcn_mfma_f32_16x16x32_bf16(a, b, c, 0, 0, 0); }
__device__ __forceinline__ bf16x8 asb(u32x4 v) { return __builtin_bit_cast(bf16x8, v); }

__global__ void __launch_bounds__(512, 2)
fused_decode(const u16* x, const u16* kc, const u16* vc, const u16* wq, const u16* wo, const u16* wg, const u16* wu,
             const u16* wd, u16* out, char* ws) {
  __shared__ __attribute__((aligned(16))) char lds_raw[L_END];
  LAS char* lds = (LAS char*)lds_raw;
  const int bid = blockIdx.x, lane = threadIdx.x & 63, wave = __builtin_amdgcn_readfirstlane(threadIdx.x >> 6);
  const int hk = bid & 7, idx = bid >> 3, w4 = wave & 3;
  const bool loader = wave >= 4;
  const u64 p_x = (u64)x, p_k = (u64)kc, p_v = (u64)vc, p_wq = (u64)wq, p_wo = (u64)wo, p_wg = (u64)wg, p_wu = (u64)wu, p_wd = (u64)wd;
  const u64 p_out = (u64)out, p_ws = (u64)ws;
#define ARGS_IN "s"(p_x), "s"(p_k), "s"(p_v), "s"(p_wq), "s"(p_wo), "s"(p_wg), "s"(p_wu), "s"(p_wd), "s"(p_out), "s"(p_ws)
#define KV_OFF ((((idx >> 3) * 8 + hk) * SK + (SK - WIN) + (idx & 7) * 256) * 512 + w4 * 32768)
#define SEQ ((unsigned)(0x12075643120ull >> ((idx & 3) * 4)))
#define T_OFF (T_FIRST + (3 - (idx >> 3)) * T_STAGGER)
#define is_wo (idx >= 16)
#define late (idx >= 9)
#define is_red (idx < 7)
#define wo_rg (((hk + 1 + ((idx >> 1) & 3)) & 7) * 8 + ((idx >> 1) & 7))
#define dn_un (bid - 72)
#define late_rg (is_wo ? wo_rg : dn_un >> 1)
#define late_ch ((is_wo ? idx : dn_un) & 1)
#define oi ((bid & 3) * DM + (bid >> 2) * 32 + (lane & 31))
#define rws make_rsrc(p_ws, WS_END)
  const unsigned l16 = lane * 16;
  unsigned tag = 0, xv = 0, cpf = 0, t_k = 0;

  if (!loader) {
    const unsigned iv = (lane & 16) ? 0xffffffffu : 0u;
    lds_st<u32x2>(lds, L_SYNC + lane * 8, u32x2{iv, iv});
    cpf = __builtin_amdgcn_raw_buffer_load_b32(make_rsrc(__builtin_amdgcn_s_getpc() & ~127ull, CODE_BYTES), 0, (idx * 4 + w4) * 128, 0);
    asm volatile("" :: ARGS_IN);
    WG_BARRIER();
    t_k = (unsigned)__builtin_amdgcn_s_memrealtime();
    PIN_S(t_k);
  }
  MEM_FENCE();
  if (loader) {
    const bool is_q = idx < 16, is_gate = idx < 23, has_early = idx < 30;
    const int half = hk & 1;
    int et_q = idx + 16 * ((idx + (hk >> 1)) & 3), et_f = (idx - (is_gate ? 16 : 23)) * 4 + (hk >> 1);
    asm("" : "+s"(et_q), "+s"(et_f));
    const int etile = is_q ? et_q : et_f;
    const int r8 = lane >> 3;
    LAS char* img = lds + L_TILE + w4 * 4096;
    const unsigned stride = is_q ? QW * 2 : FF * 2;
    unsigned evo = r8 * stride + (((lane & 7) ^ (r8 & 6)) << 4), eso = (half * 224 + w4 * 32) * stride + etile * 128;
    unsigned ebytes = has_early ? DM * stride : 0, eso2 = eso + 128 * stride, estep = 8 * stride - 1024;
    PIN_V(evo);
    asm volatile("" : "+s"(eso), "+s"(estep), "+s"(ebytes), "+s"(eso2));
    SCHED_FENCE();
    asm volatile("" :: ARGS_IN : "memory");
    {
      const rsrc_t rw = make_rsrc(is_q ? p_wq : (is_gate ? p_wg : p_wu), ebytes);
      tile_fetch(rw, evo, eso, estep, img);
      if (w4 < 3) tile_fetch(rw, evo, eso2, estep, img + 4 * 4096);
    }
    WG_BARRIER_BARE();
    const unsigned t_go = (unsigned)__builtin_amdgcn_s_memrealtime() + T_OFF;
    SCHED_FENCE();
    const u32x4 xr = __builtin_amdgcn_raw_buffer_load_b128(make_rsrc(p_x, NB * DM * 2), l16, w4 * DM * 2, 0);
    const unsigned epoch = __builtin_amdgcn_raw_buffer_load_b32(rws, 0, WS_EPOCH, AUX_SC1);
    SCHED_FENCE();
    {
      float v[8], t = 0.f;
#pragma unroll
      for (int k = 0; k < 4; ++k) { v[2 * k] = bf_lo(xr[k]); v[2 * k + 1] = bf_hi(xr[k]); t += v[2 * k] + v[2 * k + 1]; }
      const float mean = wave_sum(lane < 56 ? t : 0.f) * (1.0f / DM);
      t = 0.f;
#pragma unroll
      for (int k = 0; k < 8; ++k) { const float d = v[k] - mean; t = fmaf(d, d, t); }
      const float var = round_bf16(wave_sum(lane < 56 ? t : 0.f) * (1.0f / DM)), mb = round_bf16(mean);
      const float rc = __builtin_amdgcn_rcpf(round_bf16(__builtin_amdgcn_sqrtf(round_bf16(var + 1e-5f))));
      u32x4 xn;
#pragma unroll
      for (int k = 0; k < 4; ++k) {
        const unsigned d = pack_bf16(v[2 * k] - mb, v[2 * k + 1] - mb);
        xn[k] = pack_bf16(bf_lo(d) * rc, bf_hi(d) * rc);
      }
      lds_st<u32x4>(lds, L_XN + w4 * 1024 + lane * 16, xn);
      lds_flag(lds, F_XN + w4 * 4, 1u);
      lds_wait_all(lds, F_XN);
      tag = next_tag(epoch);
      PIN_S(tag);
      const int bt = lane & 3, g = lane >> 4, jj = (lane >> 2) & 3;
      const int xo = L_XN + bt * 1024 + (half * 224 + w4 * 32 + 4 * g) * 2;
      const u32x2 a0 = lds_ld<u32x2>(lds, xo), a1 = lds_ld<u32x2>(lds, xo + 32);
      const u32x2 a2 = lds_ld<u32x2>(lds, xo + 256), a3 = lds_ld<u32x2>(lds, xo + 288);
      const u32x4 act0 = {a0[0], a0[1], a1[0], a1[1]}, act1 = {a2[0], a2[1], a3[0], a3[1]}, zero = {0u, 0u, 0u, 0u};
      VM_DRAIN();
      bf16x8 wf0[4], wf1[4];
      tile_frags(img, lane, wf0);
      f32x4 acc = {0.f, 0.f, 0.f, 0.f};
      if (w4 < 3) {
        tile_frags(img + 4 * 4096, lane, wf1);
#pragma unroll
        for (int t = 0; t < 4; ++t) acc = mma(wf1[t], asb(jj == t ? act1 : zero), acc);
      }
#pragma unroll
      for (int t = 0; t < 4; ++t) acc = mma(wf0[t], asb(jj == t ? act0 : zero), acc);
      lds_st<f32x4>(img, bt * 256 + ((lane & 12) + g) * 16, acc);
      lds_flag(lds, F_EP + w4 * 4, 1u);
      if (w4 == 3 && has_early) {
        const unsigned voff = is_q ? WS_QP + (etile >> 3) * 16384 + (lane >> 4) * 4096 + (etile & 6) * 512 + half * 512 + (etile & 1) * 256 + (lane & 15) * 16
                                   : WS_GP + etile * 4096 + (is_gate ? 0 : 2048) + half * 1024 + l16;
        lds_wait_all(lds, F_EP);
        f32x4 sum = lds_ld<f32x4>(lds, L_TILE + lane * 16);
#pragma unroll
        for (int p = 1; p < 4; ++p) sum += lds_ld<f32x4>(lds, L_TILE + p * 4096 + lane * 16);
        st_sc1(tag4_block(sum, tag), rws, voff, 0);
      }
    }
    const rsrc_t rv = make_rsrc(p_v + KV_OFF, 32768);
    const unsigned hf = lane >> 5;
    unsigned vo0 = (lane ^ hf) * 16, vo1 = (lane ^ (2 + hf)) * 16, vo2 = (lane ^ (4 + hf)) * 16, vo3 = (lane ^ (6 + hf)) * 16;
    const int lcol = late_ch * 4 + w4;
    const bool has_tile = late && lcol < 7;
    const rsrc_t rl = make_rsrc(is_wo ? p_wo : p_wd, (is_wo ? QW : FF) * DM * 2);
    unsigned lvo = r8 * (DM * 2) + (((lane & 7) ^ (r8 >> 1)) << 4), lso = late_rg * (64 * DM * 2) + lcol * 128;
    unsigned rvo = (((lane >> 3) & 1) * 4 + (lane >> 4)) * RP_BOX + (lane & 7) * 16;
    unsigned rso = WS_RP + lcol * 8 * RP_BOX + ((is_wo ? 0 : 64) + late_rg) * 128;
    LAS char *aa[4], *ta[4];
#pragma unroll
    for (int t = 0; t < 4; ++t) {
      aa[t] = lds + ((lane & 3) == t ? L_A + ((lane >> 2) & 3) * 64 + (lane >> 4) * 16 : L_ZERO);
      ta[t] = img + (lane >> 2) * 128 + (((lane & 2) * 2 + (t ^ ((lane >> 3) & 3))) << 4) + 8 * (lane & 1);
      PIN_V(aa[t]); PIN_V(ta[t]);
    }
    PIN_V(vo0); PIN_V(vo1); PIN_V(vo2); PIN_V(vo3); PIN_V(lvo); PIN_V(rvo); PIN_S(lso); PIN_S(rso);
    WG_BARRIER();
    _Pragma("nounroll") for (int g = 0; g < 8; ++g) {
      const unsigned so = ((SEQ >> (4 * g)) & 7) << 12;
      PACE((8 + g) * T_STEP);
      LAS void* dst = (LAS void*)(lds + L_V + (g * 4 + w4) * 4096);
      __builtin_amdgcn_raw_ptr_buffer_load_lds(rv, dst, 16, vo0, so, 0, AUX_NT);
      __builtin_amdgcn_raw_ptr_buffer_load_lds(rv, dst, 16, vo1, so, 1024, AUX_NT);
      __builtin_amdgcn_raw_ptr_buffer_load_lds(rv, dst, 16, vo2, so, 2048, AUX_NT);
      __builtin_amdgcn_raw_ptr_buffer_load_lds(rv, dst, 16, vo3, so, 3072, AUX_NT);
      VM_WAIT(8);
      lds_flag(lds, L_PROG + w4 * 4, g + 1);
    }
    VM_WAIT(4);
    lds_flag(lds, L_PROG + w4 * 4, 9);
    const bool before = has_tile && is_wo;
    if (!before) { VM_WAIT(0); WG_BARRIER_BARE(); }
    if (has_tile) {
      LSPIN() { const u32x4 f = lds_peek(lds, F_PV); if (wave_none((f[0] & f[1]) == 0u)) break; }
      PACE(15 * T_STEP + (is_wo ? T_WO : T_DOWN));
      tile_fetch(rl, lvo, lso, 8 * DM * 2 - 1024, img);
      tile_fetch(rl, lvo, lso + 32 * DM * 2, 8 * DM * 2 - 1024, img + 4 * 4096);
      if (before) {
        VM_WAIT(8);
        WG_BARRIER_BARE();
        VM_WAIT(4);
        lds_flag(lds, F_T + w4 * 4, 1u);
      }
      VM_DRAIN();
      bf16x8 wf[8];
#pragma unroll
      for (int t = 0; t < 4; ++t) { wf[t] = tr_frag<2048>(ta[t]); wf[4 + t] = tr_frag<2048>(ta[t] + 4 * 4096); }
      if (is_wo) WG_BARRIER();
      u32x4 act[8];
#pragma unroll
      for (int t = 0; t < 4; ++t) { act[2 * t] = lds_ld<u32x4>(aa[t], 0); act[2 * t + 1] = lds_ld<u32x4>(aa[t], 256); }
      SCHED_FENCE();
      f32x4 acc = {0.f, 0.f, 0.f, 0.f};
#pragma unroll
      for (int t = 0; t < 4; ++t) { acc = mma(asb(act[2 * t]), wf[t], acc); acc = mma(asb(act[2 * t + 1]), wf[4 + t], acc); }
      st_sc1(tag4(acc, tag), rws, rvo, rso);
    } else if (is_wo) {
      WG_BARRIER_BARE();
    }
  }
  int role = wave;
  PIN_S(role);
  if (role < 4) {
    u32x4 kr[32], qv;
#define qso (WS_QP + hk * 16384 + (idx >> 3) * 4096 + w4 * 1024)
    const unsigned kvo = (lane & 7) * 512 + ((lane >> 3) & 1) * 64 + (lane >> 4) * 16, seq = SEQ;
    const rsrc_t rk = make_rsrc(p_k + KV_OFF, 32768);
    const unsigned t_go = t_k + T_OFF;
#pragma unroll
    for (int g = 0; g < 8; ++g) {
      const unsigned so = ((seq >> (4 * g)) & 7) << 12;
      PACE(g * T_STEP);
#pragma unroll
      for (int i = 0; i < 4; ++i) kr[g * 4 + i] = __builtin_amdgcn_raw_buffer_load_b128(rk, kvo + i * 128, so, AUX_NT);
    }
    SCHED_FENCE();
    const unsigned kepoch = __builtin_amdgcn_raw_buffer_load_b32(rws, 0, WS_EPOCH, AUX_SC1);
    qv = ld_sc1(rws, l16, qso);
    WG_BARRIER_BARE();
    tag = next_tag(kepoch);
    asm volatile("" : "+s"(tag) : "v"(cpf));
    SPIN(SPIN_MAX) {
      if (wave_none((or4(qv ^ tag_bytes(tag)) & 255u) != 0u)) break;
      MEM_FENCE();
      qv = ld_sc1(rws, l16, qso);
    }
    const f32x4 qp = block_values(qv);
    lds_st<unsigned>(lds, L_Q + w4 * 256 + (lane & 31) * 8 + (lane >> 5) * 4, pack_bf16(fold32(qp[0], qp[2]), fold32(qp[1], qp[3])));
    lds_flag(lds, F_Q + w4 * 4, 1u);
    lds_wait_all(lds, F_Q);
    float m0, m1;
    {
      const int qa = L_Q + (lane & 1) * 512 + ((lane >> 1) & 1) * 64 + (lane >> 4) * 16;
      f32x4 c0, c1;
      {
        u32x4 qf[4];
#pragma unroll
        for (int s = 0; s < 4; ++s) qf[s] = lds_ld<u32x4>(lds, qa + s * 128);
        f32x4 c[8];
#pragma unroll
        for (int t = 0; t < 8; ++t) c[t] = mma(asb(qf[0]), asb(kr[t * 4]), f32x4{0.f, 0.f, 0.f, 0.f});
#pragma unroll
        for (int s = 1; s < 4; ++s)
#pragma unroll
          for (int t = 0; t < 8; ++t) c[t] = mma(asb(qf[s]), asb(kr[t * 4 + s]), c[t]);
        const bool g1 = lane & 16, g2 = lane & 32;
        c0 = g2 ? (g1 ? c[3] : c[2]) : (g1 ? c[1] : c[0]);
        c1 = g2 ? (g1 ? c[7] : c[6]) : (g1 ? c[5] : c[4]);
      }
      const bool cb = lane & 8;
      const float r0 = (cb ? c1[2] : c0[0]) + dpp<XOR8>(cb ? c0[2] : c1[0]);
      const float r1 = (cb ? c1[3] : c0[1]) + dpp<XOR8>(cb ? c0[3] : c1[1]);
      const unsigned sp = pack_bf16(r0, r1);
      const float s0 = bf_lo(sp) * 0.0625f, s1 = bf_hi(sp) * 0.0625f;
      float mw = max32(s0, s1);
      mw = row_max(max16(mw, mw));
      *(volatile LAS unsigned*)(lds + L_MX + (lane >> 5) * 16 + w4 * 4) = asu(mw) & 0xfffffffeu;
      u32x4 x0, x1;
      LSPIN() { x0 = lds_peek(lds, L_MX); x1 = lds_peek(lds, L_MX + 16); if (wave_none(unset4(x0) | unset4(x1))) break; }
      m0 = fmaxf(fmaxf(asf(x0[0]), asf(x0[1])), fmaxf(asf(x0[2]), asf(x0[3])));
      m1 = fmaxf(fmaxf(asf(x1[0]), asf(x1[1])), fmaxf(asf(x1[2]), asf(x1[3])));
      const float p0 = __builtin_amdgcn_exp2f((s0 - m0) * LOG2E), p1 = __builtin_amdgcn_exp2f((s1 - m1) * LOG2E);
      const unsigned pk = pack_bf16(p0, p1);
      const int po = L_P + (((lane >> 3) & 1) * 4 + (lane >> 4)) * 64 + ((w4 & 1) * 2 + ((lane >> 2) & 1)) * 16 + ((w4 >> 1) * 4 + (lane & 3)) * 2;
      lds_st<u16>(lds, po, (u16)pk);
      lds_st<u16>(lds, po + 512, (u16)(pk >> 16));
      float l = fold32(p0, p1);
      l = row_sum(fold16(l, l));
      MEM_FENCE();
      *(volatile LAS unsigned*)(lds + L_LS + (lane >> 5) * 16 + w4 * 4) = asu(l) & 0xfffffffeu;
    }
    u32x4 ls;
    LSPIN() { ls = lds_peek(lds, L_LS + (lane >> 5) * 16); if (wave_none(unset4(ls))) break; }
    MEM_FENCE();
    u32x4 tb = tag_bytes(tag);
    unsigned oso = WS_OP + wo_rg * 8192 + w4 * 2048;
    LAS char* ad = lds + L_A + (lane & 4) * 64 + w4 * 64 + ((lane & 1) * 2 + ((lane >> 3) & 1)) * 16 + ((lane & 2) * 2 + (lane >> 4)) * 2;
    LAS char* fa = lds + L_F + w4 * 32 + (lane >> 3) * 4;
    PIN_S(tb); PIN_S(oso); PIN_V(ad); PIN_V(fa);
    if (w4 < 2) {
      const int g = lane >> 4, row = lane >> 2, p = lane & 3, tl = lane & 7;
      LAS char* va[8];
#pragma unroll
      for (int t = 0; t < 8; ++t) va[t] = lds + L_V + row * 512 + ((w4 * 16 + (p >> 1) * 8 + (t ^ (row & 7))) << 4) + 8 * (p & 1);
      LAS char* pa = lds + L_P + ((lane >> 3) & 1) * 512 + g * 16;
      unsigned ovo = (((lane >> 3) & 1) * 4 + (g >> 1)) * 8192 + (g & 1) * 1024 + tl * 16;
      unsigned pso = WS_OP + (hk * 8 + w4 * 2) * 8192 + (idx >> 3) * 2048 + (idx & 7) * 128;
      PIN_V(ovo); PIN_S(pso);
      f32x4 o = {0.f, 0.f, 0.f, 0.f};
      u32x4 pm[8];
      {
        const u32x4 pl = lds_ld<u32x4>(pa, 0);
#pragma unroll
        for (int t = 0; t < 8; ++t) pm[t] = tl == t ? pl : u32x4{0u, 0u, 0u, 0u};
      }
      _Pragma("nounroll") for (int ks = 0; ks < 7; ++ks) {
        LSPIN() {
          const u32x4 pr = lds_peek(lds, L_PROG);
          if (wave_none(min(min(pr[0], pr[1]), min(pr[2], pr[3])) <= (unsigned)(ks + 2))) break;
        }
        lds_flag(lds, F_PV + w4 * 4, ks >= 2 ? 1u : 0u);
        pa += 64;
        const u32x4 pl = lds_ld<u32x4>(pa, 0);
        bf16x8 vf[8];
#pragma unroll
        for (int t = 0; t < 8; ++t) vf[t] = tr_frag<8192>(va[t]);
        SCHED_FENCE();
#pragma unroll
        for (int t = 0; t < 8; ++t) {
          o = mma(vf[t], asb(pm[t]), o);
          SCHED_FENCE();
          pm[t] = tl == t ? pl : u32x4{0u, 0u, 0u, 0u};
          va[t] += 16384;
          PIN_V(pm[t]); PIN_V(va[t]);
          SCHED_FENCE();
        }
      }
      WG_BARRIER_BARE();
      {
        bf16x8 vf[8];
#pragma unroll
        for (int t = 0; t < 8; ++t) vf[t] = tr_frag<8192>(va[t]);
        SCHED_FENCE();
#pragma unroll
        for (int t = 0; t < 8; ++t) o = mma(vf[t], asb(pm[t]), o);
      }
      st_sc1(tag4(o, tag), rws, ovo, pso);
    } else if (w4 == 2) {
      if (late && !is_wo) {
        u32x4 gv[4];
        SPIN(SPIN_MAX) {
          unsigned bad = 0;
#pragma unroll
          for (int u = 0; u < 4; ++u) gv[u] = ld_sc1(rws, l16 + u * 1024, WS_GP + (dn_un >> 1) * 4096);
#pragma unroll
          for (int u = 0; u < 4; ++u) bad |= or4(gv[u] ^ tb);
          if (wave_none((bad & 255u) != 0u)) break;
          MEM_FENCE();
        }
        const f32x4 gs = block_values(gv[0]) + block_values(gv[1]), us = block_values(gv[2]) + block_values(gv[3]);
        u32x2 hv;
#pragma unroll
        for (int e = 0; e < 2; ++e) {
          const unsigned gt = pack_bf16(gs[2 * e], gs[2 * e + 1]), up = pack_bf16(us[2 * e], us[2 * e + 1]);
          const float g0 = bf_lo(gt), g1 = bf_hi(gt);
          hv[e] = pack_bf16(g0 * (g0 > 0.f ? 1.f : 0.f) * bf_lo(up), g1 * (g1 > 0.f ? 1.f : 0.f) * bf_hi(up));
        }
        lds_st<u32x2>(lds, L_A + (lane & 8) * 32 + (lane >> 4) * 64 + (lane & 3) * 16 + (lane & 4) * 2, hv);
      } else if (is_red) {
        xv = __builtin_amdgcn_raw_buffer_load_b16(make_rsrc(p_x, NB * DM * 2), oi * 2, 0, 0);
        PIN_V(xv);
      }
      WG_BARRIER();
    } else {
      const float lsum = (asf(ls[0]) + asf(ls[1])) + (asf(ls[2]) + asf(ls[3]));
      const float l1 = asf(__builtin_amdgcn_readlane(asu(lsum), 32));
      if (lane == 0) st_sc1(tag4(f32x4{m0, lsum, m1, l1}, tag), rws, 0, WS_ML + (hk * 32 + idx) * 16);
      const unsigned mso = is_wo ? WS_ML + (wo_rg >> 3) * 512 : WS_END;
      LSPIN() { if (wave_none(*(volatile LAS unsigned*)(lds + L_PROG) < 7u)) break; __builtin_amdgcn_s_sleep(4); }
      u32x4 mv = ld_sc1(rws, (lane & 31) * 16, mso);
      WG_BARRIER_BARE();
      mv ^= tb;
      PIN_V(mv);
      if (is_wo) {
        SPIN(SPIN_MAX) {
          if (wave_none((or4(mv) & 255u) != 0u)) break;
          MEM_FENCE();
          mv = ld_sc1(rws, (lane & 31) * 16, mso) ^ tb;
        }
        const float m = asf((idx & 8) ? mv[2] : mv[0]);
        float mx = fmaxf(m, dpp<XOR7>(m));
        mx = fmaxf(mx, dpp<XOR2>(mx));
        mx = fmaxf(mx, dpp<XOR1>(mx));
        const float e = __builtin_amdgcn_exp2f((m - mx) * LOG2E);
        float lt = e * asf((idx & 8) ? mv[3] : mv[1]);
        lt += dpp<XOR7>(lt);
        lt += dpp<XOR2>(lt);
        lt += dpp<XOR1>(lt);
        *(volatile LAS unsigned*)(lds + L_F + (lane & 31) * 4) = asu(e * __builtin_amdgcn_rcpf(lt)) & 0xfffffffeu;
      } else if (bid == 56) {
        SPIN(SPIN_MAX) {
          unsigned bad = 0;
#pragma unroll
          for (int u = 0; u < 4; ++u) bad |= or4(ld_sc1(rws, l16 + u * 1024, WS_ML) ^ tb);
          if (wave_none((bad & 255u) != 0u)) break;
          MEM_FENCE();
        }
        if (lane == 0) __builtin_amdgcn_raw_buffer_store_b32(tag, rws, 0, WS_EPOCH, AUX_SC1);
      }
    }
    if (is_wo) {
      float f;
      LSPIN() {
        f = *(volatile LAS float*)fa;
        if (wave_none((asu(f) == 0xffffffffu) | (*(volatile LAS unsigned*)(lds + F_T) == 0u))) break;
      }
      u32x4 o0, o1;
      MEM_FENCE();
      SPIN(SPIN_MAX) {
        o0 = ld_sc1(rws, l16, oso) ^ tb;
        o1 = ld_sc1(rws, l16 + 1024, oso) ^ tb;
        if (wave_none(((or4(o0) | or4(o1)) & 255u) != 0u)) break;
        MEM_FENCE();
      }
      const float o = fold_pair(asf4(o0) * f, asf4(o1) * f);
      lds_st<u16>(ad, 0, (u16)pack_bf16(o, o));
      WG_BARRIER();
    }
  }

  if (is_red) {
    const u32x4 tb = tag_bytes(tag);
    LAS char* fw = lds + L_FIN + ((lane >> 4) * 8 + (lane & 7)) * 64 + wave * 4 + (lane & 8) * 4;
    LAS char* fr = lds + L_FIN + (lane & 31) * 64 + (lane >> 5) * 32;
    unsigned oo = (lane & 32) ? 0x7fffffffu : oi * 2;
    const bool dn_lane = wave * 64 + lane < 28 * 8;
    unsigned dvo = dn_lane ? l16 + 8192 : 0x7fffffffu;
    u32x4 td = dn_lane ? tb : u32x4{0u, 0u, 0u, 0u};
    float xf = bf_lo(xv);
    PIN_V(fw); PIN_V(fr); PIN_V(oo); PIN_V(xf); PIN_V(dvo); PIN_V(td);
    const rsrc_t ro = make_rsrc(p_out, NB * DM * 2);
    const unsigned so = WS_RP + bid * RP_BOX + wave * 1024;
    u32x4 a0, a1;
    SPIN(SPIN_MAX) {
      a1 = ld_sc1(rws, dvo, so) ^ td;
      a0 = ld_sc1(rws, l16, so) ^ tb;
      if (wave_none(((or4(a0) | or4(a1)) & 255u) != 0u)) break;
      MEM_FENCE();
    }
    lds_st<float>(fw, 0, fold_pair(asf4(a0), asf4(a1)));
    WG_BARRIER();
    if (wave == 2) {
      const f32x4 t = lds_ld<f32x4>(fr, 0) + lds_ld<f32x4>(fr, 16);
      const f32x2 t2 = f32x2{t[0], t[1]} + f32x2{t[2], t[3]};
      const float u = round_bf16(t2[0] + t2[1]);
      auto sw = __builtin_amdgcn_permlane32_swap(asu(u), asu(u), false, false);
      const float sv = asf(sw[0]), dn = asf(sw[1]);
      const float g = round_bf16(dn * __builtin_amdgcn_rcpf(1.0f + __builtin_amdgcn_exp2f(-dn * LOG2E)));
      const float res = xf + round_bf16(sv * g);
      __builtin_amdgcn_raw_buffer_store_b16((u16)pack_bf16(res, res), ro, oo, 0, 0);
    }
  }
}

}

extern "C" void kernel_launch(void* const* d_in, const int* in_sizes, int n_in, void* d_out, int out_size, void* d_ws,
                              size_t ws_size, hipStream_t stream) {
  fused_decode<<<256, 512, 0, stream>>>((const u16*)d_in[0], (const u16*)d_in[1], (const u16*)d_in[2], (const u16*)d_in[3],
                                        (const u16*)d_in[4], (const u16*)d_in[6], (const u16*)d_in[7], (const u16*)d_in[8],
                                        (u16*)d_out, (char*)d_ws);
}
```
